```python
import torch

def _softmax(x, axis=-1):
    return torch.softmax(x, dim=axis)
def _gelu(x):
    return torch.nn.functional.gelu(x, approximate="tanh")
def _silu(x):
    return torch.nn.functional.silu(x)
def _sigmoid(x):
    return torch.sigmoid(x)
def _layernorm(x, axis=-1, eps=1e-5):
    m = x.mean(dim=axis, keepdim=True)
    v = x.var(dim=axis, keepdim=True, unbiased=False)
    return (x - m) / torch.sqrt(v + eps)
def _scatter_add(tgt, idx, src, axis=0):
    return tgt.clone().index_add_(axis, idx, src)
def _segment_sum(src, seg, n):
    out = torch.zeros((n,) + src.shape[1:], dtype=src.dtype)
    return out.index_add_(0, seg, src)
def _pad(x, pw):
    flat = tuple(v for pair in reversed(pw) for v in pair)
    return torch.nn.functional.pad(x, flat)

kernel_name = 'standalone__layernorm_linear__sampling'

def setup_inputs():

    _w = (lambda a: (lambda t: t.to(torch.bfloat16) if t.is_floating_point() else t)(torch.as_tensor(a)))
    B, S, D = 1024, 8, 576
    return {
        "x": _w((lambda *s: torch.randn(*s, dtype=torch.bfloat16) * 0.05)(B, S, D)),
        "g": _w((lambda *s: torch.randn(*s, dtype=torch.bfloat16) * 0.05)(D) + 1.0),
        "b": _w((lambda *s: torch.randn(*s, dtype=torch.bfloat16) * 0.4)(D)),
        "w": _w((lambda *s: torch.randn(*s, dtype=torch.bfloat16) * 0.05)(D, D)),
    }

def reference(x, g, b, w):
    return (_layernorm(x, axis=-1) * g + b) @ w
```

```cpp
#include <hip/hip_runtime.h>
#include <hip/hip_bf16.h>
#include <stdint.h>

using bf16x8 = __attribute__((ext_vector_type(8))) short;
using f32x4 = __attribute__((ext_vector_type(4))) float;
using u32x4 = __attribute__((ext_vector_type(4))) unsigned;

constexpr int D = 576;
constexpr int BM = 64;

__device__ __forceinline__ float bflo(unsigned u) { return __uint_as_float(u << 16); }
__device__ __forceinline__ float bfhi(unsigned u) { return __uint_as_float(u & 0xffff0000u); }
__device__ __forceinline__ unsigned packbf(float a, float b) {
  __hip_bfloat16 x = __float2bfloat16(a), y = __float2bfloat16(b);
  return (unsigned)__bfloat16_as_ushort(x) | ((unsigned)__bfloat16_as_ushort(y) << 16);
}

__global__ __launch_bounds__(256) void k_simple(const uint16_t* __restrict__ x, const uint16_t* __restrict__ g,
                                                const uint16_t* __restrict__ b, const uint16_t* __restrict__ w,
                                                uint16_t* __restrict__ out) {
  __shared__ __attribute__((aligned(16))) uint16_t sA[BM * (D + 8)];
  const int tid = threadIdx.x, lane = tid & 63, wid = tid >> 6;
  const int rb = blockIdx.x >> 1, ch = blockIdx.x & 1;
  const int row0 = rb * BM;
  for (int it = 0; it < 2; ++it) {
    int r = wid * 16 + it * 8 + (lane >> 3);
    int c8 = lane & 7;
    const u32x4* xr = (const u32x4*)(x + (size_t)(row0 + r) * D);
    u32x4 v[9];
    for (int j = 0; j < 9; ++j) v[j] = xr[c8 + 8 * j];
    float s = 0;
    for (int j = 0; j < 9; ++j) for (int e = 0; e < 4; ++e) s += bflo(v[j][e]) + bfhi(v[j][e]);
    s += __shfl_xor(s, 1); s += __shfl_xor(s, 2); s += __shfl_xor(s, 4);
    float mean = s * (1.0f / D);
    float q = 0;
    for (int j = 0; j < 9; ++j) for (int e = 0; e < 4; ++e) { float a = bflo(v[j][e]) - mean, c = bfhi(v[j][e]) - mean; q += a * a + c * c; }
    q += __shfl_xor(q, 1); q += __shfl_xor(q, 2); q += __shfl_xor(q, 4);
    float rstd = rsqrtf(q * (1.0f / D) + 1e-5f);
    for (int j = 0; j < 9; ++j) {
      u32x4 gv = ((const u32x4*)g)[c8 + 8 * j], bv = ((const u32x4*)b)[c8 + 8 * j];
      u32x4 o;
      for (int e = 0; e < 4; ++e) {
        float a = (bflo(v[j][e]) - mean) * rstd * bflo(gv[e]) + bflo(bv[e]);
        float c = (bfhi(v[j][e]) - mean) * rstd * bfhi(gv[e]) + bfhi(bv[e]);
        o[e] = packbf(a, c);
      }
      *(u32x4*)(sA + r * (D + 8) + (c8 + 8 * j) * 8) = o;
    }
  }
  __syncthreads();
  const int fr = lane & 15, fq = lane >> 4;
  for (int ct = wid; ct < 18; ct += 4) {
    int n0 = ch * 288 + ct * 16;
    f32x4 acc[4] = {};
    for (int s = 0; s < 18; ++s) {
      bf16x8 bf;
      for (int j = 0; j < 8; ++j) bf[j] = (short)w[(size_t)(s * 32 + fq * 8 + j) * D + n0 + fr];
      for (int m = 0; m < 4; ++m) {
        bf16x8 af = *(const bf16x8*)(sA + (m * 16 + fr) * (D + 8) + s * 32 + fq * 8);
        acc[m] = __builtin_amdgcn_mfma_f32_16x16x32_bf16(af, bf, acc[m], 0, 0, 0);
      }
    }
    for (int m = 0; m < 4; ++m)
      for (int j = 0; j < 4; ++j)
        out[(size_t)(row0 + m * 16 + fq * 4 + j) * D + n0 + fr] = __bfloat16_as_ushort(__float2bfloat16(acc[m][j]));
  }
}

extern "C" void kernel_launch(void* const* d_in, const int* in_sizes, int n_in, void* d_out, int out_size, void* d_ws,
                              size_t ws_size, hipStream_t stream) {
  const uint16_t* x = (const uint16_t*)d_in[0];
  const uint16_t* g = (const uint16_t*)d_in[1];
  const uint16_t* b = (const uint16_t*)d_in[2];
  const uint16_t* w = (const uint16_t*)d_in[3];
  int M = in_sizes[0] / D;
  k_simple<<<(M / BM) * 2, 256, 0, stream>>>(x, g, b, w, (uint16_t*)d_out);
}
```

```cpp
#include <hip/hip_runtime.h>
#include <stdint.h>

typedef short s16x4 __attribute__((ext_vector_type(4)));
typedef __bf16 bf16x2 __attribute__((ext_vector_type(2)));
typedef __bf16 bf16x8 __attribute__((ext_vector_type(8)));
typedef float f32x2 __attribute__((ext_vector_type(2)));
typedef float f32x4 __attribute__((ext_vector_type(4)));
typedef unsigned u32x2 __attribute__((ext_vector_type(2)));
typedef unsigned u32x4 __attribute__((ext_vector_type(4)));
#define LDS_AS __attribute__((address_space(3)))

#define X_POLICY " nt"
#define STAMPS 0

namespace {
constexpr int D = 576;
constexpr int ROWB = D * 2;
constexpr int SLOT = 24576;
constexpr int OUTB = 6 * SLOT;
constexpr int LDS_BYTES = OUTB + 8192;
constexpr int NQ = 15;

constexpr int slot_of(int q) {
  return q < 3 ? q : q == 3 ? 3 : q == 4 ? 0 : q == 5 ? 4 : q == 6 ? 1 : q == 7 ? 5 : q == 8 ? 2 : slot_of(q - 6);
}
constexpr int dma_lo(int q) { return q < 3 ? 3 + 2 * q : q + 6; }
constexpr int dma_hi(int q) { return q < 3 ? 5 + 2 * q : q + 7; }
constexpr int cmin(int a, int b) { return a < b ? a : b; }
constexpr bool store_in_step(int q) { return q >= 4 && (q % 3) == 1; }
constexpr int younger_than(int c, int q) {
  int n = 0;
  if (c < 3) n += (2 - c) * 3;
  for (int s = 0; s < q; ++s) {
    bool after = c < 3 || c < dma_lo(s);
    for (int d = dma_lo(s); d < cmin(dma_hi(s), NQ); ++d) {
      if (d > c) n += 3;
    }
    bool issued_by_now = c < cmin(dma_hi(s), NQ) || after;
    if (store_in_step(s) && issued_by_now) n += 1;
  }
  return n;
}

__device__ __forceinline__ u32x4 make_srd(const void* p, unsigned bytes) {
  uint64_t a = (uint64_t)p;
  u32x4 r = {(unsigned)a, (unsigned)(a >> 32) & 0xffffu, bytes, 0x00020000u};
  return r;
}
template <int N> __device__ __forceinline__ void wait_vm() { asm volatile("s_waitcnt vmcnt(%0)" ::"i"(N) : "memory"); }
__device__ __forceinline__ void wait_lgkm0() { asm volatile("s_waitcnt lgkmcnt(0)" ::: "memory"); }
__device__ __forceinline__ void barrier() { asm volatile("s_barrier" ::: "memory"); }
__device__ __forceinline__ void dma16(unsigned lds_addr, unsigned voff, u32x4 srd, unsigned soff) {
  asm volatile("s_mov_b32 m0, %0\n\ts_nop 0\n\tbuffer_load_dwordx4 %1, %2, %3 offen lds" ::"s"(lds_addr), "v"(voff), "s"(srd), "s"(soff) : "memory");
}
template <int CTRL> __device__ __forceinline__ float dpp_add(float v) {
  return v + __builtin_bit_cast(float, __builtin_amdgcn_update_dpp(0, __builtin_bit_cast(int, v), CTRL, 0xf, 0xf, true));
}
__device__ __forceinline__ float row16_sum(float v) {
  v = dpp_add<0x128>(v); v = dpp_add<0x124>(v); v = dpp_add<0x122>(v); v = dpp_add<0x121>(v);
  return v;
}
__device__ __forceinline__ unsigned pack2(f32x2 v) { return __builtin_bit_cast(unsigned, __builtin_convertvector(v, bf16x2)); }
__device__ __forceinline__ f32x2 unpack2(unsigned u) { return f32x2{__uint_as_float(u << 16), __uint_as_float(u & 0xffff0000u)}; }

struct Frag { s16x4 lo, hi; };
__device__ __forceinline__ bf16x8 as_op(const Frag& f) {
  typedef short s16x8 __attribute__((ext_vector_type(8)));
  s16x8 v = __builtin_shufflevector(f.lo, f.hi, 0, 1, 2, 3, 4, 5, 6, 7);
  return __builtin_bit_cast(bf16x8, v);
}

#define STAMP(i) do {} while (0)

template <int V> struct IC { static constexpr int v = V; };
template <int I, int N, class F> __device__ __forceinline__ void static_for(F&& f) {
  if constexpr (I < N) { f(IC<I>{}); static_for<I + 1, N>(f); }
}

__global__ __launch_bounds__(512, 2) void ln_linear_kernel(const uint16_t* __restrict__ x, const uint16_t* __restrict__ g,
                                                           const uint16_t* __restrict__ b, const uint16_t* __restrict__ w,
                                                           uint16_t* __restrict__ out
) {
  extern __shared__ __attribute__((aligned(1024))) char smem[];
  LDS_AS char* lds = (LDS_AS char*)smem;
  STAMP(0);
  const int tid = threadIdx.x, lane = tid & 63;
  const int wv = __builtin_amdgcn_readfirstlane(tid >> 6);
  const int bid = blockIdx.x;
  const int rb = (bid & 7) * 16 + (bid >> 4), h = (bid >> 3) & 1;

  const u32x4 srdx = make_srd(x, 8192u * ROWB), srdw = make_srd(w, (unsigned)D * ROWB), srdo = make_srd(out, 8192u * ROWB);
  const unsigned ldsbase = (unsigned)(uintptr_t)lds;
  asm volatile("" ::"s"(srdo));

  unsigned gq, bq;
  {
    const u32x4 srdg = make_srd(g, ROWB), srdb = make_srd(b, ROWB);
    const unsigned voff = (unsigned)tid * 4;
    asm volatile("buffer_load_dword %0, %1, %2, 0 offen" : "=v"(gq) : "v"(voff), "s"(srdg));
    asm volatile("buffer_load_dword %0, %1, %2, 0 offen" : "=v"(bq) : "v"(voff), "s"(srdb));
  }
  u32x2 xr[2][9];
  {
    const unsigned voff = (unsigned)(tid >> 4) * ROWB + (unsigned)(lane & 15) * 8;
#pragma unroll
    for (int t = 0; t < 2; ++t) {
      const unsigned soff = (unsigned)(rb * 64 + t * 32) * ROWB;
#pragma unroll
      for (int j = 0; j < 9; ++j)
        asm volatile("buffer_load_dwordx2 %0, %1, %2, %3 offen offset:%4" X_POLICY : "=v"(xr[t][j]) : "v"(voff), "s"(srdx), "s"(soff), "i"(j * 128));
    }
  }
  const int wr = lane >> 3, wc = lane & 7;
  const unsigned wvoff = (unsigned)(tid >> 3) * ROWB + (unsigned)(((((wc >> 1) ^ (((wr >> 1) & 1) | ((tid >> 5) & 2))) << 1) | (wc & 1)) * 16);
  const unsigned line0 = (unsigned)h * 640u;
  auto dma_chunk = [&](auto qc) {
    constexpr int q = decltype(qc)::v;
    constexpr int sl = slot_of(q);
    const unsigned lineoff = (q / 3 < 4) ? line0 + (unsigned)(q / 3) * 128u : 512u;
#pragma unroll
    for (int i = 0; i < 3; ++i)
      dma16(ldsbase + sl * SLOT + i * 8192 + wv * 1024, wvoff, srdw, lineoff + (unsigned)((q % 3) * 192 + i * 64) * ROWB);
  };
  dma_chunk(IC<0>{}); dma_chunk(IC<1>{}); dma_chunk(IC<2>{});
  STAMP(1);

  asm volatile("s_waitcnt vmcnt(27)" : "+v"(gq), "+v"(bq) :: "memory");
  STAMP(2);
  *(LDS_AS f32x2*)(lds + OUTB + tid * 8) = unpack2(gq);
  *(LDS_AS f32x2*)(lds + OUTB + 4096 + tid * 8) = unpack2(bq);
  wait_lgkm0();
  barrier();

  {
    const int c = lane & 15, r = (wv & 3) * 4 + (lane >> 4);
    LDS_AS char* abase = lds + 3 * SLOT + (wv >> 2) * 6144 + (c >> 3) * 1024 + ((c >> 1) & 3) * 256 + ((r ^ (c >> 1)) * 16) + (c & 1) * 8;
    LDS_AS char* gbase = lds + OUTB + c * 16;
    const bf16x2 ones = {(__bf16)1.0f, (__bf16)1.0f};
    f32x4 gv[9], bv[9];
#pragma unroll
    for (int j = 0; j < 9; ++j) { gv[j] = *(LDS_AS f32x4*)(gbase + j * 256); bv[j] = *(LDS_AS f32x4*)(gbase + 4096 + j * 256); }
#pragma unroll
    for (int t = 0; t < 2; ++t) {
      float s0 = 0.f, s1 = 0.f, q0 = 0.f, q1 = 0.f;
#pragma unroll
      for (int j = 0; j < 9; ++j) {
        if (j % 3 == 0) asm volatile("s_waitcnt vmcnt(%3)" : "+v"(xr[t][j]), "+v"(xr[t][j + 1]), "+v"(xr[t][j + 2]) : "i"(27 - t * 9 - j - 3));
        const unsigned u0 = xr[t][j][0], u1 = xr[t][j][1];
        const bf16x2 a = __builtin_bit_cast(bf16x2, u0), c2 = __builtin_bit_cast(bf16x2, u1);
        s0 = __builtin_amdgcn_fdot2_f32_bf16(a, ones, s0, false);
        q0 = __builtin_amdgcn_fdot2_f32_bf16(a, a, q0, false);
        s1 = __builtin_amdgcn_fdot2_f32_bf16(c2, ones, s1, false);
        q1 = __builtin_amdgcn_fdot2_f32_bf16(c2, c2, q1, false);
        if (j % 3 == 2) __builtin_amdgcn_sched_barrier(0);
      }
      const float mean = row16_sum(s0 + s1) * (1.0f / D);
      const float var = fmaxf(row16_sum(q0 + q1) * (1.0f / D) - mean * mean, 0.f);
      const float rstd = __builtin_amdgcn_rsqf(var + 1e-5f);
      const f32x2 rs = {rstd, rstd}, nm = {-mean * rstd, -mean * rstd};
#pragma unroll
      for (int j = 0; j < 9; ++j) {
        f32x2 y0 = __builtin_elementwise_fma(unpack2(xr[t][j][0]), rs, nm), y1 = __builtin_elementwise_fma(unpack2(xr[t][j][1]), rs, nm);
        y0 = __builtin_elementwise_fma(y0, f32x2{gv[j][0], gv[j][1]}, f32x2{bv[j][0], bv[j][1]});
        y1 = __builtin_elementwise_fma(y1, f32x2{gv[j][2], gv[j][3]}, f32x2{bv[j][2], bv[j][3]});
        *(LDS_AS u32x2*)(abase + t * 12288 + (j / 3) * SLOT + (j % 3) * 2048) = u32x2{pack2(y0), pack2(y1)};
      }
    }
  }
  STAMP(3);

  const int m = wv >> 2, n = wv & 3;
  const int fr = lane & 15, fg = lane >> 4;
  LDS_AS char* ard[2][2];
#pragma unroll
  for (int i = 0; i < 2; ++i)
#pragma unroll
    for (int e = 0; e < 2; ++e)
      ard[i][e] = lds + 3 * SLOT + (2 * m + (i ^ h)) * 6144 + fg * 256 + ((fr ^ (4 * e + fg)) * 16);
  LDS_AS char* wrd = lds + (8 * fg + (fr >> 2)) * 128 + ((n ^ (((fr >> 3) & 1) | ((fg & 1) << 1))) * 32) + (fr & 3) * 8;
  LDS_AS char* owr[2];
#pragma unroll
  for (int i = 0; i < 2; ++i) owr[i] = lds + OUTB + ((2 * m + (i ^ h)) * 16 + fr) * 128 + (((2 * n + (fg >> 1)) ^ (fr & 7)) * 16) + (fg & 1) * 8;
  LDS_AS char* ord = lds + OUTB + tid * 16;
  const unsigned ovoff = (unsigned)(tid >> 3) * ROWB + (unsigned)((wc ^ wr) * 16);
  const unsigned osoff = (unsigned)(rb * 64) * ROWB;

  bf16x8 xa[2][18];
  Frag wf[2][6];
  f32x4 acc[2][2];

  auto read_w = [&](auto qc) {
    constexpr int q = decltype(qc)::v;
    constexpr int sl = slot_of(q);
#pragma unroll
    for (int s = 0; s < 6; ++s) {
      wf[q & 1][s].lo = __builtin_amdgcn_ds_read_tr16_b64_v4i16((LDS_AS s16x4*)(wrd + sl * SLOT + s * 4096));
      wf[q & 1][s].hi = __builtin_amdgcn_ds_read_tr16_b64_v4i16((LDS_AS s16x4*)(wrd + sl * SLOT + s * 4096 + 512));
    }
  };
  auto read_a = [&](int q) {
#pragma unroll
    for (int s = 0; s < 6; ++s)
#pragma unroll
      for (int i = 0; i < 2; ++i) xa[i][q * 6 + s] = *(LDS_AS bf16x8*)(ard[i][s & 1] + q * SLOT + s * 1024);
  };
  auto write_out = [&](int p) {
#pragma unroll
    for (int i = 0; i < (p < 4 ? 2 : 1); ++i) {
      const f32x4 a = acc[p & 1][i];
      *(LDS_AS u32x2*)(owr[i] + (p < 4 ? 0 : 3 * SLOT - OUTB)) = u32x2{pack2(f32x2{a[0], a[1]}), pack2(f32x2{a[2], a[3]})};
    }
  };
  auto store_out = [&](int p) {
    const u32x4 v = *(LDS_AS u32x4*)(ord + (p < 4 ? 0 : 3 * SLOT - OUTB));
    const unsigned so = osoff + (p < 4 ? line0 + (unsigned)p * 128u : 512u);
    asm volatile("buffer_store_dwordx4 %0, %1, %2, %3 offen sc1\n\ts_nop 1" ::"v"(v), "v"(ovoff), "s"(srdo), "s"(so) : "memory");
  };

  wait_vm<6>();
  wait_lgkm0();
  barrier();
  read_w(IC<0>{});
  read_a(0);
  STAMP(4);

  static_for<0, NQ>([&](auto qc) {
    constexpr int q = decltype(qc)::v;
    constexpr int p = q / 3;
    if constexpr (q + 1 < NQ) {
      wait_vm<younger_than(q + 1, q)>();
      wait_lgkm0();
      barrier();
      static_for<dma_lo(q), cmin(dma_hi(q), NQ)>(dma_chunk);
      if constexpr (store_in_step(q)) store_out(p - 1);
      read_w(IC<q + 1>{});
      if constexpr (q + 1 < 3) read_a(q + 1);
    }
    if constexpr (q % 3 == 0) {
      acc[p & 1][0] = f32x4{0.f, 0.f, 0.f, 0.f};
      acc[p & 1][1] = f32x4{0.f, 0.f, 0.f, 0.f};
    }
#pragma unroll
    for (int s = 0; s < 6; ++s) {
      const bf16x8 wop = as_op(wf[q & 1][s]);
      acc[p & 1][0] = __builtin_amdgcn_mfma_f32_16x16x32_bf16(wop, xa[0][(q % 3) * 6 + s], acc[p & 1][0], 0, 0, 0);
      if constexpr (p < 4) acc[p & 1][1] = __builtin_amdgcn_mfma_f32_16x16x32_bf16(wop, xa[1][(q % 3) * 6 + s], acc[p & 1][1], 0, 0, 0);
    }
    if constexpr (q % 3 == 0 && q > 0) write_out(p - 1);
    if constexpr (q % 3 == 2) STAMP(5 + p);
  });
  write_out(4);
  wait_lgkm0();
  barrier();
  if (((wv >> 1) & 1) == h) store_out(4);
  STAMP(10);
}
}

extern "C" void kernel_launch(void* const* d_in, const int* in_sizes, int n_in, void* d_out, int out_size, void* d_ws,
                              size_t ws_size, hipStream_t stream) {
  (void)in_sizes; (void)n_in; (void)out_size; (void)d_ws; (void)ws_size;
  (void)hipFuncSetAttribute((const void*)ln_linear_kernel, hipFuncAttributeMaxDynamicSharedMemorySize, LDS_BYTES);
  ln_linear_kernel<<<256, 512, LDS_BYTES, stream>>>((const uint16_t*)d_in[0], (const uint16_t*)d_in[1], (const uint16_t*)d_in[2],
                                                    (const uint16_t*)d_in[3], (uint16_t*)d_out
  );
}
```

```cpp
#define PROBE 0
#define PROBE_BLOCK 0
#include <hip/hip_runtime.h>
#include <stdint.h>

typedef short s16x4 __attribute__((ext_vector_type(4)));
typedef __bf16 bf16x2 __attribute__((ext_vector_type(2)));
typedef __bf16 bf16x8 __attribute__((ext_vector_type(8)));
typedef float f32x2 __attribute__((ext_vector_type(2)));
typedef float f32x4 __attribute__((ext_vector_type(4)));
typedef unsigned u32x2 __attribute__((ext_vector_type(2)));
typedef unsigned u32x4 __attribute__((ext_vector_type(4)));
#define LDS_AS __attribute__((address_space(3)))

#define X_POLICY " nt"
#define STAMPS 1

namespace {
constexpr int D = 576;
constexpr int ROWB = D * 2;
constexpr int SLOT = 24576;
constexpr int OUTB = 6 * SLOT;
constexpr int LDS_BYTES = OUTB + 8192;
constexpr int NQ = 15;

constexpr int slot_of(int q) {
  return q < 3 ? q : q == 3 ? 3 : q == 4 ? 0 : q == 5 ? 4 : q == 6 ? 1 : q == 7 ? 5 : q == 8 ? 2 : slot_of(q - 6);
}
constexpr int dma_lo(int q) { return q < 3 ? 3 + 2 * q : q + 6; }
constexpr int dma_hi(int q) { return q < 3 ? 5 + 2 * q : q + 7; }
constexpr int cmin(int a, int b) { return a < b ? a : b; }
constexpr bool store_in_step(int q) { return q >= 4 && (q % 3) == 1; }
constexpr int younger_than(int c, int q) {
  int n = 0;
  if (c < 3) n += (2 - c) * 3;
  for (int s = 0; s < q; ++s) {
    bool after = c < 3 || c < dma_lo(s);
    for (int d = dma_lo(s); d < cmin(dma_hi(s), NQ); ++d) {
      if (d > c) n += 3;
    }
    bool issued_by_now = c < cmin(dma_hi(s), NQ) || after;
    if (store_in_step(s) && issued_by_now) n += 1;
  }
  return n;
}

__device__ __forceinline__ u32x4 make_srd(const void* p, unsigned bytes) {
  uint64_t a = (uint64_t)p;
  u32x4 r = {(unsigned)a, (unsigned)(a >> 32) & 0xffffu, bytes, 0x00020000u};
  return r;
}
template <int N> __device__ __forceinline__ void wait_vm() { asm volatile("s_waitcnt vmcnt(%0)" ::"i"(N) : "memory"); }
__device__ __forceinline__ void wait_lgkm0() { asm volatile("s_waitcnt lgkmcnt(0)" ::: "memory"); }
__device__ __forceinline__ void barrier() { asm volatile("s_barrier" ::: "memory"); }
__device__ __forceinline__ void dma16(unsigned lds_addr, unsigned voff, u32x4 srd, unsigned soff) {
  asm volatile("s_mov_b32 m0, %0\n\ts_nop 0\n\tbuffer_load_dwordx4 %1, %2, %3 offen lds" ::"s"(lds_addr), "v"(voff), "s"(srd), "s"(soff) : "memory");
}
template <int CTRL> __device__ __forceinline__ float dpp_add(float v) {
  return v + __builtin_bit_cast(float, __builtin_amdgcn_update_dpp(0, __builtin_bit_cast(int, v), CTRL, 0xf, 0xf, true));
}
__device__ __forceinline__ float row16_sum(float v) {
  v = dpp_add<0x128>(v); v = dpp_add<0x124>(v); v = dpp_add<0x122>(v); v = dpp_add<0x121>(v);
  return v;
}
__device__ __forceinline__ unsigned pack2(f32x2 v) { return __builtin_bit_cast(unsigned, __builtin_convertvector(v, bf16x2)); }
__device__ __forceinline__ f32x2 unpack2(unsigned u) { return f32x2{__uint_as_float(u << 16), __uint_as_float(u & 0xffff0000u)}; }

struct Frag { s16x4 lo, hi; };
__device__ __forceinline__ bf16x8 as_op(const Frag& f) {
  typedef short s16x8 __attribute__((ext_vector_type(8)));
  s16x8 v = __builtin_shufflevector(f.lo, f.hi, 0, 1, 2, 3, 4, 5, 6, 7);
  return __builtin_bit_cast(bf16x8, v);
}

#define STAMP(i) do { if (i != 0 && i != PROBE) break; unsigned long long t_; asm volatile("s_memrealtime %0\n\ts_waitcnt lgkmcnt(0)" : "=s"(t_) :: "memory"); stamp[i] = t_; } while (0)

template <int V> struct IC { static constexpr int v = V; };
template <int I, int N, class F> __device__ __forceinline__ void static_for(F&& f) {
  if constexpr (I < N) { f(IC<I>{}); static_for<I + 1, N>(f); }
}

__global__ __launch_bounds__(512, 2) void ln_linear_kernel(const uint16_t* __restrict__ x, const uint16_t* __restrict__ g,
                                                           const uint16_t* __restrict__ b, const uint16_t* __restrict__ w,
                                                           uint16_t* __restrict__ out
) {
  extern __shared__ __attribute__((aligned(1024))) char smem[];
  LDS_AS char* lds = (LDS_AS char*)smem;
  unsigned long long stamp[24];
  STAMP(0);
  STAMP(11);
  const int tid = threadIdx.x, lane = tid & 63;
  const int wv = __builtin_amdgcn_readfirstlane(tid >> 6);
  const int bid = blockIdx.x;
  const int rb = (bid & 7) * 16 + (bid >> 4), h = (bid >> 3) & 1;

  const u32x4 srdx = make_srd(x, 8192u * ROWB), srdw = make_srd(w, (unsigned)D * ROWB), srdo = make_srd(out, 8192u * ROWB);
  const unsigned ldsbase = (unsigned)(uintptr_t)lds;
  asm volatile("" ::"s"(srdo));

  unsigned gq, bq;
  {
    const u32x4 srdg = make_srd(g, ROWB), srdb = make_srd(b, ROWB);
    const unsigned voff = (unsigned)tid * 4;
    asm volatile("buffer_load_dword %0, %1, %2, 0 offen" : "=v"(gq) : "v"(voff), "s"(srdg));
    asm volatile("buffer_load_dword %0, %1, %2, 0 offen" : "=v"(bq) : "v"(voff), "s"(srdb));
  }
  u32x2 xr[2][9];
  {
    const unsigned voff = (unsigned)(tid >> 4) * ROWB + (unsigned)(lane & 15) * 8;
#pragma unroll
    for (int t = 0; t < 2; ++t) {
      const unsigned soff = (unsigned)(rb * 64 + t * 32) * ROWB;
#pragma unroll
      for (int j = 0; j < 9; ++j)
        asm volatile("buffer_load_dwordx2 %0, %1, %2, %3 offen offset:%4" X_POLICY : "=v"(xr[t][j]) : "v"(voff), "s"(srdx), "s"(soff), "i"(j * 128));
    }
  }
  const int wr = lane >> 3, wc = lane & 7;
  const unsigned wvoff = (unsigned)(tid >> 3) * ROWB + (unsigned)(((((wc >> 1) ^ (((wr >> 1) & 1) | ((tid >> 5) & 2))) << 1) | (wc & 1)) * 16);
  const unsigned line0 = (unsigned)h * 640u;
  auto dma_chunk = [&](auto qc) {
    constexpr int q = decltype(qc)::v;
    constexpr int sl = slot_of(q);
    const unsigned lineoff = (q / 3 < 4) ? line0 + (unsigned)(q / 3) * 128u : 512u;
#pragma unroll
    for (int i = 0; i < 3; ++i)
      dma16(ldsbase + sl * SLOT + i * 8192 + wv * 1024, wvoff, srdw, lineoff + (unsigned)((q % 3) * 192 + i * 64) * ROWB);
  };
  dma_chunk(IC<0>{}); dma_chunk(IC<1>{}); dma_chunk(IC<2>{});
  STAMP(1);

  asm volatile("s_waitcnt vmcnt(27)" : "+v"(gq), "+v"(bq) :: "memory");
  STAMP(2);
  *(LDS_AS f32x2*)(lds + OUTB + tid * 8) = unpack2(gq);
  *(LDS_AS f32x2*)(lds + OUTB + 4096 + tid * 8) = unpack2(bq);
  wait_lgkm0();
  barrier();

  {
    const int c = lane & 15, r = (wv & 3) * 4 + (lane >> 4);
    LDS_AS char* abase = lds + 3 * SLOT + (wv >> 2) * 6144 + (c >> 3) * 1024 + ((c >> 1) & 3) * 256 + ((r ^ (c >> 1)) * 16) + (c & 1) * 8;
    LDS_AS char* gbase = lds + OUTB + c * 16;
    const bf16x2 ones = {(__bf16)1.0f, (__bf16)1.0f};
    f32x4 gv[9], bv[9];
#pragma unroll
    for (int j = 0; j < 9; ++j) { gv[j] = *(LDS_AS f32x4*)(gbase + j * 256); bv[j] = *(LDS_AS f32x4*)(gbase + 4096 + j * 256); }
#pragma unroll
    for (int t = 0; t < 2; ++t) {
      float s0 = 0.f, s1 = 0.f, q0 = 0.f, q1 = 0.f;
#pragma unroll
      for (int j = 0; j < 9; ++j) {
        if (j % 3 == 0) asm volatile("s_waitcnt vmcnt(%3)" : "+v"(xr[t][j]), "+v"(xr[t][j + 1]), "+v"(xr[t][j + 2]) : "i"(27 - t * 9 - j - 3));
        const unsigned u0 = xr[t][j][0], u1 = xr[t][j][1];
        const bf16x2 a = __builtin_bit_cast(bf16x2, u0), c2 = __builtin_bit_cast(bf16x2, u1);
        s0 = __builtin_amdgcn_fdot2_f32_bf16(a, ones, s0, false);
        q0 = __builtin_amdgcn_fdot2_f32_bf16(a, a, q0, false);
        s1 = __builtin_amdgcn_fdot2_f32_bf16(c2, ones, s1, false);
        q1 = __builtin_amdgcn_fdot2_f32_bf16(c2, c2, q1, false);
        if (j % 3 == 2) __builtin_amdgcn_sched_barrier(0);
      }
      const float mean = row16_sum(s0 + s1) * (1.0f / D);
      const float var = fmaxf(row16_sum(q0 + q1) * (1.0f / D) - mean * mean, 0.f);
      const float rstd = __builtin_amdgcn_rsqf(var + 1e-5f);
      const f32x2 rs = {rstd, rstd}, nm = {-mean * rstd, -mean * rstd};
#pragma unroll
      for (int j = 0; j < 9; ++j) {
        f32x2 y0 = __builtin_elementwise_fma(unpack2(xr[t][j][0]), rs, nm), y1 = __builtin_elementwise_fma(unpack2(xr[t][j][1]), rs, nm);
        y0 = __builtin_elementwise_fma(y0, f32x2{gv[j][0], gv[j][1]}, f32x2{bv[j][0], bv[j][1]});
        y1 = __builtin_elementwise_fma(y1, f32x2{gv[j][2], gv[j][3]}, f32x2{bv[j][2], bv[j][3]});
        *(LDS_AS u32x2*)(abase + t * 12288 + (j / 3) * SLOT + (j % 3) * 2048) = u32x2{pack2(y0), pack2(y1)};
      }
    }
  }
  STAMP(3);

  const int m = wv >> 2, n = wv & 3;
  const int fr = lane & 15, fg = lane >> 4;
  LDS_AS char* ard[2][2];
#pragma unroll
  for (int i = 0; i < 2; ++i)
#pragma unroll
    for (int e = 0; e < 2; ++e)
      ard[i][e] = lds + 3 * SLOT + (2 * m + (i ^ h)) * 6144 + fg * 256 + ((fr ^ (4 * e + fg)) * 16);
  LDS_AS char* wrd = lds + (8 * fg + (fr >> 2)) * 128 + ((n ^ (((fr >> 3) & 1) | ((fg & 1) << 1))) * 32) + (fr & 3) * 8;
  LDS_AS char* owr[2];
#pragma unroll
  for (int i = 0; i < 2; ++i) owr[i] = lds + OUTB + ((2 * m + (i ^ h)) * 16 + fr) * 128 + (((2 * n + (fg >> 1)) ^ (fr & 7)) * 16) + (fg & 1) * 8;
  LDS_AS char* ord = lds + OUTB + tid * 16;
  const unsigned ovoff = (unsigned)(tid >> 3) * ROWB + (unsigned)((wc ^ wr) * 16);
  const unsigned osoff = (unsigned)(rb * 64) * ROWB;

  bf16x8 xa[2][18];
  Frag wf[2][6];
  f32x4 acc[2][2];

  auto read_w = [&](auto qc) {
    constexpr int q = decltype(qc)::v;
    constexpr int sl = slot_of(q);
#pragma unroll
    for (int s = 0; s < 6; ++s) {
      wf[q & 1][s].lo = __builtin_amdgcn_ds_read_tr16_b64_v4i16((LDS_AS s16x4*)(wrd + sl * SLOT + s * 4096));
      wf[q & 1][s].hi = __builtin_amdgcn_ds_read_tr16_b64_v4i16((LDS_AS s16x4*)(wrd + sl * SLOT + s * 4096 + 512));
    }
  };
  auto read_a = [&](int q) {
#pragma unroll
    for (int s = 0; s < 6; ++s)
#pragma unroll
      for (int i = 0; i < 2; ++i) xa[i][q * 6 + s] = *(LDS_AS bf16x8*)(ard[i][s & 1] + q * SLOT + s * 1024);
  };
  auto write_out = [&](int p) {
#pragma unroll
    for (int i = 0; i < (p < 4 ? 2 : 1); ++i) {
      const f32x4 a = acc[p & 1][i];
      *(LDS_AS u32x2*)(owr[i] + (p < 4 ? 0 : 3 * SLOT - OUTB)) = u32x2{pack2(f32x2{a[0], a[1]}), pack2(f32x2{a[2], a[3]})};
    }
  };
  auto store_out = [&](int p) {
    const u32x4 v = *(LDS_AS u32x4*)(ord + (p < 4 ? 0 : 3 * SLOT - OUTB));
    const unsigned so = osoff + (p < 4 ? line0 + (unsigned)p * 128u : 512u);
    asm volatile("buffer_store_dwordx4 %0, %1, %2, %3 offen sc1\n\ts_nop 1" ::"v"(v), "v"(ovoff), "s"(srdo), "s"(so) : "memory");
  };

  wait_vm<6>();
  wait_lgkm0();
  barrier();
  read_w(IC<0>{});
  read_a(0);
  STAMP(4);

  static_for<0, NQ>([&](auto qc) {
    constexpr int q = decltype(qc)::v;
    constexpr int p = q / 3;
    if constexpr (q + 1 < NQ) {
      wait_vm<younger_than(q + 1, q)>();
      wait_lgkm0();
      barrier();
      static_for<dma_lo(q), cmin(dma_hi(q), NQ)>(dma_chunk);
      if constexpr (store_in_step(q)) store_out(p - 1);
      read_w(IC<q + 1>{});
      if constexpr (q + 1 < 3) read_a(q + 1);
    }
    if constexpr (q % 3 == 0) {
      acc[p & 1][0] = f32x4{0.f, 0.f, 0.f, 0.f};
      acc[p & 1][1] = f32x4{0.f, 0.f, 0.f, 0.f};
    }
#pragma unroll
    for (int s = 0; s < 6; ++s) {
      const bf16x8 wop = as_op(wf[q & 1][s]);
      acc[p & 1][0] = __builtin_amdgcn_mfma_f32_16x16x32_bf16(wop, xa[0][(q % 3) * 6 + s], acc[p & 1][0], 0, 0, 0);
      if constexpr (p < 4) acc[p & 1][1] = __builtin_amdgcn_mfma_f32_16x16x32_bf16(wop, xa[1][(q % 3) * 6 + s], acc[p & 1][1], 0, 0, 0);
    }
    if constexpr (q % 3 == 0 && q > 0) write_out(p - 1);
    if constexpr (q % 3 == 2) STAMP(5 + p);
  });
  write_out(4);
  wait_lgkm0();
  barrier();
  if (((wv >> 1) & 1) == h) store_out(4);
  STAMP(10);
  if (bid == PROBE_BLOCK && wv == 0) {
    const unsigned long long until = stamp[0] + 4000ull + (stamp[PROBE] - stamp[0]);
    while (__builtin_amdgcn_s_memrealtime() < until) __builtin_amdgcn_s_sleep(1);
  }
}
}

extern "C" void kernel_launch(void* const* d_in, const int* in_sizes, int n_in, void* d_out, int out_size, void* d_ws,
                              size_t ws_size, hipStream_t stream) {
  (void)in_sizes; (void)n_in; (void)out_size; (void)d_ws; (void)ws_size;
  (void)hipFuncSetAttribute((const void*)ln_linear_kernel, hipFuncAttributeMaxDynamicSharedMemorySize, LDS_BYTES);
  ln_linear_kernel<<<256, 512, LDS_BYTES, stream>>>((const uint16_t*)d_in[0], (const uint16_t*)d_in[1], (const uint16_t*)d_in[2],
                                                    (const uint16_t*)d_in[3], (uint16_t*)d_out
  );
}
```

```cpp
#include <hip/hip_runtime.h>
#include <stdint.h>

typedef short s16x4 __attribute__((ext_vector_type(4)));
typedef __bf16 bf16x2 __attribute__((ext_vector_type(2)));
typedef __bf16 bf16x8 __attribute__((ext_vector_type(8)));
typedef float f32x2 __attribute__((ext_vector_type(2)));
typedef float f32x4 __attribute__((ext_vector_type(4)));
typedef unsigned u32x2 __attribute__((ext_vector_type(2)));
typedef unsigned u32x4 __attribute__((ext_vector_type(4)));
typedef unsigned u32x8 __attribute__((ext_vector_type(8)));
#define LDS_AS __attribute__((address_space(3)))

#define X_POLICY " nt"
#define T_POLICY ""
#define S_POLICY " sc1"
#define W0_ROW 3
#define W1_ROW 8
#define CODE_PF 1
#define SPLIT_ROWS 0
#define THROTTLE 0
#define BPRIO 0
#define TOUCH_POS 3
#define STAMPS 0

namespace {
constexpr int D = 576;
constexpr int ROWB = D * 2;
constexpr int SLOT = 24576;
constexpr int GBB = 6 * SLOT;
constexpr int LDS_BYTES = GBB + 9216;
constexpr int NQ = 15;

template <int V> struct IC { static constexpr int v = V; };
template <int I, int N, class F> __device__ __forceinline__ void static_for(F&& f) {
  if constexpr (I < N) { f(IC<I>{}); static_for<I + 1, N>(f); }
}
constexpr int slot_of(int q) { return q < 4 ? q : q == 4 ? 0 : q == 5 ? 4 : q == 6 ? 1 : q == 7 ? 5 : q == 8 ? 2 : slot_of(q - 6); }
constexpr int dma_lo(int q) { return q == 0 ? 3 : q + 4; }
constexpr int dma_hi(int q) { return q + 5 < NQ ? q + 5 : NQ; }
constexpr int chunks_ahead(int q) { return (q == 0 ? 3 : dma_hi(q - 1)) - (q + 2); }
constexpr int OUTB = 3 * SLOT;
static_assert(slot_of(9) == 3 && slot_of(11) == 4, "staging slots");

__device__ __forceinline__ u32x4 make_srd(const void* p, unsigned bytes) {
  uint64_t a = (uint64_t)p;
  u32x4 r = {(unsigned)a, (unsigned)(a >> 32) & 0xffffu, bytes, 0x00020000u};
  return r;
}
__device__ __forceinline__ u32x4 make_srd2(unsigned lo, unsigned hi, unsigned bytes) { return u32x4{lo, hi & 0xffffu, bytes, 0x00020000u}; }
template <int N> __device__ __forceinline__ void wait_vm() { asm volatile("s_waitcnt vmcnt(%0)" ::"i"(N) : "memory"); }
template <int N> __device__ __forceinline__ void wait_lgkm() { asm volatile("s_waitcnt lgkmcnt(%0)" ::"i"(N) : "memory"); }
__device__ __forceinline__ void barrier() { asm volatile("s_barrier" ::: "memory"); }
__device__ __forceinline__ void fence() { __builtin_amdgcn_sched_barrier(0); }
__device__ __forceinline__ void dma16(unsigned lds_addr, unsigned voff, u32x4 srd, unsigned soff) {
  asm volatile("s_mov_b32 m0, %0\n\ts_nop 0\n\tbuffer_load_dwordx4 %1, %2, %3 offen lds" ::"s"(lds_addr), "v"(voff), "s"(srd), "s"(soff) : "memory");
}
__device__ __forceinline__ void st16(u32x4 v, unsigned voff, u32x4 srd, unsigned soff) {
  asm volatile("buffer_store_dwordx4 %0, %1, %2, %3 offen" S_POLICY "\n\ts_nop 1" ::"v"(v), "v"(voff), "s"(srd), "s"(soff) : "memory");
}
template <int I> __device__ __forceinline__ void ts_put(unsigned& tsv) {
  unsigned long long t_; asm volatile("s_memtime %0\n\ts_waitcnt lgkmcnt(0)" : "=s"(t_) :: "memory");
  asm volatile("v_writelane_b32 %0, %1, %2" : "+v"(tsv) : "s"((unsigned)t_), "i"(I));
}
template <int CTRL> __device__ __forceinline__ float dpp_add(float v) {
  return v + __builtin_bit_cast(float, __builtin_amdgcn_update_dpp(0, __builtin_bit_cast(int, v), CTRL, 0xf, 0xf, true));
}
__device__ __forceinline__ float oct_sum(float v) {
  v = dpp_add<0xB1>(v); v = dpp_add<0x4E>(v); v = dpp_add<0x141>(v);
  return v;
}
__device__ __forceinline__ unsigned pack2(f32x2 v) { return __builtin_bit_cast(unsigned, __builtin_convertvector(v, bf16x2)); }
__device__ __forceinline__ f32x2 unpack2(unsigned u) { return f32x2{__uint_as_float(u << 16), __uint_as_float(u & 0xffff0000u)}; }
template <int N> __device__ __forceinline__ void row_stats(const u32x4& x, float& s, float& q, f32x2 (&y)[4]) {
  float a0, a1, a2, a3, a4, a5, a6, a7;
  const unsigned x0 = x[0], x1 = x[1], x2 = x[2], x3 = x[3];
  asm volatile(
      "s_waitcnt vmcnt(%16)\n\t"
      "v_dot2_f32_bf16 %0, %10, %14, 0\n\tv_dot2_f32_bf16 %1, %10, %10, 0\n\t"
      "v_lshlrev_b32 %2, 16, %10\n\t"
      "v_dot2_f32_bf16 %0, %11, %14, %0\n\tv_dot2_f32_bf16 %1, %11, %11, %1\n\t"
      "v_and_b32 %3, %15, %10\n\t"
      "v_dot2_f32_bf16 %0, %12, %14, %0\n\tv_dot2_f32_bf16 %1, %12, %12, %1\n\t"
      "v_lshlrev_b32 %4, 16, %11\n\t"
      "v_dot2_f32_bf16 %0, %13, %14, %0\n\tv_dot2_f32_bf16 %1, %13, %13, %1\n\t"
      "v_and_b32 %5, %15, %11\n\t"
      "v_lshlrev_b32 %6, 16, %12\n\tv_and_b32 %7, %15, %12\n\t"
      "v_lshlrev_b32 %8, 16, %13\n\tv_and_b32 %9, %15, %13"
      : "=&v"(s), "=&v"(q), "=&v"(a0), "=&v"(a1), "=&v"(a2), "=&v"(a3), "=&v"(a4), "=&v"(a5), "=&v"(a6), "=&v"(a7)
      : "v"(x0), "v"(x1), "v"(x2), "v"(x3), "s"(0x3f803f80u), "s"(0xffff0000u), "i"(N));
  y[0] = f32x2{a0, a1}; y[1] = f32x2{a2, a3}; y[2] = f32x2{a4, a5}; y[3] = f32x2{a6, a7};
}
__device__ __forceinline__ void pin(f32x2& v) { asm volatile("" : "+v"(v)); }
__device__ __forceinline__ LDS_AS char* opaque(LDS_AS char* p) { asm volatile("" : "+v"(p)); return p; }

#define TS(i) do {} while (0)
#define TS2(i) do {} while (0)

__global__ __launch_bounds__(512, 2) void ln_linear_kernel(const uint16_t* __restrict__ x, const uint16_t* __restrict__ g,
                                                           const uint16_t* __restrict__ b, const uint16_t* __restrict__ w,
                                                           uint16_t* __restrict__ out
) {
  extern __shared__ __attribute__((aligned(1024))) char smem[];
  LDS_AS char* lds = (LDS_AS char*)smem;
  u32x8 ka;
  u32x2 ko;
  asm volatile("s_load_dwordx8 %0, %2, 0x0\n\ts_load_dwordx2 %1, %2, 0x20" : "=&s"(ka), "=&s"(ko) : "s"(__builtin_amdgcn_kernarg_segment_ptr()));
  const int tid = threadIdx.x, lane = tid & 63;
  const int wv = __builtin_amdgcn_readfirstlane(tid >> 6);
  const int bid = blockIdx.x;
  const int rb = (bid & 7) * 16 + (bid >> 4), h = (bid >> 3) & 1;
  const int wi = wv & 3;
  if (wv == 4) {
    unsigned long long pc;
    unsigned len;
    asm volatile("s_getpc_b64 %0\n.Lpf_here:\n\ts_mov_b32 %1, .Lpf_end-.Lpf_here" : "=s"(pc), "=s"(len));
    const u32x4 srdc = make_srd((const void*)pc, len);
    const unsigned voff = lane < 4 ? (unsigned)((bid >> 3) + lane * 32) * 128u : 0xfffffff0u;
    asm volatile("s_mov_b32 m0, %0\n\ts_nop 0\n\tbuffer_load_dword %1, %2, 0 offen lds" ::"s"((unsigned)(uintptr_t)lds + GBB + 8192), "v"(voff), "s"(srdc) : "memory");
  }
  __builtin_amdgcn_sched_barrier(0);
  const int r7 = lane >> 3, c7 = lane & 7;
  const unsigned ldsbase = (unsigned)(uintptr_t)lds;
  asm volatile("s_waitcnt lgkmcnt(0)" : "+s"(ka), "+s"(ko));
  const u32x4 srdx = make_srd2(ka[0], ka[1], 8192u * ROWB), srdw = make_srd2(ka[6], ka[7], (unsigned)D * ROWB), srdo = make_srd2(ko[0], ko[1], 8192u * ROWB);

  auto touch_w = [&]() {
    const unsigned li = (unsigned)(TOUCH_POS == 3 ? tid - 256 : tid & 255);
    const unsigned voff = li < 162u ? ((unsigned)(bid >> 3) * 162u + li) * 128u : 0xfffffff0u;
    asm volatile("s_mov_b32 m0, %0\n\ts_nop 0\n\tbuffer_load_dword %1, %2, 0 offen" T_POLICY " lds" ::"s"(ldsbase + GBB + 8192), "v"(voff), "s"(srdw) : "memory");
  };
  u32x4 xr[9];
  const unsigned vm = (unsigned)lane * 16, vt = (unsigned)c7 * 16;
  if (wv == 0) {
    const u32x4 srdg = make_srd2(ka[2], ka[3], ROWB), srdb = make_srd2(ka[4], ka[5], ROWB);
    asm volatile("s_mov_b32 m0, %0\n\ts_nop 0\n\tbuffer_load_dwordx4 %1, %2, 0 offen lds\n\tbuffer_load_dwordx4 %1, %2, 0 offen offset:1024 lds" ::"s"(ldsbase + GBB), "v"(vm), "s"(srdg) : "memory");
    asm volatile("s_mov_b32 m0, %0\n\ts_nop 0\n\tbuffer_load_dwordx4 %1, %2, 0 offen lds\n\tbuffer_load_dwordx4 %1, %2, 0 offen offset:1024 lds" ::"s"(ldsbase + GBB + 2048), "v"(vm), "s"(srdb) : "memory");
  }
  if (wv >= 4) barrier();
  const int oct = wv;
  const unsigned rowoff = (unsigned)r7 * ROWB;
  {
    const unsigned soff = (unsigned)(rb * 64 + oct * 8) * ROWB;
    TS(12);
    asm volatile("buffer_load_dwordx4 %0, %1, %2, %3 offen offset:1024" X_POLICY : "=v"(xr[8]) : "v"(rowoff + vt), "s"(srdx), "s"(soff));
#pragma unroll
    for (int r = 0; r < 8; ++r)
      asm volatile("buffer_load_dwordx4 %0, %1, %2, %3 offen offset:%4" X_POLICY : "=v"(xr[r]) : "v"(vm), "s"(srdx), "s"(soff + (r >> 2) * 4 * ROWB), "i"((r & 3) * ROWB));
  }
  touch_w();
  constexpr int XO = 1;
  TS(13);
  if (wv < 4) { wait_vm<9 + XO>(); barrier(); }
  TS(1);

  const unsigned wvoff = rowoff + (unsigned)(((((c7 >> 1) ^ (((r7 >> 1) & 1) | ((tid >> 5) & 2))) << 1) | (c7 & 1)) * 16);
  auto dma_chunk = [&](auto qc) {
    constexpr int q = decltype(qc)::v, sl = slot_of(q);
    const unsigned lineoff = ((q / 3 < 4) ? (unsigned)h * 640u + (unsigned)(q / 3) * 128u : 512u) + (unsigned)wi * 8u * ROWB;
#pragma unroll
    for (int i = 0; i < 6; ++i)
    {
      dma16(ldsbase + sl * SLOT + (i * 4 + wi) * 1024, wvoff, srdw, lineoff + (unsigned)((q % 3) * 192 + i * 32) * ROWB);
      if constexpr (q < THROTTLE) __builtin_amdgcn_s_sleep(2);
    }
  };

  {
    float sm[9], sq[9];
    f32x2 y[9][4];
    u32x4 gq[4];
    gq[0] = *(LDS_AS u32x4*)(lds + GBB + vm);
    gq[1] = *(LDS_AS u32x4*)(lds + GBB + 2048 + vm);
    gq[2] = *(LDS_AS u32x4*)(lds + GBB + 1024 + vt);
    gq[3] = *(LDS_AS u32x4*)(lds + GBB + 3072 + vt);
    fence();
    static_for<0, 9>([&](auto rc) {
      constexpr int rr = decltype(rc)::v, r = rr == 0 ? 8 : rr - 1;
      row_stats<8 - rr + XO>(xr[r], sm[r], sq[r], y[r]);
      if constexpr (rr == 0) TS(14);
    });
    fence();
    TS(3);
    LDS_AS char* abase = lds + 3 * SLOT + oct * 3072;
    LDS_AS char* aw[8];
#pragma unroll
    for (int r = 0; r < 8; ++r) aw[r] = abase + (r7 / 3) * SLOT + (r7 % 3) * 1024 + ((c7 ^ r) * 16);
    LDS_AS char* awt = abase + 2 * SLOT + 2048 + r7 * 128 + ((c7 ^ r7) * 16);
    f32x2 gf[4][4];
#pragma unroll
    for (int i = 0; i < 4; ++i)
#pragma unroll
      for (int e = 0; e < 4; ++e) { const unsigned u = gq[i][e]; gf[i][e] = unpack2(u); }
    auto fold32 = [](float a, float b) {
      auto r = __builtin_amdgcn_permlane32_swap(__builtin_bit_cast(unsigned, a), __builtin_bit_cast(unsigned, b), false, false);
      return __builtin_bit_cast(float, (unsigned)r[0]) + __builtin_bit_cast(float, (unsigned)r[1]);
    };
    auto fold16 = [](float a, float b) {
      auto r = __builtin_amdgcn_permlane16_swap(__builtin_bit_cast(unsigned, a), __builtin_bit_cast(unsigned, b), false, false);
      return __builtin_bit_cast(float, (unsigned)r[0]) + __builtin_bit_cast(float, (unsigned)r[1]);
    };
    auto fold8 = [](float a, float b) {
      float t;
      asm("s_nop 1\n\tv_add_f32_dpp %0, %1, %1 row_ror:8 row_mask:0xf bank_mask:0x3\n\tv_add_f32_dpp %0, %2, %2 row_ror:8 row_mask:0xf bank_mask:0xc" : "=&v"(t) : "v"(a), "v"(b));
      return t;
    };
    float s4[4], q4[4];
#pragma unroll
    for (int r = 0; r < 4; ++r) { s4[r] = fold32(sm[r], sm[r + 4]); q4[r] = fold32(sq[r], sq[r + 4]); }
    const float s2a = fold16(s4[0], s4[2]), s2b = fold16(s4[1], s4[3]), q2a = fold16(q4[0], q4[2]), q2b = fold16(q4[1], q4[3]);
    const float mean = oct_sum(fold8(s2a, s2b) + sm[8]) * (1.0f / D);
    const float var = fmaxf(oct_sum(fold8(q2a, q2b) + sq[8]) * (1.0f / D) - mean * mean, 0.f);
    const float rstd = __builtin_amdgcn_rsqf(var + 1e-5f);
    const float nmr = -mean * rstd;
    unsigned long long sc[8];
#pragma unroll
    for (int r = 0; r < 8; ++r)
      sc[r] = (unsigned long long)(unsigned)__builtin_amdgcn_readlane(__builtin_bit_cast(int, rstd), r * 8) |
              ((unsigned long long)(unsigned)__builtin_amdgcn_readlane(__builtin_bit_cast(int, nmr), r * 8) << 32);
    fence();
#pragma unroll
    for (int rr = 0; rr < 9; ++rr) {
      const int r = rr == 0 ? 8 : rr - 1;
#pragma unroll
      for (int e = 0; e < 4; ++e) {
        if (r == 8) y[r][e] = __builtin_elementwise_fma(y[r][e], f32x2{rstd, rstd}, f32x2{nmr, nmr});
        else asm("v_pk_fma_f32 %0, %0, %1, %1 op_sel:[0,0,1] op_sel_hi:[1,0,1]" : "+v"(y[r][e]) : "s"(sc[r]));
      }
      fence();
#pragma unroll
      for (int e = 0; e < 4; ++e) y[r][e] = __builtin_elementwise_fma(y[r][e], gf[r == 8 ? 2 : 0][e], gf[r == 8 ? 3 : 1][e]);
      fence();
      *(LDS_AS u32x4*)(r == 8 ? awt : aw[r] + r * 128) = u32x4{pack2(y[r][0]), pack2(y[r][1]), pack2(y[r][2]), pack2(y[r][3])};
      fence();
      if (rr == W0_ROW && wv >= 4) dma_chunk(IC<0>{});
      if (rr == W1_ROW && wv >= 4) dma_chunk(IC<1>{});
    }
  }
  TS(4);

  if (wv < 4) {
    wait_lgkm<0>();
    TS(5);
    barrier();
    TS(6);
    dma_chunk(IC<2>{});
    static_for<0, NQ - 1>([&](auto qc) {
      constexpr int q = decltype(qc)::v;
      TS2(16 + 3 * q);
      wait_vm<chunks_ahead(q) * 6>();
      TS2(17 + 3 * q);
      barrier();
      TS2(18 + 3 * q);
      static_for<dma_lo(q), dma_hi(q)>(dma_chunk);
    });
    LDS_AS char* ord = lds + OUTB + tid * 16;
    const unsigned ovoff = rowoff + (unsigned)((c7 ^ r7) * 16);
    const unsigned osoff = (unsigned)(rb * 64 + wi * 8) * ROWB + (unsigned)h * 640u;
    u32x4 ov[8];
#pragma unroll
    for (int i = 0; i < 8; ++i) ov[i] = *(LDS_AS u32x4*)(ord + i * 4096);
#pragma unroll
    for (int i = 0; i < 8; ++i) st16(ov[i], ovoff, srdo, osoff + (i >> 1) * 128u + (i & 1) * 32u * ROWB);
    TS(9);
    barrier();
    st16(*(LDS_AS u32x4*)(lds + GBB + tid * 16), ovoff, srdo, (unsigned)(rb * 64 + (wi >> 1) * 32 + h * 16 + (wi & 1) * 8) * ROWB + 512u);
    TS(10);
  } else {
    const int m = wi >> 1, n = wi & 1;
    const int fr = lane & 15, fg = lane >> 4;
    LDS_AS char* ard[2][2];
#pragma unroll
    for (int i = 0; i < 2; ++i)
#pragma unroll
      for (int e = 0; e < 2; ++e)
        ard[i][e] = opaque(lds + 3 * SLOT + ((2 * m + (i ^ h)) * 2 + (fr >> 3)) * 3072 + (fr & 7) * 128 + (((4 * e + fg) ^ (fr & 7)) * 16));
    LDS_AS char* wrd[2][3];
#pragma unroll
    for (int t = 0; t < 2; ++t)
#pragma unroll
      for (int z = 0; z < 3; ++z)
        wrd[t][z] = opaque(lds + z * 2 * SLOT + (8 * fg + (fr >> 2)) * 128 + (((2 * n + t) ^ (((fr >> 3) & 1) | ((fg & 1) << 1))) * 32) + (fr & 3) * 8);

    bf16x8 xa[2][18];
    bf16x8 wf[2][3][2];
    f32x4 acc[2][2];
    u32x2 po[5][2][2];

    auto read_half = [&](auto hc) {
      constexpr int hs = decltype(hc)::v;
      constexpr int sl = slot_of(hs / 2);
#pragma unroll
      for (int s = 0; s < 3; ++s) {
        constexpr int base = (sl & 1) * SLOT + (hs & 1) * 3 * 4096;
#pragma unroll
        for (int t = 0; t < 2; ++t) {
          const s16x4 lo = __builtin_amdgcn_ds_read_tr16_b64_v4i16((LDS_AS s16x4*)(wrd[t][sl >> 1] + base + s * 4096));
          const s16x4 hi = __builtin_amdgcn_ds_read_tr16_b64_v4i16((LDS_AS s16x4*)(wrd[t][sl >> 1] + base + s * 4096 + 512));
          wf[hs & 1][s][t] = __builtin_bit_cast(bf16x8, __builtin_shufflevector(lo, hi, 0, 1, 2, 3, 4, 5, 6, 7));
        }
        if constexpr (hs < 6) {
          const int ks = hs * 3 + s;
#pragma unroll
          for (int i = 0; i < 2; ++i) xa[i][ks] = *(LDS_AS bf16x8*)(ard[i][ks & 1] + (ks / 6) * SLOT + ((ks % 6) >> 1) * 1024);
        }
      }
    };
    auto mma_half = [&](auto hc) {
      constexpr int hs = decltype(hc)::v;
      constexpr int p = hs / 6;
#pragma unroll
      for (int s = 0; s < 3; ++s) {
        const int ks = (hs % 6) * 3 + s;
#pragma unroll
        for (int t = 0; t < 2; ++t)
#pragma unroll
          for (int i = 0; i < (p < 4 ? 2 : 1); ++i) {
            const f32x4 cin = ks == 0 ? f32x4{0.f, 0.f, 0.f, 0.f} : acc[i][t];
            acc[i][t] = __builtin_amdgcn_mfma_f32_16x16x32_bf16(wf[hs & 1][s][t], xa[i][ks], cin, 0, 0, 0);
          }
      }
    };
    auto interleave = [&](auto nr, auto nm, auto nv) {
      constexpr int R = decltype(nr)::v, M = decltype(nm)::v, V = decltype(nv)::v;
      static_for<0, M>([&](auto ic) {
        constexpr int i = decltype(ic)::v;
        constexpr int r = (R * (i + 1) + M - 1) / M - (R * i + M - 1) / M;
        __builtin_amdgcn_sched_group_barrier(0x008, 1, 0);
        if constexpr (r > 0) __builtin_amdgcn_sched_group_barrier(0x100, r, 0);
        if constexpr (i >= 2 && i - 2 < V / 2) __builtin_amdgcn_sched_group_barrier(0x002, 2, 0);
      });
    };
    auto pack_acc = [&](auto pc) {
      constexpr int p = decltype(pc)::v;
#pragma unroll
      for (int i = 0; i < (p < 4 ? 2 : 1); ++i)
#pragma unroll
        for (int t = 0; t < 2; ++t) po[p][i][t] = u32x2{pack2(f32x2{acc[i][t][0], acc[i][t][1]}), pack2(f32x2{acc[i][t][2], acc[i][t][3]})};
    };
    auto ocol = [&](int t) { return fr * 128 + (((4 * n + 2 * t + (fg >> 1)) ^ (fr & 7)) * 16) + (fg & 1) * 8; };

    wait_lgkm<0>();
    TS(7);
    wait_vm<6>();
    TS(5);
    barrier();
    TS(6);
    read_half(IC<0>{});
    fence();
    static_for<0, NQ>([&](auto qc) {
      constexpr int q = decltype(qc)::v;
      constexpr int M = q < 12 ? 12 : 6;
      constexpr bool pk = q % 3 == 0 && q > 0;
      if constexpr (q == 13) {
#pragma unroll
        for (int i = 0; i < 2; ++i)
#pragma unroll
          for (int t = 0; t < 2; ++t) {
            LDS_AS char* o = lds + OUTB + (2 * m + (i ^ h)) * 2048 + ocol(t);
#pragma unroll
            for (int p = 0; p < 4; ++p) *(LDS_AS u32x2*)(o + p * 8192) = po[p][i][t];
          }
        fence();
      }
      if constexpr (pk) pack_acc(IC<q / 3 - 1>{});
      read_half(IC<2 * q + 1>{});
      mma_half(IC<2 * q>{});
      if constexpr (q < 2) interleave(IC<18>{}, IC<6>{}, IC<0>{}); else interleave(IC<(q < 3 ? 18 : 12)>{}, IC<M>{}, IC<(pk ? 8 : 0)>{});
      fence();
      if constexpr (q + 1 < NQ) {
        if constexpr (q < 2) wait_lgkm<0>();
        if constexpr (q == 0) wait_vm<0>();
        if constexpr (q == 13) wait_lgkm<12>();
        TS2(17 + 3 * q);
        barrier();
        TS2(18 + 3 * q);
        fence();
        read_half(IC<2 * q + 2>{});
      }
      mma_half(IC<2 * q + 1>{});
      if constexpr (q + 1 < NQ) interleave(IC<(q < 2 ? 18 : 12)>{}, IC<M>{}, IC<0>{});
      fence();
    });
    pack_acc(IC<4>{});
#pragma unroll
    for (int t = 0; t < 2; ++t) *(LDS_AS u32x2*)(lds + GBB + m * 2048 + ocol(t)) = po[4][0][t];
    wait_lgkm<0>();
    TS(9);
    barrier();
    TS(10);
  }
  asm volatile(".Lpf_end:");
}
}

extern "C" void kernel_launch(void* const* d_in, const int* in_sizes, int n_in, void* d_out, int out_size, void* d_ws,
                              size_t ws_size, hipStream_t stream) {
  (void)in_sizes; (void)n_in; (void)out_size; (void)d_ws; (void)ws_size;
  (void)hipFuncSetAttribute((const void*)ln_linear_kernel, hipFuncAttributeMaxDynamicSharedMemorySize, LDS_BYTES);
  ln_linear_kernel<<<256, 512, LDS_BYTES, stream>>>((const uint16_t*)d_in[0], (const uint16_t*)d_in[1], (const uint16_t*)d_in[2],
                                                    (const uint16_t*)d_in[3], (uint16_t*)d_out
  );
}
```

```cpp
#include <hip/hip_runtime.h>
#include <stdint.h>

typedef short s16x4 __attribute__((ext_vector_type(4)));
typedef __bf16 bf16x2 __attribute__((ext_vector_type(2)));
typedef __bf16 bf16x8 __attribute__((ext_vector_type(8)));
typedef float f32x2 __attribute__((ext_vector_type(2)));
typedef float f32x4 __attribute__((ext_vector_type(4)));
typedef unsigned u32x2 __attribute__((ext_vector_type(2)));
typedef unsigned u32x4 __attribute__((ext_vector_type(4)));
typedef unsigned u32x8 __attribute__((ext_vector_type(8)));
#define LDS_AS __attribute__((address_space(3)))

#define X_POLICY ""
#define T_POLICY ""
#define S_POLICY " nt"
#define THROTTLE 0
#define DEPTH 4
#define NLOAD 4
#define OUTMODE 0
#define BM_ROW 6
#define STAMPS 0

namespace {
constexpr int D = 576;
constexpr int ROWB = D * 2;
constexpr int SLOT = 24576;
constexpr int GBB = 6 * SLOT;
constexpr int SINK = GBB + 8192;
constexpr int LDS_BYTES = GBB + 16384;
constexpr int NQ = 15;

template <int V> struct IC { static constexpr int v = V; };
template <int I, int N, class F> __device__ __forceinline__ void static_for(F&& f) {
  if constexpr (I < N) { f(IC<I>{}); static_for<I + 1, N>(f); }
}
constexpr int slot_of(int q) { return q < 4 ? q : q == 4 ? 0 : q == 5 ? 4 : q == 6 ? 1 : q == 7 ? 5 : q == 8 ? 2 : slot_of(q - 6); }
constexpr int dma_hi(int q) { return (q == 0 ? 5 : q + 1 + DEPTH) < NQ ? (q == 0 ? 5 : q + 1 + DEPTH) : NQ; }
constexpr int dma_lo(int q) { return q == 0 ? 3 : dma_hi(q - 1); }
constexpr int pieces(int q) { return q < 12 ? 24 : 12; }
constexpr int loader_ahead(int q) {
  int n = 0;
  for (int c = q + 2; q > 0 && c < dma_hi(q - 1); ++c) n += pieces(c);
  return n;
}
constexpr int OUTB = GBB;

__device__ __forceinline__ u32x4 make_srd(unsigned lo, unsigned hi, unsigned bytes) { return u32x4{lo, hi & 0xffffu, bytes, 0x00020000u}; }
template <int N> __device__ __forceinline__ void wait_vm() { asm volatile("s_waitcnt vmcnt(%0)" ::"i"(N) : "memory"); }
template <int N> __device__ __forceinline__ void wait_lgkm() { asm volatile("s_waitcnt lgkmcnt(%0)" ::"i"(N) : "memory"); }
__device__ __forceinline__ void barrier() { asm volatile("s_barrier" ::: "memory"); }
__device__ __forceinline__ void fence() { __builtin_amdgcn_sched_barrier(0); }
__device__ __forceinline__ void dma16(unsigned lds_addr, unsigned voff, u32x4 srd, unsigned soff) {
  asm volatile("s_mov_b32 m0, %0\n\ts_nop 0\n\tbuffer_load_dwordx4 %1, %2, %3 offen lds" ::"s"(lds_addr), "v"(voff), "s"(srd), "s"(soff) : "memory");
}
__device__ __forceinline__ void st16(u32x4 v, unsigned voff, u32x4 srd, unsigned soff) {
  asm volatile("buffer_store_dwordx4 %0, %1, %2, %3 offen" S_POLICY "\n\ts_nop 1" ::"v"(v), "v"(voff), "s"(srd), "s"(soff) : "memory");
}
__device__ __forceinline__ void ts_raw(unsigned long long& t) { asm volatile("s_memtime %0" : "=s"(t)); }
template <int I> __device__ __forceinline__ void ts_lane(unsigned& tsv, unsigned long long t) { asm volatile("" : "+s"(t)); asm volatile("v_writelane_b32 %0, %1, %2" : "+v"(tsv) : "s"((unsigned)t), "i"(I)); }
template <int I> __device__ __forceinline__ void ts_put(unsigned& tsv) {
  unsigned long long t_; asm volatile("s_memtime %0\n\ts_waitcnt lgkmcnt(0)" : "=s"(t_) :: "memory");
  asm volatile("v_writelane_b32 %0, %1, %2" : "+v"(tsv) : "s"((unsigned)t_), "i"(I));
}
template <int CTRL> __device__ __forceinline__ float dpp_add(float v) {
  return v + __builtin_bit_cast(float, __builtin_amdgcn_update_dpp(0, __builtin_bit_cast(int, v), CTRL, 0xf, 0xf, true));
}
__device__ __forceinline__ float oct_sum(float v) {
  v = dpp_add<0xB1>(v); v = dpp_add<0x4E>(v); v = dpp_add<0x141>(v);
  return v;
}
__device__ __forceinline__ unsigned pack2(f32x2 v) { return __builtin_bit_cast(unsigned, __builtin_convertvector(v, bf16x2)); }
__device__ __forceinline__ f32x2 unpack2(unsigned u) { return f32x2{__uint_as_float(u << 16), __uint_as_float(u & 0xffff0000u)}; }
template <int N> __device__ __forceinline__ void row_stats(const u32x4& x, float& s, float& q, f32x2 (&y)[4]) {
  float a0, a1, a2, a3, a4, a5, a6, a7;
  const unsigned x0 = x[0], x1 = x[1], x2 = x[2], x3 = x[3];
  asm volatile(
      "s_waitcnt vmcnt(%16)\n\t"
      "v_dot2c_f32_bf16 %0, %14, %10\n\tv_dot2c_f32_bf16 %1, %10, %10\n\t"
      "v_dot2c_f32_bf16 %0, %14, %11\n\tv_dot2c_f32_bf16 %1, %11, %11\n\t"
      "v_dot2c_f32_bf16 %0, %14, %12\n\tv_dot2c_f32_bf16 %1, %12, %12\n\t"
      "v_dot2c_f32_bf16 %0, %14, %13\n\tv_dot2c_f32_bf16 %1, %13, %13\n\t"
      "v_lshlrev_b32 %2, 16, %10\n\tv_and_b32 %3, %15, %10\n\t"
      "v_lshlrev_b32 %4, 16, %11\n\tv_and_b32 %5, %15, %11\n\t"
      "v_lshlrev_b32 %6, 16, %12\n\tv_and_b32 %7, %15, %12\n\t"
      "v_lshlrev_b32 %8, 16, %13\n\tv_and_b32 %9, %15, %13"
      : "+v"(s), "+v"(q), "=&v"(a0), "=&v"(a1), "=&v"(a2), "=&v"(a3), "=&v"(a4), "=&v"(a5), "=&v"(a6), "=&v"(a7)
      : "v"(x0), "v"(x1), "v"(x2), "v"(x3), "s"(0x3f803f80u), "s"(0xffff0000u), "i"(N));
  y[0] = f32x2{a0, a1}; y[1] = f32x2{a2, a3}; y[2] = f32x2{a4, a5}; y[3] = f32x2{a6, a7};
}
__device__ __forceinline__ void pinf(float& v) { asm volatile("" : "+v"(v)); }
__device__ __forceinline__ void pin2(f32x2& v) { asm volatile("" : "+v"(v)); }
__device__ __forceinline__ LDS_AS char* opaque(LDS_AS char* p) { asm volatile("" : "+v"(p)); return p; }
__device__ __forceinline__ float fold32(float a, float b) {
  auto r = __builtin_amdgcn_permlane32_swap(__builtin_bit_cast(unsigned, a), __builtin_bit_cast(unsigned, b), false, false);
  return __builtin_bit_cast(float, (unsigned)r[0]) + __builtin_bit_cast(float, (unsigned)r[1]);
}
__device__ __forceinline__ float fold16(float a, float b) {
  auto r = __builtin_amdgcn_permlane16_swap(__builtin_bit_cast(unsigned, a), __builtin_bit_cast(unsigned, b), false, false);
  return __builtin_bit_cast(float, (unsigned)r[0]) + __builtin_bit_cast(float, (unsigned)r[1]);
}
template <int MASK> __device__ __forceinline__ float sel8(float v) {
  const int i = __builtin_bit_cast(int, v);
  return __builtin_bit_cast(float, __builtin_amdgcn_update_dpp(i, i, 0x128, 0xf, MASK, false));
}
__device__ __forceinline__ float fold8(float a, float b) {
  float t;
  asm("s_nop 1\n\tv_add_f32_dpp %0, %1, %1 row_ror:8 row_mask:0xf bank_mask:0x3\n\tv_add_f32_dpp %0, %2, %2 row_ror:8 row_mask:0xf bank_mask:0xc" : "=&v"(t) : "v"(a), "v"(b));
  return t;
}

#define TS(i) do {} while (0)
#define TS2(i) do {} while (0)
#define TS3(i) do {} while (0)

__global__ __launch_bounds__(512, 2) void ln_linear_kernel(const uint16_t* __restrict__ x, const uint16_t* __restrict__ g,
                                                           const uint16_t* __restrict__ b, const uint16_t* __restrict__ w,
                                                           uint16_t* __restrict__ out
) {
  extern __shared__ __attribute__((aligned(1024))) char smem[];
  LDS_AS char* lds = (LDS_AS char*)smem;
  u32x8 ka;
  u32x2 ko;
  asm volatile("s_load_dwordx8 %0, %2, 0x0\n\ts_load_dwordx2 %1, %2, 0x20" : "=&s"(ka), "=&s"(ko) : "s"(__builtin_amdgcn_kernarg_segment_ptr()));
  const int tid = threadIdx.x, lane = tid & 63;
  const int wv = __builtin_amdgcn_readfirstlane(tid >> 6);
  const int bid = blockIdx.x;
  const int rb = (bid & 7) * 16 + (bid >> 4), h = (bid >> 3) & 1;
  const int wi = wv & 3;
  const int r7 = lane >> 3, c7 = lane & 7;
  const unsigned ldsbase = (unsigned)(uintptr_t)lds;
  const unsigned vm = (unsigned)lane * 16, vt = (unsigned)c7 * 16, rowoff = (unsigned)r7 * ROWB;
  const unsigned xsoff = (unsigned)(rb * 64 + wv * 8) * ROWB;

  if (wv >= 4) {
    unsigned long long pc;
    unsigned len;
    asm volatile("s_getpc_b64 %0\n.Lpf_here:\n\ts_mov_b32 %1, .Lpf_end-.Lpf_here" : "=s"(pc), "=s"(len));
    const u32x4 srdc = make_srd((unsigned)pc, (unsigned)(pc >> 32), len);
    const unsigned voff = lane == 0 ? (unsigned)((bid >> 3) + wi * 32) * 128u : 0xfffffff0u;
    asm volatile("s_mov_b32 m0, %0\n\ts_nop 0\n\tbuffer_load_dword %1, %2, 0 offen lds" ::"s"(ldsbase + SINK), "v"(voff), "s"(srdc) : "memory");
    barrier();
  }
  asm volatile("s_waitcnt lgkmcnt(0)" : "+s"(ka), "+s"(ko));
  const u32x4 srdx = make_srd(ka[0], ka[1], 8192u * ROWB), srdw = make_srd(ka[6], ka[7], (unsigned)D * ROWB), srdo = make_srd(ko[0], ko[1], 8192u * ROWB);
  u32x4 xr[9], gq[4];
  if (wv == 0) {
    const u32x4 srdg = make_srd(ka[2], ka[3], ROWB), srdb = make_srd(ka[4], ka[5], ROWB);
    asm volatile("buffer_load_dwordx4 %0, %1, %2, 0 offen" : "=v"(gq[0]) : "v"(vm), "s"(srdg));
    asm volatile("buffer_load_dwordx4 %0, %1, %2, 0 offen offset:1024" : "=v"(gq[1]) : "v"(vm), "s"(srdg));
    asm volatile("buffer_load_dwordx4 %0, %1, %2, 0 offen" : "=v"(gq[2]) : "v"(vm), "s"(srdb));
    asm volatile("buffer_load_dwordx4 %0, %1, %2, 0 offen offset:1024" : "=v"(gq[3]) : "v"(vm), "s"(srdb));
  }
  TS(12);
  const int c15 = lane & 15, rq = lane >> 4;
  asm volatile("buffer_load_dwordx4 %0, %1, %2, %3 offen offset:1024" X_POLICY : "=v"(xr[8]) : "v"(rowoff + vt), "s"(srdx), "s"(xsoff));
#pragma unroll
  for (int j = 0; j < 8; ++j)
    asm volatile("buffer_load_dwordx4 %0, %1, %2, %3 offen offset:%4" X_POLICY : "=v"(xr[j]) : "v"(rq * 2 * ROWB + c15 * 16), "s"(srdx), "s"(xsoff), "i"((j >> 2) * ROWB + (j & 3) * 256));
  {
    const unsigned li = (unsigned)(tid - 256);
    const unsigned voff = li < 162u ? ((unsigned)(bid >> 3) * 162u + li) * 128u : 0xfffffff0u;
    asm volatile("s_mov_b32 m0, %0\n\ts_nop 0\n\tbuffer_load_dword %1, %2, 0 offen" T_POLICY " lds" ::"s"(ldsbase + SINK), "v"(voff), "s"(srdw) : "memory");
  }
  TS(13);
  if (wv == 0) {
    asm volatile("s_waitcnt vmcnt(10)" : "+v"(gq[0]), "+v"(gq[1]), "+v"(gq[2]), "+v"(gq[3]));
#pragma unroll
    for (int i = 0; i < 4; ++i) {
      const unsigned g0 = gq[i][0], g1 = gq[i][1], g2 = gq[i][2], g3 = gq[i][3];
      const f32x2 a0 = unpack2(g0), a1 = unpack2(g1), a2 = unpack2(g2), a3 = unpack2(g3);
      *(LDS_AS f32x4*)(lds + GBB + i * 2048 + rq * 512 + c15 * 16) = f32x4{a0[0], a0[1], a1[0], a1[1]};
      *(LDS_AS f32x4*)(lds + GBB + i * 2048 + rq * 512 + c15 * 16 + 256) = f32x4{a2[0], a2[1], a3[0], a3[1]};
    }
    wait_lgkm<0>();
  }

  const unsigned wvoff = rowoff + (unsigned)(((((c7 >> 1) ^ (((r7 >> 1) & 1) | ((tid >> 5) & 2))) << 1) | (c7 & 1)) * 16);
  const unsigned wvoffh = (unsigned)(lane >> 2) * ROWB + (unsigned)(((lane & 3) ^ ((lane >> 4) & 2)) * 16);
  auto dma_chunk = [&](auto qc, auto nwc) {
    constexpr int q = decltype(qc)::v, sl = slot_of(q), NW = decltype(nwc)::v;
    const int id = wv & (NW - 1);
    if constexpr (q < 12) {
      const unsigned so = (unsigned)h * 640u + (unsigned)id * 8u * ROWB + (unsigned)((q / 3) * 128 + (q % 3) * 192 * ROWB);
#pragma unroll
      for (int i = 0; i < 24 / NW; ++i) dma16(ldsbase + sl * SLOT + (i * NW + id) * 1024, wvoff, srdw, so + (unsigned)(i * NW * 8) * ROWB);
    } else {
      const unsigned so = (unsigned)h * 64u + (unsigned)id * 16u * ROWB + (unsigned)(512 + (q % 3) * 192 * ROWB);
#pragma unroll
      for (int i = 0; i < 12 / NW; ++i) dma16(ldsbase + sl * SLOT + (i * NW + id) * 1024, wvoffh, srdw, so + (unsigned)(i * NW * 16) * ROWB);
    }
  };

  {
    float sm[3] = {0.f, 0.f, 0.f}, sq[3] = {0.f, 0.f, 0.f};
    f32x2 y[9][4];
#pragma unroll
    for (int i = 0; i < 3; ++i) { pinf(sm[i]); pinf(sq[i]); }
    LDS_AS char* aw[3];
#pragma unroll
    for (int G = 0; G < 2; ++G) aw[G] = opaque(lds + 3 * SLOT + wv * 1024 + (c15 >> 3) * 8192 + (2 * rq + G) * 128 + ((c7 ^ (2 * rq + G)) * 16));
    aw[2] = opaque(lds + 3 * SLOT + 8 * 8192 + wv * 1024 + r7 * 128 + ((c7 ^ r7) * 16));
    LDS_AS char* gr = opaque(lds + GBB + c15 * 16);
    LDS_AS char* grt = opaque(lds + GBB + 2048 + c7 * 16);
    if (wv < 4) barrier();
    TS(1);
    f32x4 gv[5][2], bv[5][2];
    fence();
    static_for<0, 9>([&](auto jc) {
      constexpr int jj = decltype(jc)::v, j = jj == 0 ? 8 : jj - 1;
      if constexpr (jj < 5) {
        constexpr int k = jj == 0 ? 4 : jj - 1;
#pragma unroll
        for (int e = 0; e < 2; ++e) {
          gv[k][e] = *(LDS_AS f32x4*)((k < 4 ? gr + k * 512 : grt) + e * 256);
          bv[k][e] = *(LDS_AS f32x4*)((k < 4 ? gr + k * 512 : grt) + 4096 + e * 256);
        }
        fence();
      }
      row_stats<9 - jj>(xr[j], sm[j >> 2], sq[j >> 2], y[j]);
      if constexpr (jj == 0) TS(14);
    });
    fence();
    TS(3);
    const float mean = oct_sum(fold8(sm[0], sm[1]) + sm[2]) * (1.0f / D);
    const float var = fmaxf(oct_sum(fold8(sq[0], sq[1]) + sq[2]) * (1.0f / D) - mean * mean, 0.f);
    float rs[3], nm[3];
    rs[2] = __builtin_amdgcn_rsqf(var + 1e-5f);
    nm[2] = -mean * rs[2];
    rs[0] = sel8<0xc>(rs[2]); rs[1] = sel8<0x3>(rs[2]);
    nm[0] = sel8<0xc>(nm[2]); nm[1] = sel8<0x3>(nm[2]);
#pragma unroll
    for (int jj = 0; jj < 9; ++jj) {
      const int j = jj == 0 ? 8 : jj - 1, G = j >> 2, k = j == 8 ? 4 : (j & 3);
      if (jj == BM_ROW && wv >= 4) barrier();
#pragma unroll
      for (int e = 0; e < 4; ++e) y[j][e] = __builtin_elementwise_fma(y[j][e], f32x2{rs[G], rs[G]}, f32x2{nm[G], nm[G]});
      fence();
#pragma unroll
      for (int e = 0; e < 4; ++e) {
        const f32x4 gg = gv[k][e >> 1], bb = bv[k][e >> 1];
        y[j][e] = __builtin_elementwise_fma(y[j][e], (e & 1) ? f32x2{gg[2], gg[3]} : f32x2{gg[0], gg[1]}, (e & 1) ? f32x2{bb[2], bb[3]} : f32x2{bb[0], bb[1]});
      }
      fence();
      *(LDS_AS u32x4*)(aw[G] + (j == 8 ? 0 : (j & 3) * 16384)) = u32x4{pack2(y[j][0]), pack2(y[j][1]), pack2(y[j][2]), pack2(y[j][3])};
      fence();
    }
    if (BM_ROW == 9 && wv >= 4) barrier();
  }
  TS(4);

  if (wv >= 4 + NLOAD) {
    wait_lgkm<0>();
    TS(5);
    barrier();
    TS(6);
    LDS_AS char* ord = lds + OUTB + (tid & 127) * 16;
    const unsigned ovoff = rowoff + (unsigned)((c7 ^ r7) * 16);
    const unsigned osoff = (unsigned)(rb * 64 + (wv & 1) * 8) * ROWB + (unsigned)h * 640u;
    static_for<0, NQ - 1>([&](auto qc) {
      constexpr int q = decltype(qc)::v;
      barrier();
      if constexpr (OUTMODE == 1 && q >= 4) {
        constexpr int p = (q - 4) / 3, ph = (q - 4) % 3;
        constexpr int lo = p == 3 ? 0 : (ph == 0 ? 0 : ph == 1 ? 2 : 3), hi = p == 3 ? 4 : (ph == 0 ? 2 : ph == 1 ? 3 : 4);
#pragma unroll
        for (int i = lo; i < hi; ++i) st16(*(LDS_AS u32x4*)(ord + (p & 1) * 8192 + i * 2048), ovoff, srdo, osoff + p * 128u + i * 16u * ROWB);
      }
    });
    TS(9);
    barrier();
#pragma unroll
    for (int i = 0; i < (OUTMODE == 1 ? 2 : 0); ++i)
      st16(*(LDS_AS u32x4*)(ord + i * 2048), (unsigned)(lane >> 2) * ROWB + (unsigned)(((lane & 3) ^ ((lane >> 3) & 3)) * 16), srdo,
           (unsigned)(rb * 64 + (wv & 1) * 16 + i * 32) * ROWB + 512u + (unsigned)h * 64u);
    TS(10);
  } else if (wv >= 4) {
    wait_lgkm<0>();
    dma_chunk(IC<2>{}, IC<NLOAD>{});
    TS(5);
    barrier();
    TS(6);
    static_for<0, NQ - 1>([&](auto qc) {
      constexpr int q = decltype(qc)::v;
      TS2(16 + 3 * q);
      if constexpr (q >= 1) wait_vm<loader_ahead(q) / NLOAD>();
      TS2(17 + 3 * q);
      barrier();
      TS2(18 + 3 * q);
      static_for<dma_lo(q), dma_hi(q)>([&](auto dc) { dma_chunk(dc, IC<NLOAD>{}); });
    });
    barrier();
    TS(10);
  } else {
    const int m = wi >> 1, n = wi & 1;
    const int fr = lane & 15, fg = lane >> 4;
    LDS_AS char* ard[2];
#pragma unroll
    for (int e = 0; e < 2; ++e) ard[e] = opaque(lds + 3 * SLOT + (m * 2 + (fr >> 3)) * 1024 + (fr & 7) * 128 + (((4 * e + fg) ^ (fr & 7)) * 16));
    LDS_AS char* wrd[2][3];
#pragma unroll
    for (int t = 0; t < 2; ++t)
#pragma unroll
      for (int z = 0; z < 3; ++z)
        wrd[t][z] = opaque(lds + z * 2 * SLOT + (8 * fg + (fr >> 2)) * 128 + (((2 * n + t) ^ (((fr >> 3) & 1) | ((fg & 1) << 1))) * 32) + (fr & 3) * 8);

    bf16x8 xa[2][18];
    bf16x8 wf[2][3][2];
    f32x4 acc[2][2];
    u32x4 po[2], pall[4][2];
    LDS_AS char* owr = opaque(lds + OUTB + (m * 16 + fr) * 128 + (((4 * n + 2 * (fg & 1) + (fg >> 1)) ^ (fr & 7)) * 16));

    auto read_a = [&](int i, int ks) { xa[i][ks] = *(LDS_AS bf16x8*)(ard[ks & 1] + i * 4096 + (ks >> 1) * 8192); };
    auto tr_frag = [&](LDS_AS char* p, int step) {
      const s16x4 lo = __builtin_amdgcn_ds_read_tr16_b64_v4i16((LDS_AS s16x4*)p);
      const s16x4 hi = __builtin_amdgcn_ds_read_tr16_b64_v4i16((LDS_AS s16x4*)(p + step));
      return __builtin_bit_cast(bf16x8, __builtin_shufflevector(lo, hi, 0, 1, 2, 3, 4, 5, 6, 7));
    };
    auto read_half = [&](auto hc) {
      constexpr int hs = decltype(hc)::v;
      constexpr int sl = slot_of(hs / 2);
      if constexpr (hs < 24) {
#pragma unroll
        for (int s = 0; s < 3; ++s) {
#pragma unroll
          for (int t = 0; t < 2; ++t) wf[hs & 1][s][t] = tr_frag(wrd[t][sl >> 1] + (sl & 1) * SLOT + ((hs & 1) * 3 + s) * 4096, 512);
          if constexpr (hs < 6) read_a(1, hs * 3 + s);
        }
      } else {
        LDS_AS char* wh = lds + sl * SLOT + (8 * fg + (fr >> 2)) * 64 + ((n ^ (fg & 1)) * 32) + (fr & 3) * 8;
#pragma unroll
        for (int s = 0; s < 3; ++s) wf[hs & 1][s][0] = tr_frag(wh + ((hs & 1) * 3 + s) * 2048, 256);
      }
    };
    auto mma_half = [&](auto hc) {
      constexpr int hs = decltype(hc)::v;
#pragma unroll
      for (int s = 0; s < 3; ++s) {
        const int ks = (hs % 6) * 3 + s;
#pragma unroll
        for (int t = 0; t < (hs < 24 ? 2 : 1); ++t)
#pragma unroll
          for (int i = 0; i < 2; ++i) {
            const f32x4 cin = ks == 0 ? f32x4{0.f, 0.f, 0.f, 0.f} : acc[i][t];
            acc[i][t] = __builtin_amdgcn_mfma_f32_16x16x32_bf16(wf[hs & 1][s][t], xa[i][ks], cin, 0, 0, 0);
          }
      }
    };
    auto interleave = [&](auto nr, auto nm, auto nv) {
      constexpr int R = decltype(nr)::v, M = decltype(nm)::v, V = decltype(nv)::v;
      static_for<0, M>([&](auto ic) {
        constexpr int i = decltype(ic)::v;
        constexpr int r = (R * (i + 1) + M - 1) / M - (R * i + M - 1) / M;
        __builtin_amdgcn_sched_group_barrier(0x008, 1, 0);
        if constexpr (r > 0) __builtin_amdgcn_sched_group_barrier(0x100, r, 0);
        if constexpr (i >= 2 && i - 2 < V / 2) __builtin_amdgcn_sched_group_barrier(0x002, 2, 0);
      });
    };
    auto pack_acc = [&]() {
#pragma unroll
      for (int i = 0; i < 2; ++i) {
        const auto lo = __builtin_amdgcn_permlane16_swap(pack2(f32x2{acc[i][0][0], acc[i][0][1]}), pack2(f32x2{acc[i][1][0], acc[i][1][1]}), false, false);
        const auto hi = __builtin_amdgcn_permlane16_swap(pack2(f32x2{acc[i][0][2], acc[i][0][3]}), pack2(f32x2{acc[i][1][2], acc[i][1][3]}), false, false);
        po[i] = u32x4{(unsigned)lo[0], (unsigned)hi[0], (unsigned)lo[1], (unsigned)hi[1]};
      }
    };
    constexpr auto nreads = [](int hs) { return hs < 6 ? 15 : hs < 24 ? 12 : 6; };

    wait_lgkm<0>();
    TS(7);
    barrier();
    TS(8);
    dma_chunk(IC<0>{}, IC<4>{});
#pragma unroll
    for (int ks = 0; ks < 18; ++ks) read_a(0, ks);
    dma_chunk(IC<1>{}, IC<4>{});
    wait_vm<6>();
    TS(5);
    barrier();
    TS(6);
    read_half(IC<0>{});
    fence();
    static_for<0, NQ>([&](auto qc) {
      constexpr int q = decltype(qc)::v;
      constexpr int M = q < 12 ? 12 : 6;
      constexpr bool pk = q % 3 == 0 && q > 0, wr2 = OUTMODE == 1 && q % 3 == 1 && q > 1;
      if constexpr (pk) { pack_acc(); pall[q / 3 - 1][0] = po[0]; pall[q / 3 - 1][1] = po[1]; }
      if constexpr (wr2) { *(LDS_AS u32x4*)(owr + ((q / 3 - 1) & 1) * 8192 + 4096) = po[1]; fence(); }
      read_half(IC<2 * q + 1>{});
      mma_half(IC<2 * q>{});
      if constexpr (q < 2) interleave(IC<15>{}, IC<6>{}, IC<0>{}); else interleave(IC<nreads(2 * q + 1)>{}, IC<M>{}, IC<(pk ? 12 : 0)>{});
      fence();
      if constexpr (q + 1 < NQ) {
        if constexpr (q < 2 || (DEPTH > 4 && q == 2)) wait_lgkm<0>();
        if constexpr (q == 0) wait_vm<0>();
        if constexpr (wr2) wait_lgkm<nreads(2 * q + 1)>();
        TS3(17 + 3 * q);
        barrier();
        TS3(18 + 3 * q);
        fence();
        if constexpr (pk && OUTMODE == 1) { *(LDS_AS u32x4*)(owr + ((q / 3 - 1) & 1) * 8192) = po[0]; fence(); }
        read_half(IC<2 * q + 2>{});
      }
      mma_half(IC<2 * q + 1>{});
      if constexpr (q + 1 < NQ) interleave(IC<nreads(2 * q + 2)>{}, IC<M>{}, IC<0>{});
      fence();
    });
#pragma unroll
    for (int i = 0; i < 2; ++i)
      *(LDS_AS u32x2*)(lds + OUTB + (2 * i + m) * 1024 + fr * 64 + (((2 * n + (fg >> 1)) ^ ((fr >> 1) & 3)) * 16) + (fg & 1) * 8) =
          u32x2{pack2(f32x2{acc[i][0][0], acc[i][0][1]}), pack2(f32x2{acc[i][0][2], acc[i][0][3]})};
    TS(9);
    if constexpr (OUTMODE == 0) {
#pragma unroll
      for (int p = 0; p < 4; ++p)
#pragma unroll
        for (int i = 0; i < 2; ++i) *(LDS_AS u32x4*)(owr - OUTB + 3 * SLOT + p * 8192 + i * 4096) = pall[p][i];
    }
    wait_lgkm<0>();
    barrier();
    TS(10);
  }
  if constexpr (OUTMODE == 0) {
    const unsigned ovoff = rowoff + (unsigned)((c7 ^ r7) * 16);
    u32x4 ov[4];
#pragma unroll
    for (int p = 0; p < 4; ++p) ov[p] = *(LDS_AS u32x4*)(lds + 3 * SLOT + p * 8192 + tid * 16);
#pragma unroll
    for (int p = 0; p < 4; ++p) st16(ov[p], ovoff, srdo, xsoff + (unsigned)h * 640u + p * 128u);
    if (wv < 4)
      st16(*(LDS_AS u32x4*)(lds + OUTB + tid * 16), (unsigned)(lane >> 2) * ROWB + (unsigned)(((lane & 3) ^ ((lane >> 3) & 3)) * 16), srdo,
           (unsigned)(rb * 64 + wv * 16) * ROWB + 512u + (unsigned)h * 64u);
  }
  asm volatile(".Lpf_end:");
}
}

extern "C" void kernel_launch(void* const* d_in, const int* in_sizes, int n_in, void* d_out, int out_size, void* d_ws,
                              size_t ws_size, hipStream_t stream) {
  (void)in_sizes; (void)n_in; (void)out_size; (void)d_ws; (void)ws_size;
  (void)hipFuncSetAttribute((const void*)ln_linear_kernel, hipFuncAttributeMaxDynamicSharedMemorySize, LDS_BYTES);
  ln_linear_kernel<<<256, 512, LDS_BYTES, stream>>>((const uint16_t*)d_in[0], (const uint16_t*)d_in[1], (const uint16_t*)d_in[2],
                                                    (const uint16_t*)d_in[3], (uint16_t*)d_out
  );
}
```

```cpp
#include <hip/hip_runtime.h>
#include <stdint.h>

typedef short s16x4 __attribute__((ext_vector_type(4)));
typedef __bf16 bf16x2 __attribute__((ext_vector_type(2)));
typedef __bf16 bf16x8 __attribute__((ext_vector_type(8)));
typedef float f32x2 __attribute__((ext_vector_type(2)));
typedef float f32x4 __attribute__((ext_vector_type(4)));
typedef unsigned u32x2 __attribute__((ext_vector_type(2)));
typedef unsigned u32x4 __attribute__((ext_vector_type(4)));
typedef unsigned u32x8 __attribute__((ext_vector_type(8)));
#define LDS_AS __attribute__((address_space(3)))

#define X_POLICY " nt"
#define S_POLICY " nt"
#define LPRIO 2
#define NE 1
#define GB_PIECE 8
#define GQ_POS 4
#define ST_LIST 19, 20, 21, 22, 23, 24, 25
#define STAMPS 0

namespace {
constexpr int D = 576;
constexpr int ROWB = D * 2;
constexpr int SLOT = 24576;
constexpr int XRES = 3 * SLOT;
constexpr int GBB = 6 * SLOT;
constexpr int FLAG = GBB + 8192;
constexpr int SINK = FLAG + 1024;
constexpr int LDS_BYTES = SINK + 1024;

constexpr int store_at(int u) {
  constexpr int at[7] = {ST_LIST};
  for (int k = 0; k < 7; ++k) if (at[k] == u) return k;
  return -1;
}
template <int V> struct IC { static constexpr int v = V; };
template <int I, int N, class F> __device__ __forceinline__ void static_for(F&& f) {
  if constexpr (I < N) { f(IC<I>{}); static_for<I + 1, N>(f); }
}
__device__ __forceinline__ u32x4 make_srd(unsigned lo, unsigned hi, unsigned bytes) { return u32x4{lo, hi & 0xffffu, bytes, 0x00020000u}; }
template <int N> __device__ __forceinline__ void wait_vm() { asm volatile("s_waitcnt vmcnt(%0)" ::"i"(N) : "memory"); }
template <int N> __device__ __forceinline__ void wait_lgkm() { asm volatile("s_waitcnt lgkmcnt(%0)" ::"i"(N) : "memory"); }
__device__ __forceinline__ void barrier() { asm volatile("s_barrier" ::: "memory"); }
__device__ __forceinline__ void fence() { __builtin_amdgcn_sched_barrier(0); }
__device__ __forceinline__ void dma16(unsigned lds_addr, unsigned voff, u32x4 srd, unsigned soff) {
  asm volatile("s_mov_b32 m0, %0\n\ts_nop 0\n\tbuffer_load_dwordx4 %1, %2, %3 offen lds" ::"s"(lds_addr), "v"(voff), "s"(srd), "s"(soff) : "memory");
}
__device__ __forceinline__ void dma3(unsigned lds_addr, unsigned voff, u32x4 srd, unsigned soff) {
  unsigned s1, s2;
  asm volatile("s_mov_b32 m0, %2\n\ts_add_u32 %0, %5, 0x9000\n\tbuffer_load_dwordx4 %3, %4, %5 offen lds\n\t"
               "s_add_u32 m0, m0, 0x1000\n\ts_add_u32 %1, %5, 0x12000\n\tbuffer_load_dwordx4 %3, %4, %0 offen lds\n\t"
               "s_add_u32 m0, m0, 0x1000\n\ts_nop 0\n\tbuffer_load_dwordx4 %3, %4, %1 offen lds"
               : "=&s"(s1), "=&s"(s2) : "s"(lds_addr), "v"(voff), "s"(srd), "s"(soff) : "memory", "scc");
}
__device__ __forceinline__ void touch(unsigned lds_addr, unsigned voff, u32x4 srd) {
  asm volatile("s_mov_b32 m0, %0\n\ts_nop 0\n\tbuffer_load_dword %1, %2, 0 offen lds" ::"s"(lds_addr), "v"(voff), "s"(srd) : "memory");
}
__device__ __forceinline__ void st16(u32x4 v, unsigned voff, u32x4 srd, unsigned soff) {
  asm volatile("buffer_store_dwordx4 %0, %1, %2, %3 offen" S_POLICY "\n\ts_nop 1" ::"v"(v), "v"(voff), "s"(srd), "s"(soff) : "memory");
}
template <int OFF> __device__ __forceinline__ void ldx(u32x4& dst, unsigned voff, u32x4 srd, unsigned soff) {
  asm volatile("buffer_load_dwordx4 %0, %1, %2, %3 offen offset:%4" X_POLICY : "=v"(dst) : "v"(voff), "s"(srd), "s"(soff), "i"(OFF));
}
__device__ __forceinline__ void ldg(u32x4& dst, unsigned voff, u32x4 srd) {
  asm volatile("buffer_load_dwordx4 %0, %1, %2, 0 offen" : "=v"(dst) : "v"(voff), "s"(srd));
}
template <int N> __device__ __forceinline__ void wait_vm_for(u32x4& v) { asm volatile("s_waitcnt vmcnt(%1)" : "+v"(v) : "i"(N)); }
__device__ __forceinline__ void ts_raw(unsigned long long& t) { asm volatile("s_memtime %0" : "=s"(t)); }
template <int I> __device__ __forceinline__ void ts_lane(unsigned& tsv, unsigned long long t) { asm volatile("" : "+s"(t)); asm volatile("v_writelane_b32 %0, %1, %2" : "+v"(tsv) : "s"((unsigned)t), "i"(I)); }
template <int I> __device__ __forceinline__ void ts_put(unsigned& tsv) {
  unsigned long long t_; asm volatile("s_memtime %0\n\ts_waitcnt lgkmcnt(0)" : "=s"(t_) :: "memory");
  asm volatile("v_writelane_b32 %0, %1, %2" : "+v"(tsv) : "s"((unsigned)t_), "i"(I));
}
template <int CTRL> __device__ __forceinline__ float dpp_add(float v) {
  return v + __builtin_bit_cast(float, __builtin_amdgcn_update_dpp(0, __builtin_bit_cast(int, v), CTRL, 0xf, 0xf, true));
}
__device__ __forceinline__ float oct_sum(float v) {
  v = dpp_add<0xB1>(v); v = dpp_add<0x4E>(v); v = dpp_add<0x141>(v);
  return v;
}
__device__ __forceinline__ unsigned pack2(f32x2 v) { return __builtin_bit_cast(unsigned, __builtin_convertvector(v, bf16x2)); }
__device__ __forceinline__ f32x2 unpack2(unsigned u) { return f32x2{__uint_as_float(u << 16), __uint_as_float(u & 0xffff0000u)}; }
template <int N> __device__ __forceinline__ void piece_stats(const u32x4& x, float& s, float& q, f32x2 (&y)[4]) {
  float a0, a1, a2, a3, a4, a5, a6, a7;
  const unsigned x0 = x[0], x1 = x[1], x2 = x[2], x3 = x[3];
  asm volatile(
      "s_waitcnt vmcnt(%16)\n\t"
      "v_dot2c_f32_bf16 %0, %14, %10\n\tv_dot2c_f32_bf16 %1, %10, %10\n\t"
      "v_dot2c_f32_bf16 %0, %14, %11\n\tv_dot2c_f32_bf16 %1, %11, %11\n\t"
      "v_dot2c_f32_bf16 %0, %14, %12\n\tv_dot2c_f32_bf16 %1, %12, %12\n\t"
      "v_dot2c_f32_bf16 %0, %14, %13\n\tv_dot2c_f32_bf16 %1, %13, %13\n\t"
      "v_lshlrev_b32 %2, 16, %10\n\tv_and_b32 %3, %15, %10\n\t"
      "v_lshlrev_b32 %4, 16, %11\n\tv_and_b32 %5, %15, %11\n\t"
      "v_lshlrev_b32 %6, 16, %12\n\tv_and_b32 %7, %15, %12\n\t"
      "v_lshlrev_b32 %8, 16, %13\n\tv_and_b32 %9, %15, %13"
      : "+v"(s), "+v"(q), "=&v"(a0), "=&v"(a1), "=&v"(a2), "=&v"(a3), "=&v"(a4), "=&v"(a5), "=&v"(a6), "=&v"(a7)
      : "v"(x0), "v"(x1), "v"(x2), "v"(x3), "s"(0x3f803f80u), "s"(0xffff0000u), "i"(N));
  y[0] = f32x2{a0, a1}; y[1] = f32x2{a2, a3}; y[2] = f32x2{a4, a5}; y[3] = f32x2{a6, a7};
}
__device__ __forceinline__ void count_in(unsigned lds_addr) { asm volatile("s_mov_b64 exec, 1\n\tds_add_u32 %0, %1\n\ts_mov_b64 exec, -1" ::"v"(lds_addr), "v"(1u) : "memory"); }
__device__ __forceinline__ void keep2(unsigned a, unsigned b) { asm volatile("s_waitcnt lgkmcnt(0)" ::"s"(a), "s"(b) : "memory"); }
__device__ __forceinline__ void pinf(float& v) { asm volatile("" : "+v"(v)); }
__device__ __forceinline__ LDS_AS char* opaque(LDS_AS char* p) { asm volatile("" : "+v"(p)); return p; }
template <int MASK> __device__ __forceinline__ float sel8(float v) {
  const int i = __builtin_bit_cast(int, v);
  return __builtin_bit_cast(float, __builtin_amdgcn_update_dpp(i, i, 0x128, 0xf, MASK, false));
}
__device__ __forceinline__ float fold8(float a, float b) {
  float t;
  asm("s_nop 1\n\tv_add_f32_dpp %0, %1, %1 row_ror:8 row_mask:0xf bank_mask:0x3\n\tv_add_f32_dpp %0, %2, %2 row_ror:8 row_mask:0xf bank_mask:0xc" : "=&v"(t) : "v"(a), "v"(b));
  return t;
}

#define TS(i) do {} while (0)
#define TS2(i) do {} while (0)

__global__ __launch_bounds__(512, 2) void ln_linear_kernel(const uint16_t* __restrict__ x, const uint16_t* __restrict__ g,
                                                           const uint16_t* __restrict__ b, const uint16_t* __restrict__ w,
                                                           uint16_t* __restrict__ out
) {
  extern __shared__ __attribute__((aligned(1024))) char smem[];
  LDS_AS char* lds = (LDS_AS char*)smem;
  u32x8 ka;
  u32x2 ko;
  asm volatile("s_load_dwordx8 %0, %2, 0x0\n\ts_load_dwordx2 %1, %2, 0x20" : "=&s"(ka), "=&s"(ko) : "s"(__builtin_amdgcn_kernarg_segment_ptr()));
  const int tid = threadIdx.x, lane = tid & 63;
  const int wv = __builtin_amdgcn_readfirstlane(tid >> 6);
  const int bid = blockIdx.x, jb = bid >> 3, h = jb & 1;
  const int row0 = (bid & 7) * 1024 + (jb >> 1) * 64;
  const int wi = wv & 3;
  const int r7 = lane >> 3, c7 = lane & 7, c15 = lane & 15, rq = lane >> 4;
  const unsigned ldsbase = (unsigned)(uintptr_t)lds;
  const unsigned rowoff = (unsigned)r7 * ROWB;

  unsigned pf0 = 0, pf1 = 0;
  if (wv >= 4) *(LDS_AS unsigned*)(lds + FLAG) = 0u;
  asm volatile("s_waitcnt lgkmcnt(0)" : "+s"(ka), "+s"(ko)::"memory");
  if (wv >= 4) {
    unsigned long long pc;
    unsigned len;
    asm volatile("s_getpc_b64 %0\n.Lpf_here:\n\ts_mov_b32 %1, .Lpf_end-.Lpf_here" : "=s"(pc), "=s"(len));
    const unsigned o = min((unsigned)(jb + wi * 32) * 128u, len - 128u);
    asm volatile("s_load_dword %0, %2, %3\n\ts_load_dword %1, %2, %4" : "=&s"(pf0), "=&s"(pf1) : "s"(pc), "s"(o), "s"(o + 64u));
    barrier();
  }
  const u32x4 srdx = make_srd(ka[0], ka[1], 8192u * ROWB), srdw = make_srd(ka[6], ka[7], (unsigned)D * ROWB), srdo = make_srd(ko[0], ko[1], 8192u * ROWB);
  u32x4 xr[9], gq;
  {
    const u32x4 srdt = make_srd(wv < 2 ? ka[2] : ka[4], wv < 2 ? ka[3] : ka[5], wv < 4 ? (unsigned)ROWB : 0u);
    const unsigned so = (unsigned)(row0 + wv * 8) * ROWB, xvm = (unsigned)(rq * 2 * ROWB + c15 * 16);
    ldx<1024>(xr[8], rowoff + (unsigned)c7 * 16, srdx, so);
    static_for<0, 8>([&](auto jc) {
      constexpr int j = decltype(jc)::v;
      if constexpr (j == GQ_POS) ldg(gq, (unsigned)(tid & 127) * 16u, srdt);
      ldx<(j >> 2) * ROWB + (j & 3) * 256>(xr[j], xvm, srdx, so);
    });
    touch(ldsbase + SINK, (wv >= 4 && wv < 7 && lane < 18) ? ((unsigned)jb * 18u + (unsigned)lane) * ROWB + (wv == 4 ? 512u : wv == 5 ? 0u : 640u) : 0xfffffff0u, srdw);
  }
  if (wv < 4) barrier();
  TS(13);

  {
    f32x4 gv[5][2], bv[5][2];
    f32x2 y[9][4];
    LDS_AS char* aw[3] = {opaque(lds + XRES + wv * 1024 + (c15 >> 3) * 8192 + (2 * rq) * 128 + ((c7 ^ (2 * rq)) * 16)),
                          opaque(lds + XRES + wv * 1024 + (c15 >> 3) * 8192 + (2 * rq + 1) * 128 + ((c7 ^ (2 * rq + 1)) * 16)),
                          opaque(lds + XRES + 8 * 8192 + wv * 1024 + r7 * 128 + ((c7 ^ r7) * 16))};
    LDS_AS char* gr = opaque(lds + GBB + c15 * 16);
    LDS_AS char* grt = opaque(lds + GBB + 2048 + c7 * 16);
    float sm[3] = {0.f, 0.f, 0.f}, sq[3] = {0.f, 0.f, 0.f};
#pragma unroll
    for (int i = 0; i < 3; ++i) { pinf(sm[i]); pinf(sq[i]); }
    fence();
    static_for<0, 9>([&](auto jc) {
      constexpr int jj = decltype(jc)::v, j = jj == 0 ? 8 : jj - 1;
      piece_stats<9 - jj + (jj <= GQ_POS ? 1 : 0)>(xr[j], sm[j >> 2], sq[j >> 2], y[j]);
      if constexpr (jj == 0) TS(14);
      if constexpr (jj == GQ_POS) {
        wait_vm_for<9 - jj>(gq);
        if (wv < 4) {
          const unsigned g0 = gq[0], g1 = gq[1], g2 = gq[2], g3 = gq[3];
          const f32x2 a0 = unpack2(g0), a1 = unpack2(g1), a2 = unpack2(g2), a3 = unpack2(g3);
          *(LDS_AS f32x4*)(lds + GBB + wi * 2048 + rq * 512 + c15 * 16) = f32x4{a0[0], a0[1], a1[0], a1[1]};
          *(LDS_AS f32x4*)(lds + GBB + wi * 2048 + rq * 512 + c15 * 16 + 256) = f32x4{a2[0], a2[1], a3[0], a3[1]};
          count_in(ldsbase + FLAG);
        }
        fence();
      }
      if constexpr (jj == GB_PIECE) {
        while (__builtin_amdgcn_readfirstlane(*(volatile LDS_AS unsigned*)(lds + FLAG)) < 4u) __builtin_amdgcn_s_sleep(1);
        keep2(pf0, pf1);
      }
      if constexpr (jj >= GB_PIECE) {
#pragma unroll
        for (int kk = jj - GB_PIECE; kk < (jj == 8 ? 5 : jj - GB_PIECE + 1); ++kk) {
          if (kk < 5) {
            const int k = kk == 0 ? 4 : kk - 1;
            LDS_AS char* p = k < 4 ? gr + k * 512 : grt;
#pragma unroll
            for (int e = 0; e < 2; ++e) { gv[k][e] = *(LDS_AS f32x4*)(p + e * 256); bv[k][e] = *(LDS_AS f32x4*)(p + 4096 + e * 256); }
          }
        }
        fence();
      }
    });
    fence();
    TS(3);
    const float mean = oct_sum(fold8(sm[0], sm[1]) + sm[2]) * (1.0f / D);
    const float var = fmaxf(oct_sum(fold8(sq[0], sq[1]) + sq[2]) * (1.0f / D) - mean * mean, 0.f);
    float rs[3], nm[3];
    rs[2] = __builtin_amdgcn_rsqf(var + 1e-5f);
    nm[2] = -mean * rs[2];
    rs[0] = sel8<0xc>(rs[2]); rs[1] = sel8<0x3>(rs[2]);
    nm[0] = sel8<0xc>(nm[2]); nm[1] = sel8<0x3>(nm[2]);
#pragma unroll
    for (int jj = 0; jj < 9; ++jj) {
      const int j = jj == 0 ? 8 : jj - 1, G = j >> 2, k = j == 8 ? 4 : (j & 3);
#pragma unroll
      for (int e = 0; e < 4; ++e) y[j][e] = __builtin_elementwise_fma(y[j][e], f32x2{rs[G], rs[G]}, f32x2{nm[G], nm[G]});
      fence();
#pragma unroll
      for (int e = 0; e < 4; ++e) {
        const f32x4 gg = gv[k][e >> 1], bb = bv[k][e >> 1];
        y[j][e] = __builtin_elementwise_fma(y[j][e], (e & 1) ? f32x2{gg[2], gg[3]} : f32x2{gg[0], gg[1]}, (e & 1) ? f32x2{bb[2], bb[3]} : f32x2{bb[0], bb[1]});
      }
      fence();
      *(LDS_AS u32x4*)(aw[G] + (j == 8 ? 0 : (j & 3) * 16384)) = u32x4{pack2(y[j][0]), pack2(y[j][1]), pack2(y[j][2]), pack2(y[j][3])};
      fence();
    }
  }
  TS(4);

  const unsigned wvoff = rowoff + (unsigned)(((((c7 >> 1) ^ (((r7 >> 1) & 1) | ((tid >> 5) & 2))) << 1) | (c7 & 1)) * 16);
  const unsigned wbase = (unsigned)h * 640u + (unsigned)wi * 8u * ROWB;
  auto request = [&](unsigned hslot, unsigned so) {
    dma3(ldsbase + hslot * (SLOT / 2) + (unsigned)wi * 1024u, wvoff, srdw, so);
  };

  if (wv >= 4) {
    wait_lgkm<0>();
    barrier();
    TS(6);
    __builtin_amdgcn_s_setprio(LPRIO);
    auto req = [&](auto cc) {
      constexpr int c = decltype(cc)::v;
      unsigned wb = wbase, lb = ldsbase + (unsigned)wi * 1024u;
      asm volatile("" : "+s"(wb), "+s"(lb));
      dma3(lb + ((c + 3) % 12) * (SLOT / 2), wvoff, srdw, wb + (c / 6) * 128u + (c % 6) * 96u * ROWB);
    };
    static_for<NE, 3>(req);
    barrier(); TS2(18); static_for<3, 5>(req);
    barrier(); TS2(21); static_for<5, 6>(req);
    wait_vm<12>();
    barrier(); TS2(24); static_for<6, 8>(req);
    static_for<0, 11>([&](auto qc) {
      constexpr int q = decltype(qc)::v;
      wait_vm<(q < 9 ? 12 : q == 9 ? 6 : 0)>();
      barrier();
      TS2(27 + 3 * q);
      if constexpr (q < 8) static_for<2 * q + 8, 2 * q + 10>(req);
    });
    TS(8);
  } else {
    {
      const unsigned hv = (unsigned)(lane >> 2) * ROWB + (unsigned)(((lane & 3) ^ ((lane >> 4) & 2)) * 16);
      const unsigned hs = 512u + (unsigned)h * 64u + (unsigned)wi * 16u * ROWB;
#pragma unroll
      for (int i = 0; i < 9; ++i) dma16(ldsbase + (i * 4 + wi) * 1024, hv, srdw, hs + (unsigned)(i * 64) * ROWB);
#pragma unroll
      for (int c = 0; c < NE; ++c) request(c + 3, wbase + c * 96u * ROWB);
      const unsigned li = (unsigned)tid, kk = (li * 43u) >> 8, ln = li - kk * 6u;
      touch(ldsbase + SINK, li < 108u ? ((unsigned)jb * 18u + kk) * ROWB + (ln + 1u + (ln >= 3u) * 2u) * 128u : 0xfffffff0u, srdw);
    }
    const int m = wi >> 1, n = wi & 1;
    const int fr = lane & 15, fg = lane >> 4;
    bf16x8 xa[2][18];
    LDS_AS char* ard[2][2];
#pragma unroll
    for (int e = 0; e < 2; ++e) {
      const unsigned o = (m * 2 + (fr >> 3)) * 1024 + (fr & 7) * 128 + (((4 * e + fg) ^ (fr & 7)) * 16);
      ard[e][0] = opaque(lds + XRES + o);
      ard[e][1] = opaque(lds + XRES + 5 * 8192 + o);
    }
    LDS_AS char* wrd[2][3];
#pragma unroll
    for (int t = 0; t < 2; ++t)
#pragma unroll
      for (int z = 0; z < 3; ++z)
        wrd[t][z] = opaque(lds + z * 2 * SLOT + (8 * fg + (fr >> 2)) * 128 + (((2 * n + t) ^ (((fr >> 3) & 1) | ((fg & 1) << 1))) * 32) + (fr & 3) * 8);
    LDS_AS char* wh = opaque(lds + (8 * fg + (fr >> 2)) * 64 + ((n ^ (fg & 1)) * 32) + (fr & 3) * 8);
    const unsigned ovoff = (unsigned)fr * ROWB + (unsigned)(2 * (fg & 1) + (fg >> 1)) * 16u;
    const unsigned orow = (unsigned)(row0 + m * 16) * ROWB;
    const unsigned osoff = orow + (unsigned)h * 640u + (unsigned)n * 64u;
    wait_lgkm<0>();
    wait_vm<6 + NE * 3 + 1>();
    TS(7);
    barrier();
    TS(6);

    bf16x8 wf[2][6];
    f32x4 acc[2][2], fin[2][2];
    u32x4 po[4][2];
    auto tr_frag = [&](LDS_AS char* p, int step) {
      const s16x4 lo = __builtin_amdgcn_ds_read_tr16_b64_v4i16((LDS_AS s16x4*)p);
      const s16x4 hi = __builtin_amdgcn_ds_read_tr16_b64_v4i16((LDS_AS s16x4*)(p + step));
      return __builtin_bit_cast(bf16x8, __builtin_shufflevector(lo, hi, 0, 1, 2, 3, 4, 5, 6, 7));
    };
    auto rd = [&](auto uc) {
      constexpr int u = decltype(uc)::v, hsl = u % 12;
      if constexpr (u >= 3) {
#pragma unroll
        for (int s = 0; s < 3; ++s)
#pragma unroll
          for (int t = 0; t < 2; ++t) wf[u & 1][s * 2 + t] = tr_frag(wrd[t][hsl >> 2] + (hsl & 3) * (SLOT / 2) + s * 4096, 512);
      } else {
#pragma unroll
        for (int s = 0; s < 6; ++s) {
          const int ks = u * 6 + s;
          wf[u & 1][s] = tr_frag(wh + ks * 2048, 256);
#pragma unroll
          for (int i = 0; i < 2; ++i) xa[i][ks] = *(LDS_AS bf16x8*)(ard[ks & 1][ks >= 10] + ((ks >> 1) % 5) * 8192 + i * 4096);
        }
      }
    };
    auto mm = [&](auto uc) {
      constexpr int u = decltype(uc)::v;
#pragma unroll
      for (int f = 0; f < 6; ++f) {
        const int ks = u >= 3 ? ((u - 3) % 6) * 3 + f / 2 : u * 6 + f, t = f & 1;
        const bool first = u >= 3 ? ks == 0 : ks < 2;
#pragma unroll
        for (int i = 0; i < 2; ++i) acc[i][t] = __builtin_amdgcn_mfma_f32_16x16x32_bf16(wf[u & 1][f], xa[i][ks], first ? f32x4{0.f, 0.f, 0.f, 0.f} : acc[i][t], 0, 0, 0);
      }
    };
    auto pack = [&](const f32x4& a, const f32x4& c) {
      const auto lo = __builtin_amdgcn_permlane16_swap(pack2(f32x2{a[0], a[1]}), pack2(f32x2{c[0], c[1]}), false, false);
      const auto hi = __builtin_amdgcn_permlane16_swap(pack2(f32x2{a[2], a[3]}), pack2(f32x2{c[2], c[3]}), false, false);
      return u32x4{(unsigned)lo[0], (unsigned)hi[0], (unsigned)lo[1], (unsigned)hi[1]};
    };
    const unsigned hvoff = (unsigned)(fr + (fg & 1) * 32) * ROWB + (unsigned)(fg >> 1) * 16u;
    u32x4 ph;
    auto store = [&](int k) {
      if (k == 0) st16(ph, hvoff, srdo, orow + 512u + (unsigned)h * 64u + (unsigned)n * 32u);
      else st16(po[(k - 1) / 2][(k - 1) & 1], ovoff, srdo, osoff + ((k - 1) / 2) * 128u + ((k - 1) & 1) * 32u * ROWB);
    };
    rd(IC<0>{});
    fence();
    static_for<0, 27>([&](auto uc) {
      constexpr int u = decltype(uc)::v, v = u - 3;
      constexpr bool pk = v >= 6 && v % 6 < 2;
      if constexpr (u < 3) {
        wait_vm<(u == 0 ? 3 + NE * 3 + 1 : u == 1 ? NE * 3 + 1 : (NE > 2 ? 3 : 0) + 1)>();
        wait_lgkm<0>();
        barrier();
        fence();
      } else if constexpr ((v & 1) && u < 26) {
        if constexpr (v == 1) wait_vm<1>();
        barrier();
        fence();
      }
      if constexpr (u < 26) rd(IC<u + 1>{});
      if constexpr (pk) po[v / 6 - 1][v % 6] = pack(fin[v % 6][0], fin[v % 6][1]);
      if constexpr (u == 3) ph = pack(fin[0][0] + fin[0][1], fin[1][0] + fin[1][1]);
      mm(uc);
      if constexpr (u < 26) {
        static_for<0, 12>([&](auto ic) {
          constexpr int i = decltype(ic)::v;
          __builtin_amdgcn_sched_group_barrier(0x008, 1, 0);
          if constexpr (u < 2) { if constexpr (i < 8) __builtin_amdgcn_sched_group_barrier(0x100, 3, 0); }
          else __builtin_amdgcn_sched_group_barrier(0x100, 1, 0);
          if constexpr ((pk && i >= 2 && i < 8) || (u == 3 && i >= 2)) __builtin_amdgcn_sched_group_barrier(0x002, 1, 0);
        });
      }
      fence();
      if constexpr (store_at(u) >= 0) { store(store_at(u)); fence(); }
      if constexpr (u == 2 || (v >= 0 && v % 6 == 5)) {
#pragma unroll
        for (int i = 0; i < 2; ++i) { fin[i][0] = acc[i][0]; fin[i][1] = acc[i][1]; }
      }
    });
    po[3][0] = pack(fin[0][0], fin[0][1]);
    po[3][1] = pack(fin[1][0], fin[1][1]);
    store(7);
    store(8);
    TS(9);
  }
  asm volatile(".Lpf_end:");
}
}

extern "C" void kernel_launch(void* const* d_in, const int* in_sizes, int n_in, void* d_out, int out_size, void* d_ws,
                              size_t ws_size, hipStream_t stream) {
  (void)in_sizes; (void)n_in; (void)out_size; (void)d_ws; (void)ws_size;
  (void)hipFuncSetAttribute((const void*)ln_linear_kernel, hipFuncAttributeMaxDynamicSharedMemorySize, LDS_BYTES);
  ln_linear_kernel<<<256, 512, LDS_BYTES, stream>>>((const uint16_t*)d_in[0], (const uint16_t*)d_in[1], (const uint16_t*)d_in[2],
                                                    (const uint16_t*)d_in[3], (uint16_t*)d_out
  );
}
```

```cpp
#define PROBE 0
#define PROBE_WAVE 0
#include <hip/hip_runtime.h>
#include <stdint.h>

typedef short s16x4 __attribute__((ext_vector_type(4)));
typedef __bf16 bf16x2 __attribute__((ext_vector_type(2)));
typedef __bf16 bf16x8 __attribute__((ext_vector_type(8)));
typedef float f32x2 __attribute__((ext_vector_type(2)));
typedef float f32x4 __attribute__((ext_vector_type(4)));
typedef unsigned u32x2 __attribute__((ext_vector_type(2)));
typedef unsigned u32x4 __attribute__((ext_vector_type(4)));
typedef unsigned u32x8 __attribute__((ext_vector_type(8)));
#define LDS_AS __attribute__((address_space(3)))

#define X_POLICY ""
#define S_POLICY " sc1"
#define LPRIO 2
#define NE 1
#define GB_PIECE 8
#define GQ_POS 4
#define ST_LIST 19, 20, 21, 22, 23, 24, 25
#define STAMPS 0

namespace {
constexpr int D = 576;
constexpr int ROWB = D * 2;
constexpr int SLOT = 24576;
constexpr int XRES = 3 * SLOT;
constexpr int GBB = 6 * SLOT;
constexpr int FLAG = GBB + 8192;
constexpr int SINK = FLAG + 1024;
constexpr int LDS_BYTES = SINK + 1024;

constexpr int store_at(int u) {
  constexpr int at[7] = {ST_LIST};
  for (int k = 0; k < 7; ++k) if (at[k] == u) return k;
  return -1;
}
template <int V> struct IC { static constexpr int v = V; };
template <int I, int N, class F> __device__ __forceinline__ void static_for(F&& f) {
  if constexpr (I < N) { f(IC<I>{}); static_for<I + 1, N>(f); }
}
__device__ __forceinline__ u32x4 make_srd(unsigned lo, unsigned hi, unsigned bytes) { return u32x4{lo, hi & 0xffffu, bytes, 0x00020000u}; }
template <int N> __device__ __forceinline__ void wait_vm() { asm volatile("s_waitcnt vmcnt(%0)" ::"i"(N) : "memory"); }
template <int N> __device__ __forceinline__ void wait_lgkm() { asm volatile("s_waitcnt lgkmcnt(%0)" ::"i"(N) : "memory"); }
__device__ __forceinline__ void barrier() { asm volatile("s_barrier" ::: "memory"); }
__device__ __forceinline__ void fence() { __builtin_amdgcn_sched_barrier(0); }
__device__ __forceinline__ void dma16(unsigned lds_addr, unsigned voff, u32x4 srd, unsigned soff) {
  asm volatile("s_mov_b32 m0, %0\n\ts_nop 0\n\tbuffer_load_dwordx4 %1, %2, %3 offen lds" ::"s"(lds_addr), "v"(voff), "s"(srd), "s"(soff) : "memory");
}
__device__ __forceinline__ void dma3(unsigned lds_addr, unsigned voff, u32x4 srd, unsigned soff) {
  unsigned s1, s2;
  asm volatile("s_mov_b32 m0, %2\n\ts_add_u32 %0, %5, 0x9000\n\tbuffer_load_dwordx4 %3, %4, %5 offen lds\n\t"
               "s_add_u32 m0, m0, 0x1000\n\ts_add_u32 %1, %5, 0x12000\n\tbuffer_load_dwordx4 %3, %4, %0 offen lds\n\t"
               "s_add_u32 m0, m0, 0x1000\n\ts_nop 0\n\tbuffer_load_dwordx4 %3, %4, %1 offen lds"
               : "=&s"(s1), "=&s"(s2) : "s"(lds_addr), "v"(voff), "s"(srd), "s"(soff) : "memory", "scc");
}
__device__ __forceinline__ void touch(unsigned lds_addr, unsigned voff, u32x4 srd) {
  asm volatile("s_mov_b32 m0, %0\n\ts_nop 0\n\tbuffer_load_dword %1, %2, 0 offen lds" ::"s"(lds_addr), "v"(voff), "s"(srd) : "memory");
}
__device__ __forceinline__ void st16(u32x4 v, unsigned voff, u32x4 srd, unsigned soff) {
  asm volatile("buffer_store_dwordx4 %0, %1, %2, %3 offen" S_POLICY "\n\ts_nop 1" ::"v"(v), "v"(voff), "s"(srd), "s"(soff) : "memory");
}
template <int OFF> __device__ __forceinline__ void ldx(u32x4& dst, unsigned voff, u32x4 srd, unsigned soff) {
  asm volatile("buffer_load_dwordx4 %0, %1, %2, %3 offen offset:%4" X_POLICY : "=v"(dst) : "v"(voff), "s"(srd), "s"(soff), "i"(OFF));
}
__device__ __forceinline__ void ldg(u32x4& dst, unsigned voff, u32x4 srd) {
  asm volatile("buffer_load_dwordx4 %0, %1, %2, 0 offen" : "=v"(dst) : "v"(voff), "s"(srd));
}
template <int N> __device__ __forceinline__ void wait_vm_for(u32x4& v) { asm volatile("s_waitcnt vmcnt(%1)" : "+v"(v) : "i"(N)); }
__device__ __forceinline__ void ts_raw(unsigned long long& t) { asm volatile("s_memtime %0" : "=s"(t)); }
template <int I> __device__ __forceinline__ void ts_lane(unsigned& tsv, unsigned long long t) { asm volatile("" : "+s"(t)); asm volatile("v_writelane_b32 %0, %1, %2" : "+v"(tsv) : "s"((unsigned)t), "i"(I)); }
__device__ __forceinline__ void probe_now(unsigned long long& t) { asm volatile("s_memrealtime %0\n\ts_waitcnt lgkmcnt(0)" : "=s"(t) :: "memory"); }
template <int I> __device__ __forceinline__ void ts_put(unsigned& tsv) {
  unsigned long long t_; asm volatile("s_memtime %0\n\ts_waitcnt lgkmcnt(0)" : "=s"(t_) :: "memory");
  asm volatile("v_writelane_b32 %0, %1, %2" : "+v"(tsv) : "s"((unsigned)t_), "i"(I));
}
template <int CTRL> __device__ __forceinline__ float dpp_add(float v) {
  return v + __builtin_bit_cast(float, __builtin_amdgcn_update_dpp(0, __builtin_bit_cast(int, v), CTRL, 0xf, 0xf, true));
}
__device__ __forceinline__ float oct_sum(float v) {
  v = dpp_add<0xB1>(v); v = dpp_add<0x4E>(v); v = dpp_add<0x141>(v);
  return v;
}
__device__ __forceinline__ unsigned pack2(f32x2 v) { return __builtin_bit_cast(unsigned, __builtin_convertvector(v, bf16x2)); }
__device__ __forceinline__ f32x2 unpack2(unsigned u) { return f32x2{__uint_as_float(u << 16), __uint_as_float(u & 0xffff0000u)}; }
template <int N> __device__ __forceinline__ void piece_stats(const u32x4& x, float& s, float& q, f32x2 (&y)[4]) {
  float a0, a1, a2, a3, a4, a5, a6, a7;
  const unsigned x0 = x[0], x1 = x[1], x2 = x[2], x3 = x[3];
  asm volatile(
      "s_waitcnt vmcnt(%16)\n\t"
      "v_dot2c_f32_bf16 %0, %14, %10\n\tv_dot2c_f32_bf16 %1, %10, %10\n\t"
      "v_dot2c_f32_bf16 %0, %14, %11\n\tv_dot2c_f32_bf16 %1, %11, %11\n\t"
      "v_dot2c_f32_bf16 %0, %14, %12\n\tv_dot2c_f32_bf16 %1, %12, %12\n\t"
      "v_dot2c_f32_bf16 %0, %14, %13\n\tv_dot2c_f32_bf16 %1, %13, %13\n\t"
      "v_lshlrev_b32 %2, 16, %10\n\tv_and_b32 %3, %15, %10\n\t"
      "v_lshlrev_b32 %4, 16, %11\n\tv_and_b32 %5, %15, %11\n\t"
      "v_lshlrev_b32 %6, 16, %12\n\tv_and_b32 %7, %15, %12\n\t"
      "v_lshlrev_b32 %8, 16, %13\n\tv_and_b32 %9, %15, %13"
      : "+v"(s), "+v"(q), "=&v"(a0), "=&v"(a1), "=&v"(a2), "=&v"(a3), "=&v"(a4), "=&v"(a5), "=&v"(a6), "=&v"(a7)
      : "v"(x0), "v"(x1), "v"(x2), "v"(x3), "s"(0x3f803f80u), "s"(0xffff0000u), "i"(N));
  y[0] = f32x2{a0, a1}; y[1] = f32x2{a2, a3}; y[2] = f32x2{a4, a5}; y[3] = f32x2{a6, a7};
}
__device__ __forceinline__ void count_in(unsigned lds_addr) { asm volatile("s_mov_b64 exec, 1\n\tds_add_u32 %0, %1\n\ts_mov_b64 exec, -1" ::"v"(lds_addr), "v"(1u) : "memory"); }
__device__ __forceinline__ void keep2(unsigned a, unsigned b) { asm volatile("s_waitcnt lgkmcnt(0)" ::"s"(a), "s"(b) : "memory"); }
__device__ __forceinline__ void pinf(float& v) { asm volatile("" : "+v"(v)); }
__device__ __forceinline__ LDS_AS char* opaque(LDS_AS char* p) { asm volatile("" : "+v"(p)); return p; }
template <int MASK> __device__ __forceinline__ float sel8(float v) {
  const int i = __builtin_bit_cast(int, v);
  return __builtin_bit_cast(float, __builtin_amdgcn_update_dpp(i, i, 0x128, 0xf, MASK, false));
}
__device__ __forceinline__ float fold8(float a, float b) {
  float t;
  asm("s_nop 1\n\tv_add_f32_dpp %0, %1, %1 row_ror:8 row_mask:0xf bank_mask:0x3\n\tv_add_f32_dpp %0, %2, %2 row_ror:8 row_mask:0xf bank_mask:0xc" : "=&v"(t) : "v"(a), "v"(b));
  return t;
}

#define TS(i) do { if constexpr ((i) == PROBE) probe_now(tprobe); } while (0)
#define TS2(i) do {} while (0)

__global__ __launch_bounds__(512, 2) void ln_linear_kernel(const uint16_t* __restrict__ x, const uint16_t* __restrict__ g,
                                                           const uint16_t* __restrict__ b, const uint16_t* __restrict__ w,
                                                           uint16_t* __restrict__ out
) {
  extern __shared__ __attribute__((aligned(1024))) char smem[];
  LDS_AS char* lds = (LDS_AS char*)smem;
  u32x8 ka;
  u32x2 ko;
  asm volatile("s_load_dwordx8 %0, %2, 0x0\n\ts_load_dwordx2 %1, %2, 0x20" : "=&s"(ka), "=&s"(ko) : "s"(__builtin_amdgcn_kernarg_segment_ptr()));
  unsigned long long tprobe = 0;
  if constexpr (PROBE == 0) probe_now(tprobe);
  const int tid = threadIdx.x, lane = tid & 63;
  const int wv = __builtin_amdgcn_readfirstlane(tid >> 6);
  const int bid = blockIdx.x, jb = bid >> 3, h = jb & 1;
  const int row0 = (bid & 7) * 1024 + (jb >> 1) * 64;
  const int wi = wv & 3;
  const int r7 = lane >> 3, c7 = lane & 7, c15 = lane & 15, rq = lane >> 4;
  const unsigned ldsbase = (unsigned)(uintptr_t)lds;
  const unsigned rowoff = (unsigned)r7 * ROWB;

  unsigned pf0 = 0, pf1 = 0;
  if (wv >= 4) *(LDS_AS unsigned*)(lds + FLAG) = 0u;
  asm volatile("s_waitcnt lgkmcnt(0)" : "+s"(ka), "+s"(ko)::"memory");
  if (wv >= 4) {
    unsigned long long pc;
    unsigned len;
    asm volatile("s_getpc_b64 %0\n.Lpf_here:\n\ts_mov_b32 %1, .Lpf_end-.Lpf_here" : "=s"(pc), "=s"(len));
    const unsigned o = min((unsigned)(jb + wi * 32) * 128u, len - 128u);
    asm volatile("s_load_dword %0, %2, %3\n\ts_load_dword %1, %2, %4" : "=&s"(pf0), "=&s"(pf1) : "s"(pc), "s"(o), "s"(o + 64u));
    barrier();
  }
  const u32x4 srdx = make_srd(ka[0], ka[1], 8192u * ROWB), srdw = make_srd(ka[6], ka[7], (unsigned)D * ROWB), srdo = make_srd(ko[0], ko[1], 8192u * ROWB);
  u32x4 xr[9], gq;
  {
    const u32x4 srdt = make_srd(wv < 2 ? ka[2] : ka[4], wv < 2 ? ka[3] : ka[5], wv < 4 ? (unsigned)ROWB : 0u);
    const unsigned so = (unsigned)(row0 + wv * 8) * ROWB, xvm = (unsigned)(rq * 2 * ROWB + c15 * 16);
    ldx<1024>(xr[8], rowoff + (unsigned)c7 * 16, srdx, so);
    static_for<0, 8>([&](auto jc) {
      constexpr int j = decltype(jc)::v;
      if constexpr (j == GQ_POS) ldg(gq, (unsigned)(tid & 127) * 16u, srdt);
      ldx<(j >> 2) * ROWB + (j & 3) * 256>(xr[j], xvm, srdx, so);
    });
    touch(ldsbase + SINK, (wv >= 4 && wv < 7 && lane < 18) ? ((unsigned)jb * 18u + (unsigned)lane) * ROWB + (wv == 4 ? 512u : wv == 5 ? 0u : 640u) : 0xfffffff0u, srdw);
  }
  if (wv < 4) barrier();
  TS(13);

  {
    f32x4 gv[5][2], bv[5][2];
    f32x2 y[9][4];
    LDS_AS char* aw[3] = {opaque(lds + XRES + wv * 1024 + (c15 >> 3) * 8192 + (2 * rq) * 128 + ((c7 ^ (2 * rq)) * 16)),
                          opaque(lds + XRES + wv * 1024 + (c15 >> 3) * 8192 + (2 * rq + 1) * 128 + ((c7 ^ (2 * rq + 1)) * 16)),
                          opaque(lds + XRES + 8 * 8192 + wv * 1024 + r7 * 128 + ((c7 ^ r7) * 16))};
    LDS_AS char* gr = opaque(lds + GBB + c15 * 16);
    LDS_AS char* grt = opaque(lds + GBB + 2048 + c7 * 16);
    float sm[3] = {0.f, 0.f, 0.f}, sq[3] = {0.f, 0.f, 0.f};
#pragma unroll
    for (int i = 0; i < 3; ++i) { pinf(sm[i]); pinf(sq[i]); }
    fence();
    static_for<0, 9>([&](auto jc) {
      constexpr int jj = decltype(jc)::v, j = jj == 0 ? 8 : jj - 1;
      piece_stats<9 - jj + (jj <= GQ_POS ? 1 : 0)>(xr[j], sm[j >> 2], sq[j >> 2], y[j]);
      if constexpr (jj == 0) TS(14);
      if constexpr (jj == GQ_POS) {
        wait_vm_for<9 - jj>(gq);
        if (wv < 4) {
          const unsigned g0 = gq[0], g1 = gq[1], g2 = gq[2], g3 = gq[3];
          const f32x2 a0 = unpack2(g0), a1 = unpack2(g1), a2 = unpack2(g2), a3 = unpack2(g3);
          *(LDS_AS f32x4*)(lds + GBB + wi * 2048 + rq * 512 + c15 * 16) = f32x4{a0[0], a0[1], a1[0], a1[1]};
          *(LDS_AS f32x4*)(lds + GBB + wi * 2048 + rq * 512 + c15 * 16 + 256) = f32x4{a2[0], a2[1], a3[0], a3[1]};
          count_in(ldsbase + FLAG);
        }
        fence();
      }
      if constexpr (jj == GB_PIECE) {
        while (__builtin_amdgcn_readfirstlane(*(volatile LDS_AS unsigned*)(lds + FLAG)) < 4u) __builtin_amdgcn_s_sleep(1);
        keep2(pf0, pf1);
      }
      if constexpr (jj >= GB_PIECE) {
#pragma unroll
        for (int kk = jj - GB_PIECE; kk < (jj == 8 ? 5 : jj - GB_PIECE + 1); ++kk) {
          if (kk < 5) {
            const int k = kk == 0 ? 4 : kk - 1;
            LDS_AS char* p = k < 4 ? gr + k * 512 : grt;
#pragma unroll
            for (int e = 0; e < 2; ++e) { gv[k][e] = *(LDS_AS f32x4*)(p + e * 256); bv[k][e] = *(LDS_AS f32x4*)(p + 4096 + e * 256); }
          }
        }
        fence();
      }
    });
    fence();
    TS(3);
    const float mean = oct_sum(fold8(sm[0], sm[1]) + sm[2]) * (1.0f / D);
    const float var = fmaxf(oct_sum(fold8(sq[0], sq[1]) + sq[2]) * (1.0f / D) - mean * mean, 0.f);
    float rs[3], nm[3];
    rs[2] = __builtin_amdgcn_rsqf(var + 1e-5f);
    nm[2] = -mean * rs[2];
    rs[0] = sel8<0xc>(rs[2]); rs[1] = sel8<0x3>(rs[2]);
    nm[0] = sel8<0xc>(nm[2]); nm[1] = sel8<0x3>(nm[2]);
#pragma unroll
    for (int jj = 0; jj < 9; ++jj) {
      const int j = jj == 0 ? 8 : jj - 1, G = j >> 2, k = j == 8 ? 4 : (j & 3);
#pragma unroll
      for (int e = 0; e < 4; ++e) y[j][e] = __builtin_elementwise_fma(y[j][e], f32x2{rs[G], rs[G]}, f32x2{nm[G], nm[G]});
      fence();
#pragma unroll
      for (int e = 0; e < 4; ++e) {
        const f32x4 gg = gv[k][e >> 1], bb = bv[k][e >> 1];
        y[j][e] = __builtin_elementwise_fma(y[j][e], (e & 1) ? f32x2{gg[2], gg[3]} : f32x2{gg[0], gg[1]}, (e & 1) ? f32x2{bb[2], bb[3]} : f32x2{bb[0], bb[1]});
      }
      fence();
      *(LDS_AS u32x4*)(aw[G] + (j == 8 ? 0 : (j & 3) * 16384)) = u32x4{pack2(y[j][0]), pack2(y[j][1]), pack2(y[j][2]), pack2(y[j][3])};
      fence();
    }
  }
  TS(4);

  const unsigned wvoff = rowoff + (unsigned)(((((c7 >> 1) ^ (((r7 >> 1) & 1) | ((tid >> 5) & 2))) << 1) | (c7 & 1)) * 16);
  const unsigned wbase = (unsigned)h * 640u + (unsigned)wi * 8u * ROWB;
  auto request = [&](unsigned hslot, unsigned so) {
    dma3(ldsbase + hslot * (SLOT / 2) + (unsigned)wi * 1024u, wvoff, srdw, so);
  };

  if (wv >= 4) {
    wait_lgkm<0>();
    barrier();
    TS(6);
    __builtin_amdgcn_s_setprio(LPRIO);
    auto req = [&](auto cc) {
      constexpr int c = decltype(cc)::v;
      unsigned wb = wbase, lb = ldsbase + (unsigned)wi * 1024u;
      asm volatile("" : "+s"(wb), "+s"(lb));
      dma3(lb + ((c + 3) % 12) * (SLOT / 2), wvoff, srdw, wb + (c / 6) * 128u + (c % 6) * 96u * ROWB);
    };
    static_for<NE, 3>(req);
    barrier(); TS2(18); static_for<3, 5>(req);
    barrier(); TS2(21); static_for<5, 6>(req);
    wait_vm<12>();
    barrier(); TS2(24); static_for<6, 8>(req);
    static_for<0, 11>([&](auto qc) {
      constexpr int q = decltype(qc)::v;
      wait_vm<(q < 9 ? 12 : q == 9 ? 6 : 0)>();
      barrier();
      TS2(27 + 3 * q);
      if constexpr (q < 8) static_for<2 * q + 8, 2 * q + 10>(req);
    });
    TS(8);
  } else {
    {
      const unsigned hv = (unsigned)(lane >> 2) * ROWB + (unsigned)(((lane & 3) ^ ((lane >> 4) & 2)) * 16);
      const unsigned hs = 512u + (unsigned)h * 64u + (unsigned)wi * 16u * ROWB;
#pragma unroll
      for (int i = 0; i < 9; ++i) dma16(ldsbase + (i * 4 + wi) * 1024, hv, srdw, hs + (unsigned)(i * 64) * ROWB);
#pragma unroll
      for (int c = 0; c < NE; ++c) request(c + 3, wbase + c * 96u * ROWB);
      const unsigned li = (unsigned)tid, kk = (li * 43u) >> 8, ln = li - kk * 6u;
      touch(ldsbase + SINK, li < 108u ? ((unsigned)jb * 18u + kk) * ROWB + (ln + 1u + (ln >= 3u) * 2u) * 128u : 0xfffffff0u, srdw);
    }
    const int m = wi >> 1, n = wi & 1;
    const int fr = lane & 15, fg = lane >> 4;
    bf16x8 xa[2][18];
    LDS_AS char* ard[2][2];
#pragma unroll
    for (int e = 0; e < 2; ++e) {
      const unsigned o = (m * 2 + (fr >> 3)) * 1024 + (fr & 7) * 128 + (((4 * e + fg) ^ (fr & 7)) * 16);
      ard[e][0] = opaque(lds + XRES + o);
      ard[e][1] = opaque(lds + XRES + 5 * 8192 + o);
    }
    LDS_AS char* wrd[2][3];
#pragma unroll
    for (int t = 0; t < 2; ++t)
#pragma unroll
      for (int z = 0; z < 3; ++z)
        wrd[t][z] = opaque(lds + z * 2 * SLOT + (8 * fg + (fr >> 2)) * 128 + (((2 * n + t) ^ (((fr >> 3) & 1) | ((fg & 1) << 1))) * 32) + (fr & 3) * 8);
    LDS_AS char* wh = opaque(lds + (8 * fg + (fr >> 2)) * 64 + ((n ^ (fg & 1)) * 32) + (fr & 3) * 8);
    const unsigned ovoff = (unsigned)fr * ROWB + (unsigned)(2 * (fg & 1) + (fg >> 1)) * 16u;
    const unsigned orow = (unsigned)(row0 + m * 16) * ROWB;
    const unsigned osoff = orow + (unsigned)h * 640u + (unsigned)n * 64u;
    wait_lgkm<0>();
    wait_vm<6 + NE * 3 + 1>();
    TS(7);
    barrier();
    TS(6);

    bf16x8 wf[2][6];
    f32x4 acc[2][2], fin[2][2];
    u32x4 po[4][2];
    auto tr_frag = [&](LDS_AS char* p, int step) {
      const s16x4 lo = __builtin_amdgcn_ds_read_tr16_b64_v4i16((LDS_AS s16x4*)p);
      const s16x4 hi = __builtin_amdgcn_ds_read_tr16_b64_v4i16((LDS_AS s16x4*)(p + step));
      return __builtin_bit_cast(bf16x8, __builtin_shufflevector(lo, hi, 0, 1, 2, 3, 4, 5, 6, 7));
    };
    auto rd = [&](auto uc) {
      constexpr int u = decltype(uc)::v, hsl = u % 12;
      if constexpr (u >= 3) {
#pragma unroll
        for (int s = 0; s < 3; ++s)
#pragma unroll
          for (int t = 0; t < 2; ++t) wf[u & 1][s * 2 + t] = tr_frag(wrd[t][hsl >> 2] + (hsl & 3) * (SLOT / 2) + s * 4096, 512);
      } else {
#pragma unroll
        for (int s = 0; s < 6; ++s) {
          const int ks = u * 6 + s;
          wf[u & 1][s] = tr_frag(wh + ks * 2048, 256);
#pragma unroll
          for (int i = 0; i < 2; ++i) xa[i][ks] = *(LDS_AS bf16x8*)(ard[ks & 1][ks >= 10] + ((ks >> 1) % 5) * 8192 + i * 4096);
        }
      }
    };
    auto mm = [&](auto uc) {
      constexpr int u = decltype(uc)::v;
#pragma unroll
      for (int f = 0; f < 6; ++f) {
        const int ks = u >= 3 ? ((u - 3) % 6) * 3 + f / 2 : u * 6 + f, t = f & 1;
        const bool first = u >= 3 ? ks == 0 : ks < 2;
#pragma unroll
        for (int i = 0; i < 2; ++i) acc[i][t] = __builtin_amdgcn_mfma_f32_16x16x32_bf16(wf[u & 1][f], xa[i][ks], first ? f32x4{0.f, 0.f, 0.f, 0.f} : acc[i][t], 0, 0, 0);
      }
    };
    auto pack = [&](const f32x4& a, const f32x4& c) {
      const auto lo = __builtin_amdgcn_permlane16_swap(pack2(f32x2{a[0], a[1]}), pack2(f32x2{c[0], c[1]}), false, false);
      const auto hi = __builtin_amdgcn_permlane16_swap(pack2(f32x2{a[2], a[3]}), pack2(f32x2{c[2], c[3]}), false, false);
      return u32x4{(unsigned)lo[0], (unsigned)hi[0], (unsigned)lo[1], (unsigned)hi[1]};
    };
    const unsigned hvoff = (unsigned)(fr + (fg & 1) * 32) * ROWB + (unsigned)(fg >> 1) * 16u;
    u32x4 ph;
    auto store = [&](int k) {
      if (k == 0) st16(ph, hvoff, srdo, orow + 512u + (unsigned)h * 64u + (unsigned)n * 32u);
      else st16(po[(k - 1) / 2][(k - 1) & 1], ovoff, srdo, osoff + ((k - 1) / 2) * 128u + ((k - 1) & 1) * 32u * ROWB);
    };
    rd(IC<0>{});
    fence();
    static_for<0, 27>([&](auto uc) {
      constexpr int u = decltype(uc)::v, v = u - 3;
      constexpr bool pk = v >= 6 && v % 6 < 2;
      if constexpr (u < 3) {
        wait_vm<(u == 0 ? 3 + NE * 3 + 1 : u == 1 ? NE * 3 + 1 : (NE > 2 ? 3 : 0) + 1)>();
        wait_lgkm<0>();
        barrier();
        fence();
      } else if constexpr ((v & 1) && u < 26) {
        if constexpr (v == 1) wait_vm<1>();
        barrier();
        fence();
      }
      if constexpr (u < 26) rd(IC<u + 1>{});
      if constexpr (pk) po[v / 6 - 1][v % 6] = pack(fin[v % 6][0], fin[v % 6][1]);
      if constexpr (u == 3) ph = pack(fin[0][0] + fin[0][1], fin[1][0] + fin[1][1]);
      mm(uc);
      if constexpr (u < 26) {
        static_for<0, 12>([&](auto ic) {
          constexpr int i = decltype(ic)::v;
          __builtin_amdgcn_sched_group_barrier(0x008, 1, 0);
          if constexpr (u < 2) { if constexpr (i < 8) __builtin_amdgcn_sched_group_barrier(0x100, 3, 0); }
          else __builtin_amdgcn_sched_group_barrier(0x100, 1, 0);
          if constexpr ((pk && i >= 2 && i < 8) || (u == 3 && i >= 2)) __builtin_amdgcn_sched_group_barrier(0x002, 1, 0);
        });
      }
      fence();
      if constexpr (store_at(u) >= 0) { store(store_at(u)); fence(); }
      if constexpr (u == 2 || (v >= 0 && v % 6 == 5)) {
#pragma unroll
        for (int i = 0; i < 2; ++i) { fin[i][0] = acc[i][0]; fin[i][1] = acc[i][1]; }
      }
    });
    po[3][0] = pack(fin[0][0], fin[0][1]);
    po[3][1] = pack(fin[1][0], fin[1][1]);
    store(7);
    store(8);
    TS(9);
  }
  if constexpr (PROBE == 11) { asm volatile("s_waitcnt vmcnt(0)" ::: "memory"); probe_now(tprobe); }
  if (wv == PROBE_WAVE) { while (__builtin_amdgcn_s_memrealtime() < tprobe + 4000ull) __builtin_amdgcn_s_sleep(1); }
  asm volatile(".Lpf_end:");
}
}

extern "C" void kernel_launch(void* const* d_in, const int* in_sizes, int n_in, void* d_out, int out_size, void* d_ws,
                              size_t ws_size, hipStream_t stream) {
  (void)in_sizes; (void)n_in; (void)out_size; (void)d_ws; (void)ws_size;
  (void)hipFuncSetAttribute((const void*)ln_linear_kernel, hipFuncAttributeMaxDynamicSharedMemorySize, LDS_BYTES);
  ln_linear_kernel<<<256, 512, LDS_BYTES, stream>>>((const uint16_t*)d_in[0], (const uint16_t*)d_in[1], (const uint16_t*)d_in[2],
                                                    (const uint16_t*)d_in[3], (uint16_t*)d_out
  );
}
```

```cpp
#define PROBE 6
#define PROBE_WAVE 0
#include <hip/hip_runtime.h>
#include <stdint.h>

typedef short s16x4 __attribute__((ext_vector_type(4)));
typedef __bf16 bf16x2 __attribute__((ext_vector_type(2)));
typedef __bf16 bf16x8 __attribute__((ext_vector_type(8)));
typedef float f32x2 __attribute__((ext_vector_type(2)));
typedef float f32x4 __attribute__((ext_vector_type(4)));
typedef unsigned u32x2 __attribute__((ext_vector_type(2)));
typedef unsigned u32x4 __attribute__((ext_vector_type(4)));
typedef unsigned u32x8 __attribute__((ext_vector_type(8)));
#define LDS_AS __attribute__((address_space(3)))

#define X_POLICY ""
#define S_POLICY " sc1"
#define LPRIO 2
#define NE 1
#define GB_PIECE 8
#define GQ_POS 4
#define ST_LIST 19, 20, 21, 22, 23, 24, 25
#define STAMPS 0

namespace {
constexpr int D = 576;
constexpr int ROWB = D * 2;
constexpr int SLOT = 24576;
constexpr int XRES = 3 * SLOT;
constexpr int GBB = 6 * SLOT;
constexpr int FLAG = GBB + 8192;
constexpr int SINK = FLAG + 1024;
constexpr int LDS_BYTES = SINK + 1024;

constexpr int store_at(int u) {
  constexpr int at[7] = {ST_LIST};
  for (int k = 0; k < 7; ++k) if (at[k] == u) return k;
  return -1;
}
template <int V> struct IC { static constexpr int v = V; };
template <int I, int N, class F> __device__ __forceinline__ void static_for(F&& f) {
  if constexpr (I < N) { f(IC<I>{}); static_for<I + 1, N>(f); }
}
__device__ __forceinline__ u32x4 make_srd(unsigned lo, unsigned hi, unsigned bytes) { return u32x4{lo, hi & 0xffffu, bytes, 0x00020000u}; }
template <int N> __device__ __forceinline__ void wait_vm() { asm volatile("s_waitcnt vmcnt(%0)" ::"i"(N) : "memory"); }
template <int N> __device__ __forceinline__ void wait_lgkm() { asm volatile("s_waitcnt lgkmcnt(%0)" ::"i"(N) : "memory"); }
__device__ __forceinline__ void barrier() { asm volatile("s_barrier" ::: "memory"); }
__device__ __forceinline__ void fence() { __builtin_amdgcn_sched_barrier(0); }
__device__ __forceinline__ void dma16(unsigned lds_addr, unsigned voff, u32x4 srd, unsigned soff) {
  asm volatile("s_mov_b32 m0, %0\n\ts_nop 0\n\tbuffer_load_dwordx4 %1, %2, %3 offen lds" ::"s"(lds_addr), "v"(voff), "s"(srd), "s"(soff) : "memory");
}
__device__ __forceinline__ void dma3(unsigned lds_addr, unsigned voff, u32x4 srd, unsigned soff) {
  unsigned s1, s2;
  asm volatile("s_mov_b32 m0, %2\n\ts_add_u32 %0, %5, 0x9000\n\tbuffer_load_dwordx4 %3, %4, %5 offen lds\n\t"
               "s_add_u32 m0, m0, 0x1000\n\ts_add_u32 %1, %5, 0x12000\n\tbuffer_load_dwordx4 %3, %4, %0 offen lds\n\t"
               "s_add_u32 m0, m0, 0x1000\n\ts_nop 0\n\tbuffer_load_dwordx4 %3, %4, %1 offen lds"
               : "=&s"(s1), "=&s"(s2) : "s"(lds_addr), "v"(voff), "s"(srd), "s"(soff) : "memory", "scc");
}
__device__ __forceinline__ void touch(unsigned lds_addr, unsigned voff, u32x4 srd) {
  asm volatile("s_mov_b32 m0, %0\n\ts_nop 0\n\tbuffer_load_dword %1, %2, 0 offen lds" ::"s"(lds_addr), "v"(voff), "s"(srd) : "memory");
}
__device__ __forceinline__ void st16(u32x4 v, unsigned voff, u32x4 srd, unsigned soff) {
  asm volatile("buffer_store_dwordx4 %0, %1, %2, %3 offen" S_POLICY "\n\ts_nop 1" ::"v"(v), "v"(voff), "s"(srd), "s"(soff) : "memory");
}
template <int OFF> __device__ __forceinline__ void ldx(u32x4& dst, unsigned voff, u32x4 srd, unsigned soff) {
  asm volatile("buffer_load_dwordx4 %0, %1, %2, %3 offen offset:%4" X_POLICY : "=v"(dst) : "v"(voff), "s"(srd), "s"(soff), "i"(OFF));
}
__device__ __forceinline__ void ldg(u32x4& dst, unsigned voff, u32x4 srd) {
  asm volatile("buffer_load_dwordx4 %0, %1, %2, 0 offen" : "=v"(dst) : "v"(voff), "s"(srd));
}
template <int N> __device__ __forceinline__ void wait_vm_for(u32x4& v) { asm volatile("s_waitcnt vmcnt(%1)" : "+v"(v) : "i"(N)); }
__device__ __forceinline__ void ts_raw(unsigned long long& t) { asm volatile("s_memtime %0" : "=s"(t)); }
template <int I> __device__ __forceinline__ void ts_lane(unsigned& tsv, unsigned long long t) { asm volatile("" : "+s"(t)); asm volatile("v_writelane_b32 %0, %1, %2" : "+v"(tsv) : "s"((unsigned)t), "i"(I)); }
__device__ __forceinline__ void probe_now(unsigned long long& t) { asm volatile("s_memrealtime %0\n\ts_waitcnt lgkmcnt(0)" : "=s"(t) :: "memory"); }
template <int I> __device__ __forceinline__ void ts_put(unsigned& tsv) {
  unsigned long long t_; asm volatile("s_memtime %0\n\ts_waitcnt lgkmcnt(0)" : "=s"(t_) :: "memory");
  asm volatile("v_writelane_b32 %0, %1, %2" : "+v"(tsv) : "s"((unsigned)t_), "i"(I));
}
template <int CTRL> __device__ __forceinline__ float dpp_add(float v) {
  return v + __builtin_bit_cast(float, __builtin_amdgcn_update_dpp(0, __builtin_bit_cast(int, v), CTRL, 0xf, 0xf, true));
}
__device__ __forceinline__ float oct_sum(float v) {
  v = dpp_add<0xB1>(v); v = dpp_add<0x4E>(v); v = dpp_add<0x141>(v);
  return v;
}
__device__ __forceinline__ unsigned pack2(f32x2 v) { return __builtin_bit_cast(unsigned, __builtin_convertvector(v, bf16x2)); }
__device__ __forceinline__ f32x2 unpack2(unsigned u) { return f32x2{__uint_as_float(u << 16), __uint_as_float(u & 0xffff0000u)}; }
template <int N> __device__ __forceinline__ void piece_stats(const u32x4& x, float& s, float& q, f32x2 (&y)[4]) {
  float a0, a1, a2, a3, a4, a5, a6, a7;
  const unsigned x0 = x[0], x1 = x[1], x2 = x[2], x3 = x[3];
  asm volatile(
      "s_waitcnt vmcnt(%16)\n\t"
      "v_dot2c_f32_bf16 %0, %14, %10\n\tv_dot2c_f32_bf16 %1, %10, %10\n\t"
      "v_dot2c_f32_bf16 %0, %14, %11\n\tv_dot2c_f32_bf16 %1, %11, %11\n\t"
      "v_dot2c_f32_bf16 %0, %14, %12\n\tv_dot2c_f32_bf16 %1, %12, %12\n\t"
      "v_dot2c_f32_bf16 %0, %14, %13\n\tv_dot2c_f32_bf16 %1, %13, %13\n\t"
      "v_lshlrev_b32 %2, 16, %10\n\tv_and_b32 %3, %15, %10\n\t"
      "v_lshlrev_b32 %4, 16, %11\n\tv_and_b32 %5, %15, %11\n\t"
      "v_lshlrev_b32 %6, 16, %12\n\tv_and_b32 %7, %15, %12\n\t"
      "v_lshlrev_b32 %8, 16, %13\n\tv_and_b32 %9, %15, %13"
      : "+v"(s), "+v"(q), "=&v"(a0), "=&v"(a1), "=&v"(a2), "=&v"(a3), "=&v"(a4), "=&v"(a5), "=&v"(a6), "=&v"(a7)
      : "v"(x0), "v"(x1), "v"(x2), "v"(x3), "s"(0x3f803f80u), "s"(0xffff0000u), "i"(N));
  y[0] = f32x2{a0, a1}; y[1] = f32x2{a2, a3}; y[2] = f32x2{a4, a5}; y[3] = f32x2{a6, a7};
}
__device__ __forceinline__ void count_in(unsigned lds_addr) { asm volatile("s_mov_b64 exec, 1\n\tds_add_u32 %0, %1\n\ts_mov_b64 exec, -1" ::"v"(lds_addr), "v"(1u) : "memory"); }
__device__ __forceinline__ void keep2(unsigned a, unsigned b) { asm volatile("s_waitcnt lgkmcnt(0)" ::"s"(a), "s"(b) : "memory"); }
__device__ __forceinline__ void pinf(float& v) { asm volatile("" : "+v"(v)); }
__device__ __forceinline__ LDS_AS char* opaque(LDS_AS char* p) { asm volatile("" : "+v"(p)); return p; }
template <int MASK> __device__ __forceinline__ float sel8(float v) {
  const int i = __builtin_bit_cast(int, v);
  return __builtin_bit_cast(float, __builtin_amdgcn_update_dpp(i, i, 0x128, 0xf, MASK, false));
}
__device__ __forceinline__ float fold8(float a, float b) {
  float t;
  asm("s_nop 1\n\tv_add_f32_dpp %0, %1, %1 row_ror:8 row_mask:0xf bank_mask:0x3\n\tv_add_f32_dpp %0, %2, %2 row_ror:8 row_mask:0xf bank_mask:0xc" : "=&v"(t) : "v"(a), "v"(b));
  return t;
}

#define TS(i) do { if constexpr ((i) == PROBE) probe_now(tprobe); } while (0)
#define TS2(i) do {} while (0)

__global__ __launch_bounds__(512, 2) void ln_linear_kernel(const uint16_t* __restrict__ x, const uint16_t* __restrict__ g,
                                                           const uint16_t* __restrict__ b, const uint16_t* __restrict__ w,
                                                           uint16_t* __restrict__ out
) {
  extern __shared__ __attribute__((aligned(1024))) char smem[];
  LDS_AS char* lds = (LDS_AS char*)smem;
  u32x8 ka;
  u32x2 ko;
  asm volatile("s_load_dwordx8 %0, %2, 0x0\n\ts_load_dwordx2 %1, %2, 0x20" : "=&s"(ka), "=&s"(ko) : "s"(__builtin_amdgcn_kernarg_segment_ptr()));
  unsigned long long tprobe = 0;
  if constexpr (PROBE == 0) probe_now(tprobe);
  const int tid = threadIdx.x, lane = tid & 63;
  const int wv = __builtin_amdgcn_readfirstlane(tid >> 6);
  const int bid = blockIdx.x, jb = bid >> 3, h = jb & 1;
  const int row0 = (bid & 7) * 1024 + (jb >> 1) * 64;
  const int wi = wv & 3;
  const int r7 = lane >> 3, c7 = lane & 7, c15 = lane & 15, rq = lane >> 4;
  const unsigned ldsbase = (unsigned)(uintptr_t)lds;
  const unsigned rowoff = (unsigned)r7 * ROWB;

  unsigned pf0 = 0, pf1 = 0;
  if (wv >= 4) *(LDS_AS unsigned*)(lds + FLAG) = 0u;
  asm volatile("s_waitcnt lgkmcnt(0)" : "+s"(ka), "+s"(ko)::"memory");
  if (wv >= 4) {
    unsigned long long pc;
    unsigned len;
    asm volatile("s_getpc_b64 %0\n.Lpf_here:\n\ts_mov_b32 %1, .Lpf_end-.Lpf_here" : "=s"(pc), "=s"(len));
    const unsigned o = min((unsigned)(jb + wi * 32) * 128u, len - 128u);
    asm volatile("s_load_dword %0, %2, %3\n\ts_load_dword %1, %2, %4" : "=&s"(pf0), "=&s"(pf1) : "s"(pc), "s"(o), "s"(o + 64u));
    barrier();
  }
  const u32x4 srdx = make_srd(ka[0], ka[1], 8192u * ROWB), srdw = make_srd(ka[6], ka[7], (unsigned)D * ROWB), srdo = make_srd(ko[0], ko[1], 8192u * ROWB);
  u32x4 xr[9], gq;
  {
    const u32x4 srdt = make_srd(wv < 2 ? ka[2] : ka[4], wv < 2 ? ka[3] : ka[5], wv < 4 ? (unsigned)ROWB : 0u);
    const unsigned so = (unsigned)(row0 + wv * 8) * ROWB, xvm = (unsigned)(rq * 2 * ROWB + c15 * 16);
    ldx<1024>(xr[8], rowoff + (unsigned)c7 * 16, srdx, so);
    static_for<0, 8>([&](auto jc) {
      constexpr int j = decltype(jc)::v;
      if constexpr (j == GQ_POS) ldg(gq, (unsigned)(tid & 127) * 16u, srdt);
      ldx<(j >> 2) * ROWB + (j & 3) * 256>(xr[j], xvm, srdx, so);
    });
    touch(ldsbase + SINK, (wv >= 4 && wv < 7 && lane < 18) ? ((unsigned)jb * 18u + (unsigned)lane) * ROWB + (wv == 4 ? 512u : wv == 5 ? 0u : 640u) : 0xfffffff0u, srdw);
  }
  if (wv < 4) barrier();
  TS(13);

  {
    f32x4 gv[5][2], bv[5][2];
    f32x2 y[9][4];
    LDS_AS char* aw[3] = {opaque(lds + XRES + wv * 1024 + (c15 >> 3) * 8192 + (2 * rq) * 128 + ((c7 ^ (2 * rq)) * 16)),
                          opaque(lds + XRES + wv * 1024 + (c15 >> 3) * 8192 + (2 * rq + 1) * 128 + ((c7 ^ (2 * rq + 1)) * 16)),
                          opaque(lds + XRES + 8 * 8192 + wv * 1024 + r7 * 128 + ((c7 ^ r7) * 16))};
    LDS_AS char* gr = opaque(lds + GBB + c15 * 16);
    LDS_AS char* grt = opaque(lds + GBB + 2048 + c7 * 16);
    float sm[3] = {0.f, 0.f, 0.f}, sq[3] = {0.f, 0.f, 0.f};
#pragma unroll
    for (int i = 0; i < 3; ++i) { pinf(sm[i]); pinf(sq[i]); }
    fence();
    static_for<0, 9>([&](auto jc) {
      constexpr int jj = decltype(jc)::v, j = jj == 0 ? 8 : jj - 1;
      piece_stats<9 - jj + (jj <= GQ_POS ? 1 : 0)>(xr[j], sm[j >> 2], sq[j >> 2], y[j]);
      if constexpr (jj == 0) TS(14);
      if constexpr (jj == GQ_POS) {
        wait_vm_for<9 - jj>(gq);
        if (wv < 4) {
          const unsigned g0 = gq[0], g1 = gq[1], g2 = gq[2], g3 = gq[3];
          const f32x2 a0 = unpack2(g0), a1 = unpack2(g1), a2 = unpack2(g2), a3 = unpack2(g3);
          *(LDS_AS f32x4*)(lds + GBB + wi * 2048 + rq * 512 + c15 * 16) = f32x4{a0[0], a0[1], a1[0], a1[1]};
          *(LDS_AS f32x4*)(lds + GBB + wi * 2048 + rq * 512 + c15 * 16 + 256) = f32x4{a2[0], a2[1], a3[0], a3[1]};
          count_in(ldsbase + FLAG);
        }
        fence();
      }
      if constexpr (jj == GB_PIECE) {
        while (__builtin_amdgcn_readfirstlane(*(volatile LDS_AS unsigned*)(lds + FLAG)) < 4u) __builtin_amdgcn_s_sleep(1);
        keep2(pf0, pf1);
      }
      if constexpr (jj >= GB_PIECE) {
#pragma unroll
        for (int kk = jj - GB_PIECE; kk < (jj == 8 ? 5 : jj - GB_PIECE + 1); ++kk) {
          if (kk < 5) {
            const int k = kk == 0 ? 4 : kk - 1;
            LDS_AS char* p = k < 4 ? gr + k * 512 : grt;
#pragma unroll
            for (int e = 0; e < 2; ++e) { gv[k][e] = *(LDS_AS f32x4*)(p + e * 256); bv[k][e] = *(LDS_AS f32x4*)(p + 4096 + e * 256); }
          }
        }
        fence();
      }
    });
    fence();
    TS(3);
    const float mean = oct_sum(fold8(sm[0], sm[1]) + sm[2]) * (1.0f / D);
    const float var = fmaxf(oct_sum(fold8(sq[0], sq[1]) + sq[2]) * (1.0f / D) - mean * mean, 0.f);
    float rs[3], nm[3];
    rs[2] = __builtin_amdgcn_rsqf(var + 1e-5f);
    nm[2] = -mean * rs[2];
    rs[0] = sel8<0xc>(rs[2]); rs[1] = sel8<0x3>(rs[2]);
    nm[0] = sel8<0xc>(nm[2]); nm[1] = sel8<0x3>(nm[2]);
#pragma unroll
    for (int jj = 0; jj < 9; ++jj) {
      const int j = jj == 0 ? 8 : jj - 1, G = j >> 2, k = j == 8 ? 4 : (j & 3);
#pragma unroll
      for (int e = 0; e < 4; ++e) y[j][e] = __builtin_elementwise_fma(y[j][e], f32x2{rs[G], rs[G]}, f32x2{nm[G], nm[G]});
      fence();
#pragma unroll
      for (int e = 0; e < 4; ++e) {
        const f32x4 gg = gv[k][e >> 1], bb = bv[k][e >> 1];
        y[j][e] = __builtin_elementwise_fma(y[j][e], (e & 1) ? f32x2{gg[2], gg[3]} : f32x2{gg[0], gg[1]}, (e & 1) ? f32x2{bb[2], bb[3]} : f32x2{bb[0], bb[1]});
      }
      fence();
      *(LDS_AS u32x4*)(aw[G] + (j == 8 ? 0 : (j & 3) * 16384)) = u32x4{pack2(y[j][0]), pack2(y[j][1]), pack2(y[j][2]), pack2(y[j][3])};
      fence();
    }
  }
  TS(4);

  const unsigned wvoff = rowoff + (unsigned)(((((c7 >> 1) ^ (((r7 >> 1) & 1) | ((tid >> 5) & 2))) << 1) | (c7 & 1)) * 16);
  const unsigned wbase = (unsigned)h * 640u + (unsigned)wi * 8u * ROWB;
  auto request = [&](unsigned hslot, unsigned so) {
    dma3(ldsbase + hslot * (SLOT / 2) + (unsigned)wi * 1024u, wvoff, srdw, so);
  };

  if (wv >= 4) {
    wait_lgkm<0>();
    barrier();
    TS(6);
    __builtin_amdgcn_s_setprio(LPRIO);
    auto req = [&](auto cc) {
      constexpr int c = decltype(cc)::v;
      unsigned wb = wbase, lb = ldsbase + (unsigned)wi * 1024u;
      asm volatile("" : "+s"(wb), "+s"(lb));
      dma3(lb + ((c + 3) % 12) * (SLOT / 2), wvoff, srdw, wb + (c / 6) * 128u + (c % 6) * 96u * ROWB);
    };
    static_for<NE, 3>(req);
    barrier(); TS2(18); static_for<3, 5>(req);
    barrier(); TS2(21); static_for<5, 6>(req);
    wait_vm<12>();
    barrier(); TS2(24); static_for<6, 8>(req);
    static_for<0, 11>([&](auto qc) {
      constexpr int q = decltype(qc)::v;
      wait_vm<(q < 9 ? 12 : q == 9 ? 6 : 0)>();
      barrier();
      TS2(27 + 3 * q);
      if constexpr (q < 8) static_for<2 * q + 8, 2 * q + 10>(req);
    });
    TS(8);
  } else {
    {
      const unsigned hv = (unsigned)(lane >> 2) * ROWB + (unsigned)(((lane & 3) ^ ((lane >> 4) & 2)) * 16);
      const unsigned hs = 512u + (unsigned)h * 64u + (unsigned)wi * 16u * ROWB;
#pragma unroll
      for (int i = 0; i < 9; ++i) dma16(ldsbase + (i * 4 + wi) * 1024, hv, srdw, hs + (unsigned)(i * 64) * ROWB);
#pragma unroll
      for (int c = 0; c < NE; ++c) request(c + 3, wbase + c * 96u * ROWB);
      const unsigned li = (unsigned)tid, kk = (li * 43u) >> 8, ln = li - kk * 6u;
      touch(ldsbase + SINK, li < 108u ? ((unsigned)jb * 18u + kk) * ROWB + (ln + 1u + (ln >= 3u) * 2u) * 128u : 0xfffffff0u, srdw);
    }
    const int m = wi >> 1, n = wi & 1;
    const int fr = lane & 15, fg = lane >> 4;
    bf16x8 xa[2][18];
    LDS_AS char* ard[2][2];
#pragma unroll
    for (int e = 0; e < 2; ++e) {
      const unsigned o = (m * 2 + (fr >> 3)) * 1024 + (fr & 7) * 128 + (((4 * e + fg) ^ (fr & 7)) * 16);
      ard[e][0] = opaque(lds + XRES + o);
      ard[e][1] = opaque(lds + XRES + 5 * 8192 + o);
    }
    LDS_AS char* wrd[2][3];
#pragma unroll
    for (int t = 0; t < 2; ++t)
#pragma unroll
      for (int z = 0; z < 3; ++z)
        wrd[t][z] = opaque(lds + z * 2 * SLOT + (8 * fg + (fr >> 2)) * 128 + (((2 * n + t) ^ (((fr >> 3) & 1) | ((fg & 1) << 1))) * 32) + (fr & 3) * 8);
    LDS_AS char* wh = opaque(lds + (8 * fg + (fr >> 2)) * 64 + ((n ^ (fg & 1)) * 32) + (fr & 3) * 8);
    const unsigned ovoff = (unsigned)fr * ROWB + (unsigned)(2 * (fg & 1) + (fg >> 1)) * 16u;
    const unsigned orow = (unsigned)(row0 + m * 16) * ROWB;
    const unsigned osoff = orow + (unsigned)h * 640u + (unsigned)n * 64u;
    wait_lgkm<0>();
    wait_vm<6 + NE * 3 + 1>();
    TS(7);
    barrier();
    TS(6);

    bf16x8 wf[2][6];
    f32x4 acc[2][2], fin[2][2];
    u32x4 po[4][2];
    auto tr_frag = [&](LDS_AS char* p, int step) {
      const s16x4 lo = __builtin_amdgcn_ds_read_tr16_b64_v4i16((LDS_AS s16x4*)p);
      const s16x4 hi = __builtin_amdgcn_ds_read_tr16_b64_v4i16((LDS_AS s16x4*)(p + step));
      return __builtin_bit_cast(bf16x8, __builtin_shufflevector(lo, hi, 0, 1, 2, 3, 4, 5, 6, 7));
    };
    auto rd = [&](auto uc) {
      constexpr int u = decltype(uc)::v, hsl = u % 12;
      if constexpr (u >= 3) {
#pragma unroll
        for (int s = 0; s < 3; ++s)
#pragma unroll
          for (int t = 0; t < 2; ++t) wf[u & 1][s * 2 + t] = tr_frag(wrd[t][hsl >> 2] + (hsl & 3) * (SLOT / 2) + s * 4096, 512);
      } else {
#pragma unroll
        for (int s = 0; s < 6; ++s) {
          const int ks = u * 6 + s;
          wf[u & 1][s] = tr_frag(wh + ks * 2048, 256);
#pragma unroll
          for (int i = 0; i < 2; ++i) xa[i][ks] = *(LDS_AS bf16x8*)(ard[ks & 1][ks >= 10] + ((ks >> 1) % 5) * 8192 + i * 4096);
        }
      }
    };
    auto mm = [&](auto uc) {
      constexpr int u = decltype(uc)::v;
#pragma unroll
      for (int f = 0; f < 6; ++f) {
        const int ks = u >= 3 ? ((u - 3) % 6) * 3 + f / 2 : u * 6 + f, t = f & 1;
        const bool first = u >= 3 ? ks == 0 : ks < 2;
#pragma unroll
        for (int i = 0; i < 2; ++i) acc[i][t] = __builtin_amdgcn_mfma_f32_16x16x32_bf16(wf[u & 1][f], xa[i][ks], first ? f32x4{0.f, 0.f, 0.f, 0.f} : acc[i][t], 0, 0, 0);
      }
    };
    auto pack = [&](const f32x4& a, const f32x4& c) {
      const auto lo = __builtin_amdgcn_permlane16_swap(pack2(f32x2{a[0], a[1]}), pack2(f32x2{c[0], c[1]}), false, false);
      const auto hi = __builtin_amdgcn_permlane16_swap(pack2(f32x2{a[2], a[3]}), pack2(f32x2{c[2], c[3]}), false, false);
      return u32x4{(unsigned)lo[0], (unsigned)hi[0], (unsigned)lo[1], (unsigned)hi[1]};
    };
    const unsigned hvoff = (unsigned)(fr + (fg & 1) * 32) * ROWB + (unsigned)(fg >> 1) * 16u;
    u32x4 ph;
    auto store = [&](int k) {
      if (k == 0) st16(ph, hvoff, srdo, orow + 512u + (unsigned)h * 64u + (unsigned)n * 32u);
      else st16(po[(k - 1) / 2][(k - 1) & 1], ovoff, srdo, osoff + ((k - 1) / 2) * 128u + ((k - 1) & 1) * 32u * ROWB);
    };
    rd(IC<0>{});
    fence();
    static_for<0, 27>([&](auto uc) {
      constexpr int u = decltype(uc)::v, v = u - 3;
      constexpr bool pk = v >= 6 && v % 6 < 2;
      if constexpr (u < 3) {
        wait_vm<(u == 0 ? 3 + NE * 3 + 1 : u == 1 ? NE * 3 + 1 : (NE > 2 ? 3 : 0) + 1)>();
        wait_lgkm<0>();
        barrier();
        fence();
      } else if constexpr ((v & 1) && u < 26) {
        if constexpr (v == 1) wait_vm<1>();
        barrier();
        fence();
      }
      if constexpr (u < 26) rd(IC<u + 1>{});
      if constexpr (pk) po[v / 6 - 1][v % 6] = pack(fin[v % 6][0], fin[v % 6][1]);
      if constexpr (u == 3) ph = pack(fin[0][0] + fin[0][1], fin[1][0] + fin[1][1]);
      mm(uc);
      if constexpr (u < 26) {
        static_for<0, 12>([&](auto ic) {
          constexpr int i = decltype(ic)::v;
          __builtin_amdgcn_sched_group_barrier(0x008, 1, 0);
          if constexpr (u < 2) { if constexpr (i < 8) __builtin_amdgcn_sched_group_barrier(0x100, 3, 0); }
          else __builtin_amdgcn_sched_group_barrier(0x100, 1, 0);
          if constexpr ((pk && i >= 2 && i < 8) || (u == 3 && i >= 2)) __builtin_amdgcn_sched_group_barrier(0x002, 1, 0);
        });
      }
      fence();
      if constexpr (u == 3) TS(12);
      if constexpr (u == 8) TS(5);
      if constexpr (u == 13) TS(2);
      if constexpr (u == 18) TS(10);
      if constexpr (store_at(u) >= 0) { store(store_at(u)); fence(); }
      if constexpr (u == 22) TS(1);
      if constexpr (u == 2 || (v >= 0 && v % 6 == 5)) {
#pragma unroll
        for (int i = 0; i < 2; ++i) { fin[i][0] = acc[i][0]; fin[i][1] = acc[i][1]; }
      }
    });
    po[3][0] = pack(fin[0][0], fin[0][1]);
    po[3][1] = pack(fin[1][0], fin[1][1]);
    store(7);
    store(8);
    TS(9);
  }
  if constexpr (PROBE == 11) { asm volatile("s_waitcnt vmcnt(0)" ::: "memory"); probe_now(tprobe); }
  if (wv == PROBE_WAVE) { while (__builtin_amdgcn_s_memrealtime() < tprobe + 4000ull) __builtin_amdgcn_s_sleep(1); }
  asm volatile(".Lpf_end:");
}
}

extern "C" void kernel_launch(void* const* d_in, const int* in_sizes, int n_in, void* d_out, int out_size, void* d_ws,
                              size_t ws_size, hipStream_t stream) {
  (void)in_sizes; (void)n_in; (void)out_size; (void)d_ws; (void)ws_size;
  (void)hipFuncSetAttribute((const void*)ln_linear_kernel, hipFuncAttributeMaxDynamicSharedMemorySize, LDS_BYTES);
  ln_linear_kernel<<<256, 512, LDS_BYTES, stream>>>((const uint16_t*)d_in[0], (const uint16_t*)d_in[1], (const uint16_t*)d_in[2],
                                                    (const uint16_t*)d_in[3], (uint16_t*)d_out
  );
}
```

```cpp
#include <hip/hip_runtime.h>
#include <stdint.h>

typedef short s16x4 __attribute__((ext_vector_type(4)));
typedef __bf16 bf16x2 __attribute__((ext_vector_type(2)));
typedef __bf16 bf16x8 __attribute__((ext_vector_type(8)));
typedef float f32x2 __attribute__((ext_vector_type(2)));
typedef float f32x4 __attribute__((ext_vector_type(4)));
typedef unsigned u32x2 __attribute__((ext_vector_type(2)));
typedef unsigned u32x4 __attribute__((ext_vector_type(4)));
typedef unsigned u32x8 __attribute__((ext_vector_type(8)));
#define LDS_AS __attribute__((address_space(3)))

#define X_POLICY ""
#define S_POLICY " nt"
#define LPRIO 2
#define OUT_WARM 0
#define NE 3
#define GB_PIECE 8
#define GQ_POS 4
#define ST_LIST 19, 20, 21, 22, 23, 24, 25
#define STAMPS 0

namespace {
constexpr int D = 576;
constexpr int ROWB = D * 2;
constexpr int SLOT = 24576;
constexpr int XRES = 3 * SLOT;
constexpr int GBB = 6 * SLOT;
constexpr int FLAG = GBB + 8192;
constexpr int SINK = FLAG + 1024;
constexpr int LDS_BYTES = SINK + 1024;

constexpr int store_at(int u) {
  constexpr int at[7] = {ST_LIST};
  for (int k = 0; k < 7; ++k) if (at[k] == u) return k;
  return -1;
}
template <int V> struct IC { static constexpr int v = V; };
template <int I, int N, class F> __device__ __forceinline__ void static_for(F&& f) {
  if constexpr (I < N) { f(IC<I>{}); static_for<I + 1, N>(f); }
}
__device__ __forceinline__ u32x4 make_srd(unsigned lo, unsigned hi, unsigned bytes) { return u32x4{lo, hi & 0xffffu, bytes, 0x00020000u}; }
template <int N> __device__ __forceinline__ void wait_vm() { asm volatile("s_waitcnt vmcnt(%0)" ::"i"(N) : "memory"); }
template <int N> __device__ __forceinline__ void wait_lgkm() { asm volatile("s_waitcnt lgkmcnt(%0)" ::"i"(N) : "memory"); }
__device__ __forceinline__ void barrier() { asm volatile("s_barrier" ::: "memory"); }
__device__ __forceinline__ void fence() { __builtin_amdgcn_sched_barrier(0); }
__device__ __forceinline__ void dma16(unsigned lds_addr, unsigned voff, u32x4 srd, unsigned soff) {
  asm volatile("s_mov_b32 m0, %0\n\ts_nop 0\n\tbuffer_load_dwordx4 %1, %2, %3 offen lds" ::"s"(lds_addr), "v"(voff), "s"(srd), "s"(soff) : "memory");
}
__device__ __forceinline__ void dma3(unsigned lds_addr, unsigned voff, u32x4 srd, unsigned soff) {
  unsigned s1, s2;
  asm volatile("s_mov_b32 m0, %2\n\ts_add_u32 %0, %5, 0x9000\n\tbuffer_load_dwordx4 %3, %4, %5 offen lds\n\t"
               "s_add_u32 m0, m0, 0x1000\n\ts_add_u32 %1, %5, 0x12000\n\tbuffer_load_dwordx4 %3, %4, %0 offen lds\n\t"
               "s_add_u32 m0, m0, 0x1000\n\ts_nop 0\n\tbuffer_load_dwordx4 %3, %4, %1 offen lds"
               : "=&s"(s1), "=&s"(s2) : "s"(lds_addr), "v"(voff), "s"(srd), "s"(soff) : "memory", "scc");
}
__device__ __forceinline__ void touch(unsigned lds_addr, unsigned voff, u32x4 srd) {
  asm volatile("s_mov_b32 m0, %0\n\ts_nop 0\n\tbuffer_load_dword %1, %2, 0 offen lds" ::"s"(lds_addr), "v"(voff), "s"(srd) : "memory");
}
__device__ __forceinline__ void st16(u32x4 v, unsigned voff, u32x4 srd, unsigned soff) {
  asm volatile("buffer_store_dwordx4 %0, %1, %2, %3 offen" S_POLICY "\n\ts_nop 1" ::"v"(v), "v"(voff), "s"(srd), "s"(soff) : "memory");
}
template <int OFF> __device__ __forceinline__ void ldx(u32x4& dst, unsigned voff, u32x4 srd, unsigned soff) {
  asm volatile("buffer_load_dwordx4 %0, %1, %2, %3 offen offset:%4" X_POLICY : "=v"(dst) : "v"(voff), "s"(srd), "s"(soff), "i"(OFF));
}
__device__ __forceinline__ void ldg(u32x4& dst, unsigned voff, u32x4 srd) {
  asm volatile("buffer_load_dwordx4 %0, %1, %2, 0 offen" : "=v"(dst) : "v"(voff), "s"(srd));
}
template <int N> __device__ __forceinline__ void wait_vm_for(u32x4& v) { asm volatile("s_waitcnt vmcnt(%1)" : "+v"(v) : "i"(N)); }
__device__ __forceinline__ void ts_raw(unsigned long long& t) { asm volatile("s_memtime %0" : "=s"(t)); }
template <int I> __device__ __forceinline__ void ts_lane(unsigned& tsv, unsigned long long t) { asm volatile("" : "+s"(t)); asm volatile("v_writelane_b32 %0, %1, %2" : "+v"(tsv) : "s"((unsigned)t), "i"(I)); }
template <int I> __device__ __forceinline__ void ts_put(unsigned& tsv) {
  unsigned long long t_; asm volatile("s_memtime %0\n\ts_waitcnt lgkmcnt(0)" : "=s"(t_) :: "memory");
  asm volatile("v_writelane_b32 %0, %1, %2" : "+v"(tsv) : "s"((unsigned)t_), "i"(I));
}
template <int CTRL> __device__ __forceinline__ float dpp_add(float v) {
  return v + __builtin_bit_cast(float, __builtin_amdgcn_update_dpp(0, __builtin_bit_cast(int, v), CTRL, 0xf, 0xf, true));
}
__device__ __forceinline__ float oct_sum(float v) {
  v = dpp_add<0xB1>(v); v = dpp_add<0x4E>(v); v = dpp_add<0x141>(v);
  return v;
}
__device__ __forceinline__ unsigned pack2(f32x2 v) { return __builtin_bit_cast(unsigned, __builtin_convertvector(v, bf16x2)); }
__device__ __forceinline__ f32x2 unpack2(unsigned u) { return f32x2{__uint_as_float(u << 16), __uint_as_float(u & 0xffff0000u)}; }
template <int N> __device__ __forceinline__ void piece_stats(const u32x4& x, float& s, float& q, f32x2 (&y)[4]) {
  float a0, a1, a2, a3, a4, a5, a6, a7;
  const unsigned x0 = x[0], x1 = x[1], x2 = x[2], x3 = x[3];
  asm volatile(
      "s_waitcnt vmcnt(%16)\n\t"
      "v_dot2c_f32_bf16 %0, %14, %10\n\tv_dot2c_f32_bf16 %1, %10, %10\n\t"
      "v_dot2c_f32_bf16 %0, %14, %11\n\tv_dot2c_f32_bf16 %1, %11, %11\n\t"
      "v_dot2c_f32_bf16 %0, %14, %12\n\tv_dot2c_f32_bf16 %1, %12, %12\n\t"
      "v_dot2c_f32_bf16 %0, %14, %13\n\tv_dot2c_f32_bf16 %1, %13, %13\n\t"
      "v_lshlrev_b32 %2, 16, %10\n\tv_and_b32 %3, %15, %10\n\t"
      "v_lshlrev_b32 %4, 16, %11\n\tv_and_b32 %5, %15, %11\n\t"
      "v_lshlrev_b32 %6, 16, %12\n\tv_and_b32 %7, %15, %12\n\t"
      "v_lshlrev_b32 %8, 16, %13\n\tv_and_b32 %9, %15, %13"
      : "+v"(s), "+v"(q), "=&v"(a0), "=&v"(a1), "=&v"(a2), "=&v"(a3), "=&v"(a4), "=&v"(a5), "=&v"(a6), "=&v"(a7)
      : "v"(x0), "v"(x1), "v"(x2), "v"(x3), "s"(0x3f803f80u), "s"(0xffff0000u), "i"(N));
  y[0] = f32x2{a0, a1}; y[1] = f32x2{a2, a3}; y[2] = f32x2{a4, a5}; y[3] = f32x2{a6, a7};
}
__device__ __forceinline__ void count_in(unsigned lds_addr) { asm volatile("s_mov_b64 exec, 1\n\tds_add_u32 %0, %1\n\ts_mov_b64 exec, -1" ::"v"(lds_addr), "v"(1u) : "memory"); }
__device__ __forceinline__ void keep2(unsigned a, unsigned b) { asm volatile("s_waitcnt lgkmcnt(0)" ::"s"(a), "s"(b) : "memory"); }
__device__ __forceinline__ void pinf(float& v) { asm volatile("" : "+v"(v)); }
__device__ __forceinline__ LDS_AS char* opaque(LDS_AS char* p) { asm volatile("" : "+v"(p)); return p; }
template <int MASK> __device__ __forceinline__ float sel8(float v) {
  const int i = __builtin_bit_cast(int, v);
  return __builtin_bit_cast(float, __builtin_amdgcn_update_dpp(i, i, 0x128, 0xf, MASK, false));
}
__device__ __forceinline__ float fold8(float a, float b) {
  float t;
  asm("s_nop 1\n\tv_add_f32_dpp %0, %1, %1 row_ror:8 row_mask:0xf bank_mask:0x3\n\tv_add_f32_dpp %0, %2, %2 row_ror:8 row_mask:0xf bank_mask:0xc" : "=&v"(t) : "v"(a), "v"(b));
  return t;
}

#define TS(i) do {} while (0)
#define TS2(i) do {} while (0)

__global__ __launch_bounds__(512, 2) void ln_linear_kernel(const uint16_t* __restrict__ x, const uint16_t* __restrict__ g,
                                                           const uint16_t* __restrict__ b, const uint16_t* __restrict__ w,
                                                           uint16_t* __restrict__ out
) {
  extern __shared__ __attribute__((aligned(1024))) char smem[];
  LDS_AS char* lds = (LDS_AS char*)smem;
  u32x8 ka;
  u32x2 ko;
  asm volatile("s_load_dwordx8 %0, %2, 0x0\n\ts_load_dwordx2 %1, %2, 0x20" : "=&s"(ka), "=&s"(ko) : "s"(__builtin_amdgcn_kernarg_segment_ptr()));
  const int tid = threadIdx.x, lane = tid & 63;
  const int wv = __builtin_amdgcn_readfirstlane(tid >> 6);
  const int bid = blockIdx.x, jb = bid >> 3, h = jb & 1;
  const int row0 = (bid & 7) * 1024 + (jb >> 1) * 64;
  const int wi = wv & 3;
  const int r7 = lane >> 3, c7 = lane & 7, c15 = lane & 15, rq = lane >> 4;
  const unsigned ldsbase = (unsigned)(uintptr_t)lds;
  const unsigned rowoff = (unsigned)r7 * ROWB;

  unsigned pf0 = 0, pf1 = 0, pf2 = 0;
  if (wv >= 4) *(LDS_AS unsigned*)(lds + FLAG) = 0u;
  asm volatile("s_waitcnt lgkmcnt(0)" : "+s"(ka), "+s"(ko)::"memory");
  if (wv >= 4) {
    unsigned long long pc;
    unsigned len;
    asm volatile("s_getpc_b64 %0\n.Lpf_here:\n\ts_mov_b32 %1, .Lpf_end-.Lpf_here" : "=s"(pc), "=s"(len));
    const unsigned o = min((unsigned)(jb + wi * 32) * 128u, len - 128u);
    asm volatile("s_load_dword %0, %2, %3\n\ts_load_dword %1, %2, %4" : "=&s"(pf0), "=&s"(pf1) : "s"(pc), "s"(o), "s"(o + 64u));
    barrier();
  }
  const u32x4 srdx = make_srd(ka[0], ka[1], 8192u * ROWB), srdw = make_srd(ka[6], ka[7], (unsigned)D * ROWB), srdo = make_srd(ko[0], ko[1], 8192u * ROWB);
  u32x4 xr[9], gq;
  {
    const u32x4 srdt = make_srd(wv < 2 ? ka[2] : ka[4], wv < 2 ? ka[3] : ka[5], wv < 4 ? (unsigned)ROWB : 0u);
    const unsigned so = (unsigned)(row0 + wv * 8) * ROWB, xvm = (unsigned)(rq * 2 * ROWB + c15 * 16);
    ldx<1024>(xr[8], rowoff + (unsigned)c7 * 16, srdx, so);
    static_for<0, 8>([&](auto jc) {
      constexpr int j = decltype(jc)::v;
      if constexpr (j == GQ_POS) ldg(gq, (unsigned)(tid & 127) * 16u, srdt);
      ldx<(j >> 2) * ROWB + (j & 3) * 256>(xr[j], xvm, srdx, so);
    });
    touch(ldsbase + SINK, (wv >= 4 && wv < 7 && lane < 18) ? ((unsigned)jb * 18u + (unsigned)lane) * ROWB + (wv == 4 ? 512u : wv == 5 ? 0u : 640u) : 0xfffffff0u, srdw);
  }
  if (wv < 4) barrier();
  TS(13);

  {
    f32x4 gv[5][2], bv[5][2];
    f32x2 y[9][4];
    LDS_AS char* aw[3] = {opaque(lds + XRES + wv * 1024 + (c15 >> 3) * 8192 + (2 * rq) * 128 + ((c7 ^ (2 * rq)) * 16)),
                          opaque(lds + XRES + wv * 1024 + (c15 >> 3) * 8192 + (2 * rq + 1) * 128 + ((c7 ^ (2 * rq + 1)) * 16)),
                          opaque(lds + XRES + 8 * 8192 + wv * 1024 + r7 * 128 + ((c7 ^ r7) * 16))};
    LDS_AS char* gr = opaque(lds + GBB + c15 * 16);
    LDS_AS char* grt = opaque(lds + GBB + 2048 + c7 * 16);
    float sm[3] = {0.f, 0.f, 0.f}, sq[3] = {0.f, 0.f, 0.f};
#pragma unroll
    for (int i = 0; i < 3; ++i) { pinf(sm[i]); pinf(sq[i]); }
    fence();
    static_for<0, 9>([&](auto jc) {
      constexpr int jj = decltype(jc)::v, j = jj == 0 ? 8 : jj - 1;
      piece_stats<9 - jj + (jj <= GQ_POS ? 1 : 0)>(xr[j], sm[j >> 2], sq[j >> 2], y[j]);
      if constexpr (jj == 0) TS(14);
      if constexpr (jj == GQ_POS) {
        wait_vm_for<9 - jj>(gq);
        if (wv < 4) {
          const unsigned g0 = gq[0], g1 = gq[1], g2 = gq[2], g3 = gq[3];
          const f32x2 a0 = unpack2(g0), a1 = unpack2(g1), a2 = unpack2(g2), a3 = unpack2(g3);
          *(LDS_AS f32x4*)(lds + GBB + wi * 2048 + rq * 512 + c15 * 16) = f32x4{a0[0], a0[1], a1[0], a1[1]};
          *(LDS_AS f32x4*)(lds + GBB + wi * 2048 + rq * 512 + c15 * 16 + 256) = f32x4{a2[0], a2[1], a3[0], a3[1]};
          count_in(ldsbase + FLAG);
        }
        fence();
      }
      if constexpr (jj == GB_PIECE) {
        while (__builtin_amdgcn_readfirstlane(*(volatile LDS_AS unsigned*)(lds + FLAG)) < 4u) __builtin_amdgcn_s_sleep(1);
        keep2(pf0, pf1); keep2(pf2, pf2);
      }
      if constexpr (jj >= GB_PIECE) {
#pragma unroll
        for (int kk = jj - GB_PIECE; kk < (jj == 8 ? 5 : jj - GB_PIECE + 1); ++kk) {
          if (kk < 5) {
            const int k = kk == 0 ? 4 : kk - 1;
            LDS_AS char* p = k < 4 ? gr + k * 512 : grt;
#pragma unroll
            for (int e = 0; e < 2; ++e) { gv[k][e] = *(LDS_AS f32x4*)(p + e * 256); bv[k][e] = *(LDS_AS f32x4*)(p + 4096 + e * 256); }
          }
        }
        fence();
      }
    });
    fence();
    TS(3);
    const float mean = oct_sum(fold8(sm[0], sm[1]) + sm[2]) * (1.0f / D);
    const float var = fmaxf(oct_sum(fold8(sq[0], sq[1]) + sq[2]) * (1.0f / D) - mean * mean, 0.f);
    float rs[3], nm[3];
    rs[2] = __builtin_amdgcn_rsqf(var + 1e-5f);
    nm[2] = -mean * rs[2];
    rs[0] = sel8<0xc>(rs[2]); rs[1] = sel8<0x3>(rs[2]);
    nm[0] = sel8<0xc>(nm[2]); nm[1] = sel8<0x3>(nm[2]);
#pragma unroll
    for (int jj = 0; jj < 9; ++jj) {
      const int j = jj == 0 ? 8 : jj - 1, G = j >> 2, k = j == 8 ? 4 : (j & 3);
#pragma unroll
      for (int e = 0; e < 4; ++e) y[j][e] = __builtin_elementwise_fma(y[j][e], f32x2{rs[G], rs[G]}, f32x2{nm[G], nm[G]});
      fence();
#pragma unroll
      for (int e = 0; e < 4; ++e) {
        const f32x4 gg = gv[k][e >> 1], bb = bv[k][e >> 1];
        y[j][e] = __builtin_elementwise_fma(y[j][e], (e & 1) ? f32x2{gg[2], gg[3]} : f32x2{gg[0], gg[1]}, (e & 1) ? f32x2{bb[2], bb[3]} : f32x2{bb[0], bb[1]});
      }
      fence();
      *(LDS_AS u32x4*)(aw[G] + (j == 8 ? 0 : (j & 3) * 16384)) = u32x4{pack2(y[j][0]), pack2(y[j][1]), pack2(y[j][2]), pack2(y[j][3])};
      fence();
    }
  }
  TS(4);

  const unsigned wvoff = rowoff + (unsigned)(((((c7 >> 1) ^ (((r7 >> 1) & 1) | ((tid >> 5) & 2))) << 1) | (c7 & 1)) * 16);
  const unsigned wbase = (unsigned)h * 640u + (unsigned)wi * 8u * ROWB;
  auto request = [&](unsigned hslot, unsigned so) {
    dma3(ldsbase + hslot * (SLOT / 2) + (unsigned)wi * 1024u, wvoff, srdw, so);
  };

  if (wv >= 4) {
    wait_lgkm<0>();
    barrier();
    TS(6);
    __builtin_amdgcn_s_setprio(LPRIO);
    auto req = [&](auto cc) {
      constexpr int c = decltype(cc)::v;
      unsigned wb = wbase, lb = ldsbase + (unsigned)wi * 1024u;
      asm volatile("" : "+s"(wb), "+s"(lb));
      dma3(lb + ((c + 3) % 12) * (SLOT / 2), wvoff, srdw, wb + (c / 6) * 128u + (c % 6) * 96u * ROWB);
    };
    static_for<NE, 3>(req);
    barrier(); TS2(18); static_for<3, 5>(req);
    barrier(); TS2(21); static_for<5, 6>(req);
    wait_vm<12>();
    barrier(); TS2(24); static_for<6, 8>(req);
    static_for<0, 11>([&](auto qc) {
      constexpr int q = decltype(qc)::v;
      wait_vm<(q < 9 ? 12 : q == 9 ? 6 : 0)>();
      barrier();
      TS2(27 + 3 * q);
      if constexpr (q < 8) static_for<2 * q + 8, 2 * q + 10>(req);
    });
    TS(8);
  } else {
    {
      const unsigned hv = (unsigned)(lane >> 2) * ROWB + (unsigned)(((lane & 3) ^ ((lane >> 4) & 2)) * 16);
      const unsigned hs = 512u + (unsigned)h * 64u + (unsigned)wi * 16u * ROWB;
#pragma unroll
      for (int i = 0; i < 9; ++i) dma16(ldsbase + (i * 4 + wi) * 1024, hv, srdw, hs + (unsigned)(i * 64) * ROWB);
#pragma unroll
      for (int c = 0; c < NE; ++c) request(c + 3, wbase + c * 96u * ROWB);
      const unsigned li = (unsigned)tid, kk = (li * 43u) >> 8, ln = li - kk * 6u;
      touch(ldsbase + SINK, li < 108u ? ((unsigned)jb * 18u + kk) * ROWB + (ln + 1u + (ln >= 3u) * 2u) * 128u : 0xfffffff0u, srdw);
    }
    const int m = wi >> 1, n = wi & 1;
    const int fr = lane & 15, fg = lane >> 4;
    bf16x8 xa[2][18];
    LDS_AS char* ard[2][2];
#pragma unroll
    for (int e = 0; e < 2; ++e) {
      const unsigned o = (m * 2 + (fr >> 3)) * 1024 + (fr & 7) * 128 + (((4 * e + fg) ^ (fr & 7)) * 16);
      ard[e][0] = opaque(lds + XRES + o);
      ard[e][1] = opaque(lds + XRES + 5 * 8192 + o);
    }
    LDS_AS char* wrd[2][3];
#pragma unroll
    for (int t = 0; t < 2; ++t)
#pragma unroll
      for (int z = 0; z < 3; ++z)
        wrd[t][z] = opaque(lds + z * 2 * SLOT + (8 * fg + (fr >> 2)) * 128 + (((2 * n + t) ^ (((fr >> 3) & 1) | ((fg & 1) << 1))) * 32) + (fr & 3) * 8);
    LDS_AS char* wh = opaque(lds + (8 * fg + (fr >> 2)) * 64 + ((n ^ (fg & 1)) * 32) + (fr & 3) * 8);
    const unsigned ovoff = (unsigned)fr * ROWB + (unsigned)(2 * (fg & 1) + (fg >> 1)) * 16u;
    const unsigned orow = (unsigned)(row0 + m * 16) * ROWB;
    const unsigned osoff = orow + (unsigned)h * 640u + (unsigned)n * 64u;
    wait_lgkm<0>();
    wait_vm<6 + NE * 3 + 1>();
    TS(7);
    barrier();
    TS(6);

    bf16x8 wf[2][6];
    f32x4 acc[2][2], fin[2][2];
    u32x4 po[4][2];
    auto tr_frag = [&](LDS_AS char* p, int step) {
      const s16x4 lo = __builtin_amdgcn_ds_read_tr16_b64_v4i16((LDS_AS s16x4*)p);
      const s16x4 hi = __builtin_amdgcn_ds_read_tr16_b64_v4i16((LDS_AS s16x4*)(p + step));
      return __builtin_bit_cast(bf16x8, __builtin_shufflevector(lo, hi, 0, 1, 2, 3, 4, 5, 6, 7));
    };
    auto rd = [&](auto uc) {
      constexpr int u = decltype(uc)::v, hsl = u % 12;
      if constexpr (u >= 3) {
#pragma unroll
        for (int s = 0; s < 3; ++s)
#pragma unroll
          for (int t = 0; t < 2; ++t) wf[u & 1][s * 2 + t] = tr_frag(wrd[t][hsl >> 2] + (hsl & 3) * (SLOT / 2) + s * 4096, 512);
      } else {
#pragma unroll
        for (int s = 0; s < 6; ++s) {
          const int ks = u * 6 + s;
          wf[u & 1][s] = tr_frag(wh + ks * 2048, 256);
#pragma unroll
          for (int i = 0; i < 2; ++i) xa[i][ks] = *(LDS_AS bf16x8*)(ard[ks & 1][ks >= 10] + ((ks >> 1) % 5) * 8192 + i * 4096);
        }
      }
    };
    auto mm = [&](auto uc) {
      constexpr int u = decltype(uc)::v;
#pragma unroll
      for (int f = 0; f < 6; ++f) {
        const int ks = u >= 3 ? ((u - 3) % 6) * 3 + f / 2 : u * 6 + f, t = f & 1;
        const bool first = u >= 3 ? ks == 0 : ks < 2;
#pragma unroll
        for (int i = 0; i < 2; ++i) acc[i][t] = __builtin_amdgcn_mfma_f32_16x16x32_bf16(wf[u & 1][f], xa[i][ks], first ? f32x4{0.f, 0.f, 0.f, 0.f} : acc[i][t], 0, 0, 0);
      }
    };
    auto pack = [&](const f32x4& a, const f32x4& c) {
      const auto lo = __builtin_amdgcn_permlane16_swap(pack2(f32x2{a[0], a[1]}), pack2(f32x2{c[0], c[1]}), false, false);
      const auto hi = __builtin_amdgcn_permlane16_swap(pack2(f32x2{a[2], a[3]}), pack2(f32x2{c[2], c[3]}), false, false);
      return u32x4{(unsigned)lo[0], (unsigned)hi[0], (unsigned)lo[1], (unsigned)hi[1]};
    };
    const unsigned hvoff = (unsigned)(fr + (fg & 1) * 32) * ROWB + (unsigned)(fg >> 1) * 16u;
    u32x4 ph;
    auto store = [&](int k) {
      if (k == 0) st16(ph, hvoff, srdo, orow + 512u + (unsigned)h * 64u + (unsigned)n * 32u);
      else st16(po[(k - 1) / 2][(k - 1) & 1], ovoff, srdo, osoff + ((k - 1) / 2) * 128u + ((k - 1) & 1) * 32u * ROWB);
    };
    rd(IC<0>{});
    fence();
    static_for<0, 27>([&](auto uc) {
      constexpr int u = decltype(uc)::v, v = u - 3;
      constexpr bool pk = v >= 6 && v % 6 < 2;
      if constexpr (u < 3) {
        wait_vm<(u == 0 ? 3 + NE * 3 + 1 : u == 1 ? NE * 3 + 1 : (NE > 2 ? 3 : 0) + 1)>();
        wait_lgkm<0>();
        barrier();
        fence();
      } else if constexpr ((v & 1) && u < 26) {
        if constexpr (v == 1) wait_vm<1>();
        barrier();
        fence();
      }
      if constexpr (u < 26) rd(IC<u + 1>{});
      if constexpr (pk) po[v / 6 - 1][v % 6] = pack(fin[v % 6][0], fin[v % 6][1]);
      if constexpr (u == 3) ph = pack(fin[0][0] + fin[0][1], fin[1][0] + fin[1][1]);
      mm(uc);
      if constexpr (u < 26) {
        static_for<0, 12>([&](auto ic) {
          constexpr int i = decltype(ic)::v;
          __builtin_amdgcn_sched_group_barrier(0x008, 1, 0);
          if constexpr (u < 2) { if constexpr (i < 8) __builtin_amdgcn_sched_group_barrier(0x100, 3, 0); }
          else __builtin_amdgcn_sched_group_barrier(0x100, 1, 0);
          if constexpr ((pk && i >= 2 && i < 8) || (u == 3 && i >= 2)) __builtin_amdgcn_sched_group_barrier(0x002, 1, 0);
        });
      }
      fence();
      if constexpr (store_at(u) >= 0) { store(store_at(u)); fence(); }
      if constexpr (u == 2 || (v >= 0 && v % 6 == 5)) {
#pragma unroll
        for (int i = 0; i < 2; ++i) { fin[i][0] = acc[i][0]; fin[i][1] = acc[i][1]; }
      }
    });
    po[3][0] = pack(fin[0][0], fin[0][1]);
    po[3][1] = pack(fin[1][0], fin[1][1]);
    store(7);
    store(8);
    TS(9);
  }
  asm volatile(".Lpf_end:");
}
}

extern "C" void kernel_launch(void* const* d_in, const int* in_sizes, int n_in, void* d_out, int out_size, void* d_ws,
                              size_t ws_size, hipStream_t stream) {
  (void)in_sizes; (void)n_in; (void)out_size; (void)d_ws; (void)ws_size;
  (void)hipFuncSetAttribute((const void*)ln_linear_kernel, hipFuncAttributeMaxDynamicSharedMemorySize, LDS_BYTES);
  ln_linear_kernel<<<256, 512, LDS_BYTES, stream>>>((const uint16_t*)d_in[0], (const uint16_t*)d_in[1], (const uint16_t*)d_in[2],
                                                    (const uint16_t*)d_in[3], (uint16_t*)d_out
  );
}
```

```cpp
#define PROBE 13
#define PROBE_WAVE 0
#include <hip/hip_runtime.h>
#include <stdint.h>

typedef short s16x4 __attribute__((ext_vector_type(4)));
typedef __bf16 bf16x2 __attribute__((ext_vector_type(2)));
typedef __bf16 bf16x8 __attribute__((ext_vector_type(8)));
typedef float f32x2 __attribute__((ext_vector_type(2)));
typedef float f32x4 __attribute__((ext_vector_type(4)));
typedef unsigned u32x2 __attribute__((ext_vector_type(2)));
typedef unsigned u32x4 __attribute__((ext_vector_type(4)));
typedef unsigned u32x8 __attribute__((ext_vector_type(8)));
#define LDS_AS __attribute__((address_space(3)))

#define X_POLICY ""
#define S_POLICY " nt"
#define LPRIO 2
#define OUT_WARM 0
#define NE 3
#define GB_PIECE 8
#define GQ_POS 4
#define ST_LIST 19, 20, 21, 22, 23, 24, 25
#define STAMPS 0

namespace {
constexpr int D = 576;
constexpr int ROWB = D * 2;
constexpr int SLOT = 24576;
constexpr int XRES = 3 * SLOT;
constexpr int GBB = 6 * SLOT;
constexpr int FLAG = GBB + 8192;
constexpr int SINK = FLAG + 1024;
constexpr int LDS_BYTES = SINK + 1024;

constexpr int store_at(int u) {
  constexpr int at[7] = {ST_LIST};
  for (int k = 0; k < 7; ++k) if (at[k] == u) return k;
  return -1;
}
template <int V> struct IC { static constexpr int v = V; };
template <int I, int N, class F> __device__ __forceinline__ void static_for(F&& f) {
  if constexpr (I < N) { f(IC<I>{}); static_for<I + 1, N>(f); }
}
__device__ __forceinline__ u32x4 make_srd(unsigned lo, unsigned hi, unsigned bytes) { return u32x4{lo, hi & 0xffffu, bytes, 0x00020000u}; }
template <int N> __device__ __forceinline__ void wait_vm() { asm volatile("s_waitcnt vmcnt(%0)" ::"i"(N) : "memory"); }
template <int N> __device__ __forceinline__ void wait_lgkm() { asm volatile("s_waitcnt lgkmcnt(%0)" ::"i"(N) : "memory"); }
__device__ __forceinline__ void barrier() { asm volatile("s_barrier" ::: "memory"); }
__device__ __forceinline__ void fence() { __builtin_amdgcn_sched_barrier(0); }
__device__ __forceinline__ void dma16(unsigned lds_addr, unsigned voff, u32x4 srd, unsigned soff) {
  asm volatile("s_mov_b32 m0, %0\n\ts_nop 0\n\tbuffer_load_dwordx4 %1, %2, %3 offen lds" ::"s"(lds_addr), "v"(voff), "s"(srd), "s"(soff) : "memory");
}
__device__ __forceinline__ void dma3(unsigned lds_addr, unsigned voff, u32x4 srd, unsigned soff) {
  unsigned s1, s2;
  asm volatile("s_mov_b32 m0, %2\n\ts_add_u32 %0, %5, 0x9000\n\tbuffer_load_dwordx4 %3, %4, %5 offen lds\n\t"
               "s_add_u32 m0, m0, 0x1000\n\ts_add_u32 %1, %5, 0x12000\n\tbuffer_load_dwordx4 %3, %4, %0 offen lds\n\t"
               "s_add_u32 m0, m0, 0x1000\n\ts_nop 0\n\tbuffer_load_dwordx4 %3, %4, %1 offen lds"
               : "=&s"(s1), "=&s"(s2) : "s"(lds_addr), "v"(voff), "s"(srd), "s"(soff) : "memory", "scc");
}
__device__ __forceinline__ void touch(unsigned lds_addr, unsigned voff, u32x4 srd) {
  asm volatile("s_mov_b32 m0, %0\n\ts_nop 0\n\tbuffer_load_dword %1, %2, 0 offen lds" ::"s"(lds_addr), "v"(voff), "s"(srd) : "memory");
}
__device__ __forceinline__ void st16(u32x4 v, unsigned voff, u32x4 srd, unsigned soff) {
  asm volatile("buffer_store_dwordx4 %0, %1, %2, %3 offen" S_POLICY "\n\ts_nop 1" ::"v"(v), "v"(voff), "s"(srd), "s"(soff) : "memory");
}
template <int OFF> __device__ __forceinline__ void ldx(u32x4& dst, unsigned voff, u32x4 srd, unsigned soff) {
  asm volatile("buffer_load_dwordx4 %0, %1, %2, %3 offen offset:%4" X_POLICY : "=v"(dst) : "v"(voff), "s"(srd), "s"(soff), "i"(OFF));
}
__device__ __forceinline__ void ldg(u32x4& dst, unsigned voff, u32x4 srd) {
  asm volatile("buffer_load_dwordx4 %0, %1, %2, 0 offen" : "=v"(dst) : "v"(voff), "s"(srd));
}
template <int N> __device__ __forceinline__ void wait_vm_for(u32x4& v) { asm volatile("s_waitcnt vmcnt(%1)" : "+v"(v) : "i"(N)); }
__device__ __forceinline__ void ts_raw(unsigned long long& t) { asm volatile("s_memtime %0" : "=s"(t)); }
template <int I> __device__ __forceinline__ void ts_lane(unsigned& tsv, unsigned long long t) { asm volatile("" : "+s"(t)); asm volatile("v_writelane_b32 %0, %1, %2" : "+v"(tsv) : "s"((unsigned)t), "i"(I)); }
__device__ __forceinline__ void probe_now(unsigned long long& t) { asm volatile("s_memrealtime %0\n\ts_waitcnt lgkmcnt(0)" : "=s"(t) :: "memory"); }
template <int I> __device__ __forceinline__ void ts_put(unsigned& tsv) {
  unsigned long long t_; asm volatile("s_memtime %0\n\ts_waitcnt lgkmcnt(0)" : "=s"(t_) :: "memory");
  asm volatile("v_writelane_b32 %0, %1, %2" : "+v"(tsv) : "s"((unsigned)t_), "i"(I));
}
template <int CTRL> __device__ __forceinline__ float dpp_add(float v) {
  return v + __builtin_bit_cast(float, __builtin_amdgcn_update_dpp(0, __builtin_bit_cast(int, v), CTRL, 0xf, 0xf, true));
}
__device__ __forceinline__ float oct_sum(float v) {
  v = dpp_add<0xB1>(v); v = dpp_add<0x4E>(v); v = dpp_add<0x141>(v);
  return v;
}
__device__ __forceinline__ unsigned pack2(f32x2 v) { return __builtin_bit_cast(unsigned, __builtin_convertvector(v, bf16x2)); }
__device__ __forceinline__ f32x2 unpack2(unsigned u) { return f32x2{__uint_as_float(u << 16), __uint_as_float(u & 0xffff0000u)}; }
template <int N> __device__ __forceinline__ void piece_stats(const u32x4& x, float& s, float& q, f32x2 (&y)[4]) {
  float a0, a1, a2, a3, a4, a5, a6, a7;
  const unsigned x0 = x[0], x1 = x[1], x2 = x[2], x3 = x[3];
  asm volatile(
      "s_waitcnt vmcnt(%16)\n\t"
      "v_dot2c_f32_bf16 %0, %14, %10\n\tv_dot2c_f32_bf16 %1, %10, %10\n\t"
      "v_dot2c_f32_bf16 %0, %14, %11\n\tv_dot2c_f32_bf16 %1, %11, %11\n\t"
      "v_dot2c_f32_bf16 %0, %14, %12\n\tv_dot2c_f32_bf16 %1, %12, %12\n\t"
      "v_dot2c_f32_bf16 %0, %14, %13\n\tv_dot2c_f32_bf16 %1, %13, %13\n\t"
      "v_lshlrev_b32 %2, 16, %10\n\tv_and_b32 %3, %15, %10\n\t"
      "v_lshlrev_b32 %4, 16, %11\n\tv_and_b32 %5, %15, %11\n\t"
      "v_lshlrev_b32 %6, 16, %12\n\tv_and_b32 %7, %15, %12\n\t"
      "v_lshlrev_b32 %8, 16, %13\n\tv_and_b32 %9, %15, %13"
      : "+v"(s), "+v"(q), "=&v"(a0), "=&v"(a1), "=&v"(a2), "=&v"(a3), "=&v"(a4), "=&v"(a5), "=&v"(a6), "=&v"(a7)
      : "v"(x0), "v"(x1), "v"(x2), "v"(x3), "s"(0x3f803f80u), "s"(0xffff0000u), "i"(N));
  y[0] = f32x2{a0, a1}; y[1] = f32x2{a2, a3}; y[2] = f32x2{a4, a5}; y[3] = f32x2{a6, a7};
}
__device__ __forceinline__ void count_in(unsigned lds_addr) { asm volatile("s_mov_b64 exec, 1\n\tds_add_u32 %0, %1\n\ts_mov_b64 exec, -1" ::"v"(lds_addr), "v"(1u) : "memory"); }
__device__ __forceinline__ void keep2(unsigned a, unsigned b) { asm volatile("s_waitcnt lgkmcnt(0)" ::"s"(a), "s"(b) : "memory"); }
__device__ __forceinline__ void pinf(float& v) { asm volatile("" : "+v"(v)); }
__device__ __forceinline__ LDS_AS char* opaque(LDS_AS char* p) { asm volatile("" : "+v"(p)); return p; }
template <int MASK> __device__ __forceinline__ float sel8(float v) {
  const int i = __builtin_bit_cast(int, v);
  return __builtin_bit_cast(float, __builtin_amdgcn_update_dpp(i, i, 0x128, 0xf, MASK, false));
}
__device__ __forceinline__ float fold8(float a, float b) {
  float t;
  asm("s_nop 1\n\tv_add_f32_dpp %0, %1, %1 row_ror:8 row_mask:0xf bank_mask:0x3\n\tv_add_f32_dpp %0, %2, %2 row_ror:8 row_mask:0xf bank_mask:0xc" : "=&v"(t) : "v"(a), "v"(b));
  return t;
}

#define TS(i) do { if constexpr ((i) == PROBE) probe_now(tprobe); } while (0)
#define TS2(i) do {} while (0)

__global__ __launch_bounds__(512, 2) void ln_linear_kernel(const uint16_t* __restrict__ x, const uint16_t* __restrict__ g,
                                                           const uint16_t* __restrict__ b, const uint16_t* __restrict__ w,
                                                           uint16_t* __restrict__ out
) {
  extern __shared__ __attribute__((aligned(1024))) char smem[];
  LDS_AS char* lds = (LDS_AS char*)smem;
  u32x8 ka;
  u32x2 ko;
  asm volatile("s_load_dwordx8 %0, %2, 0x0\n\ts_load_dwordx2 %1, %2, 0x20" : "=&s"(ka), "=&s"(ko) : "s"(__builtin_amdgcn_kernarg_segment_ptr()));
  unsigned long long tprobe = 0;
  if constexpr (PROBE == 0) probe_now(tprobe);
  const int tid = threadIdx.x, lane = tid & 63;
  const int wv = __builtin_amdgcn_readfirstlane(tid >> 6);
  const int bid = blockIdx.x, jb = bid >> 3, h = jb & 1;
  const int row0 = (bid & 7) * 1024 + (jb >> 1) * 64;
  const int wi = wv & 3;
  const int r7 = lane >> 3, c7 = lane & 7, c15 = lane & 15, rq = lane >> 4;
  const unsigned ldsbase = (unsigned)(uintptr_t)lds;
  const unsigned rowoff = (unsigned)r7 * ROWB;

  unsigned pf0 = 0, pf1 = 0, pf2 = 0;
  if (wv >= 4) *(LDS_AS unsigned*)(lds + FLAG) = 0u;
  asm volatile("s_waitcnt lgkmcnt(0)" : "+s"(ka), "+s"(ko)::"memory");
  if (wv >= 4) {
    unsigned long long pc;
    unsigned len;
    asm volatile("s_getpc_b64 %0\n.Lpf_here:\n\ts_mov_b32 %1, .Lpf_end-.Lpf_here" : "=s"(pc), "=s"(len));
    const unsigned o = min((unsigned)(jb + wi * 32) * 128u, len - 128u);
    asm volatile("s_load_dword %0, %2, %3\n\ts_load_dword %1, %2, %4" : "=&s"(pf0), "=&s"(pf1) : "s"(pc), "s"(o), "s"(o + 64u));
    barrier();
  }
  const u32x4 srdx = make_srd(ka[0], ka[1], 8192u * ROWB), srdw = make_srd(ka[6], ka[7], (unsigned)D * ROWB), srdo = make_srd(ko[0], ko[1], 8192u * ROWB);
  u32x4 xr[9], gq;
  {
    const u32x4 srdt = make_srd(wv < 2 ? ka[2] : ka[4], wv < 2 ? ka[3] : ka[5], wv < 4 ? (unsigned)ROWB : 0u);
    const unsigned so = (unsigned)(row0 + wv * 8) * ROWB, xvm = (unsigned)(rq * 2 * ROWB + c15 * 16);
    ldx<1024>(xr[8], rowoff + (unsigned)c7 * 16, srdx, so);
    static_for<0, 8>([&](auto jc) {
      constexpr int j = decltype(jc)::v;
      if constexpr (j == GQ_POS) ldg(gq, (unsigned)(tid & 127) * 16u, srdt);
      ldx<(j >> 2) * ROWB + (j & 3) * 256>(xr[j], xvm, srdx, so);
    });
    touch(ldsbase + SINK, (wv >= 4 && wv < 7 && lane < 18) ? ((unsigned)jb * 18u + (unsigned)lane) * ROWB + (wv == 4 ? 512u : wv == 5 ? 0u : 640u) : 0xfffffff0u, srdw);
  }
  if (wv < 4) barrier();
  TS(13);

  {
    f32x4 gv[5][2], bv[5][2];
    f32x2 y[9][4];
    LDS_AS char* aw[3] = {opaque(lds + XRES + wv * 1024 + (c15 >> 3) * 8192 + (2 * rq) * 128 + ((c7 ^ (2 * rq)) * 16)),
                          opaque(lds + XRES + wv * 1024 + (c15 >> 3) * 8192 + (2 * rq + 1) * 128 + ((c7 ^ (2 * rq + 1)) * 16)),
                          opaque(lds + XRES + 8 * 8192 + wv * 1024 + r7 * 128 + ((c7 ^ r7) * 16))};
    LDS_AS char* gr = opaque(lds + GBB + c15 * 16);
    LDS_AS char* grt = opaque(lds + GBB + 2048 + c7 * 16);
    float sm[3] = {0.f, 0.f, 0.f}, sq[3] = {0.f, 0.f, 0.f};
#pragma unroll
    for (int i = 0; i < 3; ++i) { pinf(sm[i]); pinf(sq[i]); }
    fence();
    static_for<0, 9>([&](auto jc) {
      constexpr int jj = decltype(jc)::v, j = jj == 0 ? 8 : jj - 1;
      piece_stats<9 - jj + (jj <= GQ_POS ? 1 : 0)>(xr[j], sm[j >> 2], sq[j >> 2], y[j]);
      if constexpr (jj == 0) TS(14);
      if constexpr (jj == GQ_POS) {
        wait_vm_for<9 - jj>(gq);
        if (wv < 4) {
          const unsigned g0 = gq[0], g1 = gq[1], g2 = gq[2], g3 = gq[3];
          const f32x2 a0 = unpack2(g0), a1 = unpack2(g1), a2 = unpack2(g2), a3 = unpack2(g3);
          *(LDS_AS f32x4*)(lds + GBB + wi * 2048 + rq * 512 + c15 * 16) = f32x4{a0[0], a0[1], a1[0], a1[1]};
          *(LDS_AS f32x4*)(lds + GBB + wi * 2048 + rq * 512 + c15 * 16 + 256) = f32x4{a2[0], a2[1], a3[0], a3[1]};
          count_in(ldsbase + FLAG);
        }
        fence();
      }
      if constexpr (jj == GB_PIECE) {
        while (__builtin_amdgcn_readfirstlane(*(volatile LDS_AS unsigned*)(lds + FLAG)) < 4u) __builtin_amdgcn_s_sleep(1);
        keep2(pf0, pf1); keep2(pf2, pf2);
      }
      if constexpr (jj >= GB_PIECE) {
#pragma unroll
        for (int kk = jj - GB_PIECE; kk < (jj == 8 ? 5 : jj - GB_PIECE + 1); ++kk) {
          if (kk < 5) {
            const int k = kk == 0 ? 4 : kk - 1;
            LDS_AS char* p = k < 4 ? gr + k * 512 : grt;
#pragma unroll
            for (int e = 0; e < 2; ++e) { gv[k][e] = *(LDS_AS f32x4*)(p + e * 256); bv[k][e] = *(LDS_AS f32x4*)(p + 4096 + e * 256); }
          }
        }
        fence();
      }
    });
    fence();
    TS(3);
    const float mean = oct_sum(fold8(sm[0], sm[1]) + sm[2]) * (1.0f / D);
    const float var = fmaxf(oct_sum(fold8(sq[0], sq[1]) + sq[2]) * (1.0f / D) - mean * mean, 0.f);
    float rs[3], nm[3];
    rs[2] = __builtin_amdgcn_rsqf(var + 1e-5f);
    nm[2] = -mean * rs[2];
    rs[0] = sel8<0xc>(rs[2]); rs[1] = sel8<0x3>(rs[2]);
    nm[0] = sel8<0xc>(nm[2]); nm[1] = sel8<0x3>(nm[2]);
#pragma unroll
    for (int jj = 0; jj < 9; ++jj) {
      const int j = jj == 0 ? 8 : jj - 1, G = j >> 2, k = j == 8 ? 4 : (j & 3);
#pragma unroll
      for (int e = 0; e < 4; ++e) y[j][e] = __builtin_elementwise_fma(y[j][e], f32x2{rs[G], rs[G]}, f32x2{nm[G], nm[G]});
      fence();
#pragma unroll
      for (int e = 0; e < 4; ++e) {
        const f32x4 gg = gv[k][e >> 1], bb = bv[k][e >> 1];
        y[j][e] = __builtin_elementwise_fma(y[j][e], (e & 1) ? f32x2{gg[2], gg[3]} : f32x2{gg[0], gg[1]}, (e & 1) ? f32x2{bb[2], bb[3]} : f32x2{bb[0], bb[1]});
      }
      fence();
      *(LDS_AS u32x4*)(aw[G] + (j == 8 ? 0 : (j & 3) * 16384)) = u32x4{pack2(y[j][0]), pack2(y[j][1]), pack2(y[j][2]), pack2(y[j][3])};
      fence();
    }
  }
  TS(4);

  const unsigned wvoff = rowoff + (unsigned)(((((c7 >> 1) ^ (((r7 >> 1) & 1) | ((tid >> 5) & 2))) << 1) | (c7 & 1)) * 16);
  const unsigned wbase = (unsigned)h * 640u + (unsigned)wi * 8u * ROWB;
  auto request = [&](unsigned hslot, unsigned so) {
    dma3(ldsbase + hslot * (SLOT / 2) + (unsigned)wi * 1024u, wvoff, srdw, so);
  };

  if (wv >= 4) {
    wait_lgkm<0>();
    barrier();
    TS(6);
    __builtin_amdgcn_s_setprio(LPRIO);
    auto req = [&](auto cc) {
      constexpr int c = decltype(cc)::v;
      unsigned wb = wbase, lb = ldsbase + (unsigned)wi * 1024u;
      asm volatile("" : "+s"(wb), "+s"(lb));
      dma3(lb + ((c + 3) % 12) * (SLOT / 2), wvoff, srdw, wb + (c / 6) * 128u + (c % 6) * 96u * ROWB);
    };
    static_for<NE, 3>(req);
    barrier(); TS2(18); static_for<3, 5>(req);
    barrier(); TS2(21); static_for<5, 6>(req);
    wait_vm<12>();
    barrier(); TS2(24); static_for<6, 8>(req);
    static_for<0, 11>([&](auto qc) {
      constexpr int q = decltype(qc)::v;
      wait_vm<(q < 9 ? 12 : q == 9 ? 6 : 0)>();
      barrier();
      TS2(27 + 3 * q);
      if constexpr (q < 8) static_for<2 * q + 8, 2 * q + 10>(req);
    });
    TS(8);
  } else {
    {
      const unsigned hv = (unsigned)(lane >> 2) * ROWB + (unsigned)(((lane & 3) ^ ((lane >> 4) & 2)) * 16);
      const unsigned hs = 512u + (unsigned)h * 64u + (unsigned)wi * 16u * ROWB;
#pragma unroll
      for (int i = 0; i < 9; ++i) dma16(ldsbase + (i * 4 + wi) * 1024, hv, srdw, hs + (unsigned)(i * 64) * ROWB);
#pragma unroll
      for (int c = 0; c < NE; ++c) request(c + 3, wbase + c * 96u * ROWB);
      const unsigned li = (unsigned)tid, kk = (li * 43u) >> 8, ln = li - kk * 6u;
      touch(ldsbase + SINK, li < 108u ? ((unsigned)jb * 18u + kk) * ROWB + (ln + 1u + (ln >= 3u) * 2u) * 128u : 0xfffffff0u, srdw);
    }
    const int m = wi >> 1, n = wi & 1;
    const int fr = lane & 15, fg = lane >> 4;
    bf16x8 xa[2][18];
    LDS_AS char* ard[2][2];
#pragma unroll
    for (int e = 0; e < 2; ++e) {
      const unsigned o = (m * 2 + (fr >> 3)) * 1024 + (fr & 7) * 128 + (((4 * e + fg) ^ (fr & 7)) * 16);
      ard[e][0] = opaque(lds + XRES + o);
      ard[e][1] = opaque(lds + XRES + 5 * 8192 + o);
    }
    LDS_AS char* wrd[2][3];
#pragma unroll
    for (int t = 0; t < 2; ++t)
#pragma unroll
      for (int z = 0; z < 3; ++z)
        wrd[t][z] = opaque(lds + z * 2 * SLOT + (8 * fg + (fr >> 2)) * 128 + (((2 * n + t) ^ (((fr >> 3) & 1) | ((fg & 1) << 1))) * 32) + (fr & 3) * 8);
    LDS_AS char* wh = opaque(lds + (8 * fg + (fr >> 2)) * 64 + ((n ^ (fg & 1)) * 32) + (fr & 3) * 8);
    const unsigned ovoff = (unsigned)fr * ROWB + (unsigned)(2 * (fg & 1) + (fg >> 1)) * 16u;
    const unsigned orow = (unsigned)(row0 + m * 16) * ROWB;
    const unsigned osoff = orow + (unsigned)h * 640u + (unsigned)n * 64u;
    wait_lgkm<0>();
    wait_vm<6 + NE * 3 + 1>();
    TS(7);
    barrier();
    TS(6);

    bf16x8 wf[2][6];
    f32x4 acc[2][2], fin[2][2];
    u32x4 po[4][2];
    auto tr_frag = [&](LDS_AS char* p, int step) {
      const s16x4 lo = __builtin_amdgcn_ds_read_tr16_b64_v4i16((LDS_AS s16x4*)p);
      const s16x4 hi = __builtin_amdgcn_ds_read_tr16_b64_v4i16((LDS_AS s16x4*)(p + step));
      return __builtin_bit_cast(bf16x8, __builtin_shufflevector(lo, hi, 0, 1, 2, 3, 4, 5, 6, 7));
    };
    auto rd = [&](auto uc) {
      constexpr int u = decltype(uc)::v, hsl = u % 12;
      if constexpr (u >= 3) {
#pragma unroll
        for (int s = 0; s < 3; ++s)
#pragma unroll
          for (int t = 0; t < 2; ++t) wf[u & 1][s * 2 + t] = tr_frag(wrd[t][hsl >> 2] + (hsl & 3) * (SLOT / 2) + s * 4096, 512);
      } else {
#pragma unroll
        for (int s = 0; s < 6; ++s) {
          const int ks = u * 6 + s;
          wf[u & 1][s] = tr_frag(wh + ks * 2048, 256);
#pragma unroll
          for (int i = 0; i < 2; ++i) xa[i][ks] = *(LDS_AS bf16x8*)(ard[ks & 1][ks >= 10] + ((ks >> 1) % 5) * 8192 + i * 4096);
        }
      }
    };
    auto mm = [&](auto uc) {
      constexpr int u = decltype(uc)::v;
#pragma unroll
      for (int f = 0; f < 6; ++f) {
        const int ks = u >= 3 ? ((u - 3) % 6) * 3 + f / 2 : u * 6 + f, t = f & 1;
        const bool first = u >= 3 ? ks == 0 : ks < 2;
#pragma unroll
        for (int i = 0; i < 2; ++i) acc[i][t] = __builtin_amdgcn_mfma_f32_16x16x32_bf16(wf[u & 1][f], xa[i][ks], first ? f32x4{0.f, 0.f, 0.f, 0.f} : acc[i][t], 0, 0, 0);
      }
    };
    auto pack = [&](const f32x4& a, const f32x4& c) {
      const auto lo = __builtin_amdgcn_permlane16_swap(pack2(f32x2{a[0], a[1]}), pack2(f32x2{c[0], c[1]}), false, false);
      const auto hi = __builtin_amdgcn_permlane16_swap(pack2(f32x2{a[2], a[3]}), pack2(f32x2{c[2], c[3]}), false, false);
      return u32x4{(unsigned)lo[0], (unsigned)hi[0], (unsigned)lo[1], (unsigned)hi[1]};
    };
    const unsigned hvoff = (unsigned)(fr + (fg & 1) * 32) * ROWB + (unsigned)(fg >> 1) * 16u;
    u32x4 ph;
    auto store = [&](int k) {
      if (k == 0) st16(ph, hvoff, srdo, orow + 512u + (unsigned)h * 64u + (unsigned)n * 32u);
      else st16(po[(k - 1) / 2][(k - 1) & 1], ovoff, srdo, osoff + ((k - 1) / 2) * 128u + ((k - 1) & 1) * 32u * ROWB);
    };
    rd(IC<0>{});
    fence();
    static_for<0, 27>([&](auto uc) {
      constexpr int u = decltype(uc)::v, v = u - 3;
      constexpr bool pk = v >= 6 && v % 6 < 2;
      if constexpr (u < 3) {
        wait_vm<(u == 0 ? 3 + NE * 3 + 1 : u == 1 ? NE * 3 + 1 : (NE > 2 ? 3 : 0) + 1)>();
        wait_lgkm<0>();
        barrier();
        fence();
      } else if constexpr ((v & 1) && u < 26) {
        if constexpr (v == 1) wait_vm<1>();
        barrier();
        fence();
      }
      if constexpr (u < 26) rd(IC<u + 1>{});
      if constexpr (pk) po[v / 6 - 1][v % 6] = pack(fin[v % 6][0], fin[v % 6][1]);
      if constexpr (u == 3) ph = pack(fin[0][0] + fin[0][1], fin[1][0] + fin[1][1]);
      mm(uc);
      if constexpr (u < 26) {
        static_for<0, 12>([&](auto ic) {
          constexpr int i = decltype(ic)::v;
          __builtin_amdgcn_sched_group_barrier(0x008, 1, 0);
          if constexpr (u < 2) { if constexpr (i < 8) __builtin_amdgcn_sched_group_barrier(0x100, 3, 0); }
          else __builtin_amdgcn_sched_group_barrier(0x100, 1, 0);
          if constexpr ((pk && i >= 2 && i < 8) || (u == 3 && i >= 2)) __builtin_amdgcn_sched_group_barrier(0x002, 1, 0);
        });
      }
      fence();
      if constexpr (store_at(u) >= 0) { store(store_at(u)); fence(); }
      if constexpr (u == 2 || (v >= 0 && v % 6 == 5)) {
#pragma unroll
        for (int i = 0; i < 2; ++i) { fin[i][0] = acc[i][0]; fin[i][1] = acc[i][1]; }
      }
    });
    po[3][0] = pack(fin[0][0], fin[0][1]);
    po[3][1] = pack(fin[1][0], fin[1][1]);
    store(7);
    store(8);
    TS(9);
  }
  if constexpr (PROBE == 11) { asm volatile("s_waitcnt vmcnt(0)" ::: "memory"); probe_now(tprobe); }
  if (wv == PROBE_WAVE) { while (__builtin_amdgcn_s_memrealtime() < tprobe + 4000ull) __builtin_amdgcn_s_sleep(1); }
  asm volatile(".Lpf_end:");
}
}

extern "C" void kernel_launch(void* const* d_in, const int* in_sizes, int n_in, void* d_out, int out_size, void* d_ws,
                              size_t ws_size, hipStream_t stream) {
  (void)in_sizes; (void)n_in; (void)out_size; (void)d_ws; (void)ws_size;
  (void)hipFuncSetAttribute((const void*)ln_linear_kernel, hipFuncAttributeMaxDynamicSharedMemorySize, LDS_BYTES);
  ln_linear_kernel<<<256, 512, LDS_BYTES, stream>>>((const uint16_t*)d_in[0], (const uint16_t*)d_in[1], (const uint16_t*)d_in[2],
                                                    (const uint16_t*)d_in[3], (uint16_t*)d_out
  );
}
```

```cpp
#include <hip/hip_runtime.h>
#include <stdint.h>

typedef short s16x4 __attribute__((ext_vector_type(4)));
typedef __bf16 bf16x2 __attribute__((ext_vector_type(2)));
typedef __bf16 bf16x8 __attribute__((ext_vector_type(8)));
typedef float f32x2 __attribute__((ext_vector_type(2)));
typedef float f32x4 __attribute__((ext_vector_type(4)));
typedef unsigned u32x2 __attribute__((ext_vector_type(2)));
typedef unsigned u32x4 __attribute__((ext_vector_type(4)));
typedef unsigned u32x8 __attribute__((ext_vector_type(8)));
#define LDS_AS __attribute__((address_space(3)))

#define X_POLICY ""
#define S_POLICY " nt"
#define LPRIO 2
#define NE 3
#define GB_PIECE 8
#define GQ_POS 4
#define ST_LIST 19, 20, 21, 22, 23, 24, 25
#define STAMPS 0

namespace {
constexpr int D = 576;
constexpr int ROWB = D * 2;
constexpr int SLOT = 24576;
constexpr int XRES = 3 * SLOT;
constexpr int GBB = 6 * SLOT;
constexpr int FLAG = GBB + 8192;
constexpr int SINK = FLAG + 1024;
constexpr int LDS_BYTES = SINK + 1024;

constexpr int store_at(int u) {
  constexpr int at[7] = {ST_LIST};
  for (int k = 0; k < 7; ++k) if (at[k] == u) return k;
  return -1;
}
template <int V> struct IC { static constexpr int v = V; };
template <int I, int N, class F> __device__ __forceinline__ void static_for(F&& f) {
  if constexpr (I < N) { f(IC<I>{}); static_for<I + 1, N>(f); }
}
__device__ __forceinline__ u32x4 make_srd(unsigned lo, unsigned hi, unsigned bytes) { return u32x4{lo, hi & 0xffffu, bytes, 0x00020000u}; }
template <int N> __device__ __forceinline__ void wait_vm() { asm volatile("s_waitcnt vmcnt(%0)" ::"i"(N) : "memory"); }
template <int N> __device__ __forceinline__ void wait_lgkm() { asm volatile("s_waitcnt lgkmcnt(%0)" ::"i"(N) : "memory"); }
__device__ __forceinline__ void barrier() { asm volatile("s_barrier" ::: "memory"); }
__device__ __forceinline__ void fence() { __builtin_amdgcn_sched_barrier(0); }
__device__ __forceinline__ void dma16(unsigned lds_addr, unsigned voff, u32x4 srd, unsigned soff) {
  asm volatile("s_mov_b32 m0, %0\n\ts_nop 0\n\tbuffer_load_dwordx4 %1, %2, %3 offen lds" ::"s"(lds_addr), "v"(voff), "s"(srd), "s"(soff) : "memory");
}
__device__ __forceinline__ void dma3(unsigned lds_addr, unsigned voff, u32x4 srd, unsigned soff) {
  unsigned s1, s2;
  asm volatile("s_mov_b32 m0, %2\n\ts_add_u32 %0, %5, 0x9000\n\tbuffer_load_dwordx4 %3, %4, %5 offen lds\n\t"
               "s_add_u32 m0, m0, 0x1000\n\ts_add_u32 %1, %5, 0x12000\n\tbuffer_load_dwordx4 %3, %4, %0 offen lds\n\t"
               "s_add_u32 m0, m0, 0x1000\n\ts_nop 0\n\tbuffer_load_dwordx4 %3, %4, %1 offen lds"
               : "=&s"(s1), "=&s"(s2) : "s"(lds_addr), "v"(voff), "s"(srd), "s"(soff) : "memory", "scc");
}
__device__ __forceinline__ void touch(unsigned lds_addr, unsigned voff, u32x4 srd) {
  asm volatile("s_mov_b32 m0, %0\n\ts_nop 0\n\tbuffer_load_dword %1, %2, 0 offen lds" ::"s"(lds_addr), "v"(voff), "s"(srd) : "memory");
}
__device__ __forceinline__ void st16(u32x4 v, unsigned voff, u32x4 srd, unsigned soff) {
  asm volatile("buffer_store_dwordx4 %0, %1, %2, %3 offen" S_POLICY "\n\ts_nop 1" ::"v"(v), "v"(voff), "s"(srd), "s"(soff) : "memory");
}
template <int OFF> __device__ __forceinline__ void ldx(u32x4& dst, unsigned voff, unsigned long long base) {
  asm volatile("global_load_dwordx4 %0, %1, %2 offset:%3" X_POLICY : "=v"(dst) : "v"(voff), "s"(base), "i"(OFF));
}
__device__ __forceinline__ void ldg(u32x4& dst, unsigned voff, u32x4 srd) {
  asm volatile("buffer_load_dwordx4 %0, %1, %2, 0 offen" : "=v"(dst) : "v"(voff), "s"(srd));
}
template <int N> __device__ __forceinline__ void wait_vm_for(u32x4& v) { asm volatile("s_waitcnt vmcnt(%1)" : "+v"(v) : "i"(N)); }
__device__ __forceinline__ void ts_raw(unsigned long long& t) { asm volatile("s_memtime %0" : "=s"(t)); }
template <int I> __device__ __forceinline__ void ts_lane(unsigned& tsv, unsigned long long t) { asm volatile("" : "+s"(t)); asm volatile("v_writelane_b32 %0, %1, %2" : "+v"(tsv) : "s"((unsigned)t), "i"(I)); }
template <int I> __device__ __forceinline__ void ts_put(unsigned& tsv) {
  unsigned long long t_; asm volatile("s_memtime %0\n\ts_waitcnt lgkmcnt(0)" : "=s"(t_) :: "memory");
  asm volatile("v_writelane_b32 %0, %1, %2" : "+v"(tsv) : "s"((unsigned)t_), "i"(I));
}
template <int CTRL> __device__ __forceinline__ float dpp_add(float v) {
  return v + __builtin_bit_cast(float, __builtin_amdgcn_update_dpp(0, __builtin_bit_cast(int, v), CTRL, 0xf, 0xf, true));
}
__device__ __forceinline__ float oct_sum(float v) {
  v = dpp_add<0xB1>(v); v = dpp_add<0x4E>(v); v = dpp_add<0x141>(v);
  return v;
}
__device__ __forceinline__ unsigned pack2(f32x2 v) { return __builtin_bit_cast(unsigned, __builtin_convertvector(v, bf16x2)); }
__device__ __forceinline__ f32x2 unpack2(unsigned u) { return f32x2{__uint_as_float(u << 16), __uint_as_float(u & 0xffff0000u)}; }
template <int N> __device__ __forceinline__ void piece_stats(const u32x4& x, float& s, float& q, f32x2 (&y)[4]) {
  float a0, a1, a2, a3, a4, a5, a6, a7;
  const unsigned x0 = x[0], x1 = x[1], x2 = x[2], x3 = x[3];
  asm volatile(
      "s_waitcnt vmcnt(%16)\n\t"
      "v_dot2c_f32_bf16 %0, %14, %10\n\tv_dot2c_f32_bf16 %1, %10, %10\n\t"
      "v_dot2c_f32_bf16 %0, %14, %11\n\tv_dot2c_f32_bf16 %1, %11, %11\n\t"
      "v_dot2c_f32_bf16 %0, %14, %12\n\tv_dot2c_f32_bf16 %1, %12, %12\n\t"
      "v_dot2c_f32_bf16 %0, %14, %13\n\tv_dot2c_f32_bf16 %1, %13, %13\n\t"
      "v_lshlrev_b32 %2, 16, %10\n\tv_and_b32 %3, %15, %10\n\t"
      "v_lshlrev_b32 %4, 16, %11\n\tv_and_b32 %5, %15, %11\n\t"
      "v_lshlrev_b32 %6, 16, %12\n\tv_and_b32 %7, %15, %12\n\t"
      "v_lshlrev_b32 %8, 16, %13\n\tv_and_b32 %9, %15, %13"
      : "+v"(s), "+v"(q), "=&v"(a0), "=&v"(a1), "=&v"(a2), "=&v"(a3), "=&v"(a4), "=&v"(a5), "=&v"(a6), "=&v"(a7)
      : "v"(x0), "v"(x1), "v"(x2), "v"(x3), "s"(0x3f803f80u), "s"(0xffff0000u), "i"(N));
  y[0] = f32x2{a0, a1}; y[1] = f32x2{a2, a3}; y[2] = f32x2{a4, a5}; y[3] = f32x2{a6, a7};
}
__device__ __forceinline__ void count_in(unsigned lds_addr) { asm volatile("s_mov_b64 exec, 1\n\tds_add_u32 %0, %1\n\ts_mov_b64 exec, -1" ::"v"(lds_addr), "v"(1u) : "memory"); }
__device__ __forceinline__ void keep2(unsigned a, unsigned b) { asm volatile("s_waitcnt lgkmcnt(0)" ::"s"(a), "s"(b) : "memory"); }
__device__ __forceinline__ void pinf(float& v) { asm volatile("" : "+v"(v)); }
__device__ __forceinline__ LDS_AS char* opaque(LDS_AS char* p) { asm volatile("" : "+v"(p)); return p; }
template <int MASK> __device__ __forceinline__ float sel8(float v) {
  const int i = __builtin_bit_cast(int, v);
  return __builtin_bit_cast(float, __builtin_amdgcn_update_dpp(i, i, 0x128, 0xf, MASK, false));
}
__device__ __forceinline__ float fold8(float a, float b) {
  float t;
  asm("s_nop 1\n\tv_add_f32_dpp %0, %1, %1 row_ror:8 row_mask:0xf bank_mask:0x3\n\tv_add_f32_dpp %0, %2, %2 row_ror:8 row_mask:0xf bank_mask:0xc" : "=&v"(t) : "v"(a), "v"(b));
  return t;
}

#define TS(i) do {} while (0)
#define TS2(i) do {} while (0)

__global__ __launch_bounds__(512, 2) void ln_linear_kernel(const uint16_t* __restrict__ x, const uint16_t* __restrict__ g,
                                                           const uint16_t* __restrict__ b, const uint16_t* __restrict__ w,
                                                           uint16_t* __restrict__ out
) {
  extern __shared__ __attribute__((aligned(1024))) char smem[];
  LDS_AS char* lds = (LDS_AS char*)smem;
  unsigned long long kx;
  u32x8 ka;
  asm volatile("s_load_dwordx2 %0, %2, 0x0\n\ts_load_dwordx8 %1, %2, 0x8" : "=&s"(kx), "=&s"(ka) : "s"(__builtin_amdgcn_kernarg_segment_ptr()));
  const int tid = threadIdx.x, lane = tid & 63;
  const unsigned bid = blockIdx.x;
  const unsigned row0 = (bid & 7) * 1024 + (bid >> 4) * 64;
  unsigned xvt = (unsigned)(tid >> 3) * ROWB + (unsigned)(tid & 7) * 16, xvm = (unsigned)(tid >> 4) * 2 * ROWB + (unsigned)(tid & 15) * 16;
  unsigned xo = row0 * ROWB;
  const unsigned t0 = __builtin_amdgcn_readfirstlane(tid);
  asm volatile("" : "+v"(xvt), "+v"(xvm));
  asm volatile("" : "+s"(xo));
  if (t0 >= 256) {
    *(LDS_AS unsigned*)(lds + FLAG) = 0u;
    wait_lgkm<0>();
    barrier();
  }
  asm volatile("s_waitcnt lgkmcnt(0)" : "+s"(kx), "+s"(ka));
  u32x4 xr[9], gq;
  {
    const unsigned long long xb = kx + xo;
    ldx<1024>(xr[8], xvt, xb);
    static_for<0, GQ_POS>([&](auto jc) { constexpr int j = decltype(jc)::v; ldx<(j >> 2) * ROWB + (j & 3) * 256>(xr[j], xvm, xb); });
    fence();
    ldg(gq, (unsigned)(tid & 127) * 16u, make_srd(t0 < 128 ? ka[0] : ka[2], t0 < 128 ? ka[1] : ka[3], t0 < 256 ? (unsigned)ROWB : 0u));
    static_for<GQ_POS, 8>([&](auto jc) { constexpr int j = decltype(jc)::v; ldx<(j >> 2) * ROWB + (j & 3) * 256>(xr[j], xvm, xb); });
    fence();
  }
  const int wv = t0 >> 6, wi = wv & 3;
  const unsigned jb = bid >> 3, h = jb & 1;
  const int r7 = lane >> 3, c7 = lane & 7, c15 = lane & 15, rq = lane >> 4;
  const unsigned ldsbase = (unsigned)(uintptr_t)lds;
  const unsigned rowoff = (unsigned)r7 * ROWB;
  unsigned pf0 = 0, pf1 = 0;
  if (wv < 4) {
    unsigned long long pc;
    unsigned len;
    asm volatile("s_getpc_b64 %0\n.Lpf_here:\n\ts_mov_b32 %1, .Lpf_end-.Lpf_here" : "=s"(pc), "=s"(len));
    const unsigned o = min((jb + wi * 32) * 128u, len - 128u);
    asm volatile("s_load_dword %0, %2, %3\n\ts_load_dword %1, %2, %4" : "=&s"(pf0), "=&s"(pf1) : "s"(pc), "s"(o), "s"(o + 64u));
    barrier();
  }
  const u32x4 srdw = make_srd(ka[4], ka[5], (unsigned)D * ROWB), srdo = make_srd(ka[6], ka[7], 8192u * ROWB);
  touch(ldsbase + SINK, (wv >= 4 && wv < 7 && lane < 18) ? (jb * 18u + (unsigned)lane) * ROWB + (wv == 4 ? 512u : wv == 5 ? 0u : 640u) : 0xfffffff0u, srdw);
  TS(13);

  {
    f32x4 gv[5][2], bv[5][2];
    f32x2 y[9][4];
    LDS_AS char* aw[3] = {opaque(lds + XRES + wv * 1024 + (c15 >> 3) * 8192 + (2 * rq) * 128 + ((c7 ^ (2 * rq)) * 16)),
                          opaque(lds + XRES + wv * 1024 + (c15 >> 3) * 8192 + (2 * rq + 1) * 128 + ((c7 ^ (2 * rq + 1)) * 16)),
                          opaque(lds + XRES + 8 * 8192 + wv * 1024 + r7 * 128 + ((c7 ^ r7) * 16))};
    LDS_AS char* gr = opaque(lds + GBB + c15 * 16);
    LDS_AS char* grt = opaque(lds + GBB + 2048 + c7 * 16);
    float sm[3] = {0.f, 0.f, 0.f}, sq[3] = {0.f, 0.f, 0.f};
#pragma unroll
    for (int i = 0; i < 3; ++i) { pinf(sm[i]); pinf(sq[i]); }
    fence();
    static_for<0, 9>([&](auto jc) {
      constexpr int jj = decltype(jc)::v, j = jj == 0 ? 8 : jj - 1;
      piece_stats<9 - jj + (jj <= GQ_POS ? 1 : 0)>(xr[j], sm[j >> 2], sq[j >> 2], y[j]);
      if constexpr (jj == 0) TS(14);
      if constexpr (jj == GQ_POS) {
        wait_vm_for<9 - jj>(gq);
        if (wv < 4) {
          const unsigned g0 = gq[0], g1 = gq[1], g2 = gq[2], g3 = gq[3];
          const f32x2 a0 = unpack2(g0), a1 = unpack2(g1), a2 = unpack2(g2), a3 = unpack2(g3);
          *(LDS_AS f32x4*)(lds + GBB + wi * 2048 + rq * 512 + c15 * 16) = f32x4{a0[0], a0[1], a1[0], a1[1]};
          *(LDS_AS f32x4*)(lds + GBB + wi * 2048 + rq * 512 + c15 * 16 + 256) = f32x4{a2[0], a2[1], a3[0], a3[1]};
          count_in(ldsbase + FLAG);
        }
        fence();
      }
      if constexpr (jj == GB_PIECE) {
        while (__builtin_amdgcn_readfirstlane(*(volatile LDS_AS unsigned*)(lds + FLAG)) < 4u) __builtin_amdgcn_s_sleep(1);
        keep2(pf0, pf1);
      }
      if constexpr (jj >= GB_PIECE) {
#pragma unroll
        for (int kk = jj - GB_PIECE; kk < (jj == 8 ? 5 : jj - GB_PIECE + 1); ++kk) {
          if (kk < 5) {
            const int k = kk == 0 ? 4 : kk - 1;
            LDS_AS char* p = k < 4 ? gr + k * 512 : grt;
#pragma unroll
            for (int e = 0; e < 2; ++e) { gv[k][e] = *(LDS_AS f32x4*)(p + e * 256); bv[k][e] = *(LDS_AS f32x4*)(p + 4096 + e * 256); }
          }
        }
        fence();
      }
    });
    fence();
    TS(3);
    const float mean = oct_sum(fold8(sm[0], sm[1]) + sm[2]) * (1.0f / D);
    const float var = fmaxf(oct_sum(fold8(sq[0], sq[1]) + sq[2]) * (1.0f / D) - mean * mean, 0.f);
    float rs[3], nm[3];
    rs[2] = __builtin_amdgcn_rsqf(var + 1e-5f);
    nm[2] = -mean * rs[2];
    rs[0] = sel8<0xc>(rs[2]); rs[1] = sel8<0x3>(rs[2]);
    nm[0] = sel8<0xc>(nm[2]); nm[1] = sel8<0x3>(nm[2]);
#pragma unroll
    for (int jj = 0; jj < 9; ++jj) {
      const int j = jj == 0 ? 8 : jj - 1, G = j >> 2, k = j == 8 ? 4 : (j & 3);
#pragma unroll
      for (int e = 0; e < 4; ++e) y[j][e] = __builtin_elementwise_fma(y[j][e], f32x2{rs[G], rs[G]}, f32x2{nm[G], nm[G]});
      fence();
#pragma unroll
      for (int e = 0; e < 4; ++e) {
        const f32x4 gg = gv[k][e >> 1], bb = bv[k][e >> 1];
        y[j][e] = __builtin_elementwise_fma(y[j][e], (e & 1) ? f32x2{gg[2], gg[3]} : f32x2{gg[0], gg[1]}, (e & 1) ? f32x2{bb[2], bb[3]} : f32x2{bb[0], bb[1]});
      }
      fence();
      *(LDS_AS u32x4*)(aw[G] + (j == 8 ? 0 : (j & 3) * 16384)) = u32x4{pack2(y[j][0]), pack2(y[j][1]), pack2(y[j][2]), pack2(y[j][3])};
      fence();
    }
  }
  TS(4);

  const unsigned wvoff = rowoff + (unsigned)(((((c7 >> 1) ^ (((r7 >> 1) & 1) | ((tid >> 5) & 2))) << 1) | (c7 & 1)) * 16);
  const unsigned wbase = (unsigned)h * 640u + (unsigned)wi * 8u * ROWB;
  auto request = [&](unsigned hslot, unsigned so) {
    dma3(ldsbase + hslot * (SLOT / 2) + (unsigned)wi * 1024u, wvoff, srdw, so);
  };

  if (wv >= 4) {
    wait_lgkm<0>();
    barrier();
    TS(6);
    __builtin_amdgcn_s_setprio(LPRIO);
    auto req = [&](auto cc) {
      constexpr int c = decltype(cc)::v;
      unsigned wb = wbase, lb = ldsbase + (unsigned)wi * 1024u;
      asm volatile("" : "+s"(wb), "+s"(lb));
      dma3(lb + ((c + 3) % 12) * (SLOT / 2), wvoff, srdw, wb + (c / 6) * 128u + (c % 6) * 96u * ROWB);
    };
    static_for<NE, 3>(req);
    barrier(); TS2(18); static_for<3, 5>(req);
    barrier(); TS2(21); static_for<5, 6>(req);
    wait_vm<12>();
    barrier(); TS2(24); static_for<6, 8>(req);
    static_for<0, 11>([&](auto qc) {
      constexpr int q = decltype(qc)::v;
      wait_vm<(q < 9 ? 12 : q == 9 ? 6 : 0)>();
      barrier();
      TS2(27 + 3 * q);
      if constexpr (q < 8) static_for<2 * q + 8, 2 * q + 10>(req);
    });
    TS(8);
  } else {
    {
      const unsigned hv = (unsigned)(lane >> 2) * ROWB + (unsigned)(((lane & 3) ^ ((lane >> 4) & 2)) * 16);
      const unsigned hs = 512u + (unsigned)h * 64u + (unsigned)wi * 16u * ROWB;
#pragma unroll
      for (int i = 0; i < 9; ++i) dma16(ldsbase + (i * 4 + wi) * 1024, hv, srdw, hs + (unsigned)(i * 64) * ROWB);
#pragma unroll
      for (int c = 0; c < NE; ++c) request(c + 3, wbase + c * 96u * ROWB);
      const unsigned li = (unsigned)tid, kk = (li * 43u) >> 8, ln = li - kk * 6u;
      touch(ldsbase + SINK, li < 108u ? ((unsigned)jb * 18u + kk) * ROWB + (ln + 1u + (ln >= 3u) * 2u) * 128u : 0xfffffff0u, srdw);
    }
    const int m = wi >> 1, n = wi & 1;
    const int fr = lane & 15, fg = lane >> 4;
    bf16x8 xa[2][18];
    LDS_AS char* ard[2][2];
#pragma unroll
    for (int e = 0; e < 2; ++e) {
      const unsigned o = (m * 2 + (fr >> 3)) * 1024 + (fr & 7) * 128 + (((4 * e + fg) ^ (fr & 7)) * 16);
      ard[e][0] = opaque(lds + XRES + o);
      ard[e][1] = opaque(lds + XRES + 5 * 8192 + o);
    }
    LDS_AS char* wrd[2][3];
#pragma unroll
    for (int t = 0; t < 2; ++t)
#pragma unroll
      for (int z = 0; z < 3; ++z)
        wrd[t][z] = opaque(lds + z * 2 * SLOT + (8 * fg + (fr >> 2)) * 128 + (((2 * n + t) ^ (((fr >> 3) & 1) | ((fg & 1) << 1))) * 32) + (fr & 3) * 8);
    LDS_AS char* wh = opaque(lds + (8 * fg + (fr >> 2)) * 64 + ((n ^ (fg & 1)) * 32) + (fr & 3) * 8);
    const unsigned ovoff = (unsigned)fr * ROWB + (unsigned)(2 * (fg & 1) + (fg >> 1)) * 16u;
    const unsigned orow = (unsigned)(row0 + m * 16) * ROWB;
    const unsigned osoff = orow + (unsigned)h * 640u + (unsigned)n * 64u;
    wait_lgkm<0>();
    wait_vm<6 + NE * 3 + 1>();
    TS(7);
    barrier();
    TS(6);

    bf16x8 wf[2][6];
    f32x4 acc[2][2], fin[2][2];
    u32x4 po[4][2];
    auto tr_frag = [&](LDS_AS char* p, int step) {
      const s16x4 lo = __builtin_amdgcn_ds_read_tr16_b64_v4i16((LDS_AS s16x4*)p);
      const s16x4 hi = __builtin_amdgcn_ds_read_tr16_b64_v4i16((LDS_AS s16x4*)(p + step));
      return __builtin_bit_cast(bf16x8, __builtin_shufflevector(lo, hi, 0, 1, 2, 3, 4, 5, 6, 7));
    };
    auto rd = [&](auto uc) {
      constexpr int u = decltype(uc)::v, hsl = u % 12;
      if constexpr (u >= 3) {
#pragma unroll
        for (int s = 0; s < 3; ++s)
#pragma unroll
          for (int t = 0; t < 2; ++t) wf[u & 1][s * 2 + t] = tr_frag(wrd[t][hsl >> 2] + (hsl & 3) * (SLOT / 2) + s * 4096, 512);
      } else {
#pragma unroll
        for (int s = 0; s < 6; ++s) {
          const int ks = u * 6 + s;
          wf[u & 1][s] = tr_frag(wh + ks * 2048, 256);
#pragma unroll
          for (int i = 0; i < 2; ++i) xa[i][ks] = *(LDS_AS bf16x8*)(ard[ks & 1][ks >= 10] + ((ks >> 1) % 5) * 8192 + i * 4096);
        }
      }
    };
    auto mm = [&](auto uc) {
      constexpr int u = decltype(uc)::v;
#pragma unroll
      for (int f = 0; f < 6; ++f) {
        const int ks = u >= 3 ? ((u - 3) % 6) * 3 + f / 2 : u * 6 + f, t = f & 1;
        const bool first = u >= 3 ? ks == 0 : ks < 2;
#pragma unroll
        for (int i = 0; i < 2; ++i) acc[i][t] = __builtin_amdgcn_mfma_f32_16x16x32_bf16(wf[u & 1][f], xa[i][ks], first ? f32x4{0.f, 0.f, 0.f, 0.f} : acc[i][t], 0, 0, 0);
      }
    };
    auto pack = [&](const f32x4& a, const f32x4& c) {
      const auto lo = __builtin_amdgcn_permlane16_swap(pack2(f32x2{a[0], a[1]}), pack2(f32x2{c[0], c[1]}), false, false);
      const auto hi = __builtin_amdgcn_permlane16_swap(pack2(f32x2{a[2], a[3]}), pack2(f32x2{c[2], c[3]}), false, false);
      return u32x4{(unsigned)lo[0], (unsigned)hi[0], (unsigned)lo[1], (unsigned)hi[1]};
    };
    const unsigned hvoff = (unsigned)(fr + (fg & 1) * 32) * ROWB + (unsigned)(fg >> 1) * 16u;
    u32x4 ph;
    auto store = [&](int k) {
      if (k == 0) st16(ph, hvoff, srdo, orow + 512u + (unsigned)h * 64u + (unsigned)n * 32u);
      else st16(po[(k - 1) / 2][(k - 1) & 1], ovoff, srdo, osoff + ((k - 1) / 2) * 128u + ((k - 1) & 1) * 32u * ROWB);
    };
    rd(IC<0>{});
    fence();
    static_for<0, 27>([&](auto uc) {
      constexpr int u = decltype(uc)::v, v = u - 3;
      constexpr bool pk = v >= 6 && v % 6 < 2;
      if constexpr (u < 3) {
        wait_vm<(u == 0 ? 3 + NE * 3 + 1 : u == 1 ? NE * 3 + 1 : (NE > 2 ? 3 : 0) + 1)>();
        wait_lgkm<0>();
        barrier();
        fence();
      } else if constexpr ((v & 1) && u < 26) {
        if constexpr (v == 1) wait_vm<1>();
        barrier();
        fence();
      }
      if constexpr (u < 26) rd(IC<u + 1>{});
      if constexpr (pk) po[v / 6 - 1][v % 6] = pack(fin[v % 6][0], fin[v % 6][1]);
      if constexpr (u == 3) ph = pack(fin[0][0] + fin[0][1], fin[1][0] + fin[1][1]);
      mm(uc);
      if constexpr (u < 26) {
        static_for<0, 12>([&](auto ic) {
          constexpr int i = decltype(ic)::v;
          __builtin_amdgcn_sched_group_barrier(0x008, 1, 0);
          if constexpr (u < 2) { if constexpr (i < 8) __builtin_amdgcn_sched_group_barrier(0x100, 3, 0); }
          else __builtin_amdgcn_sched_group_barrier(0x100, 1, 0);
          if constexpr ((pk && i >= 2 && i < 8) || (u == 3 && i >= 2)) __builtin_amdgcn_sched_group_barrier(0x002, 1, 0);
        });
      }
      fence();
      if constexpr (store_at(u) >= 0) { store(store_at(u)); fence(); }
      if constexpr (u == 2 || (v >= 0 && v % 6 == 5)) {
#pragma unroll
        for (int i = 0; i < 2; ++i) { fin[i][0] = acc[i][0]; fin[i][1] = acc[i][1]; }
      }
    });
    po[3][0] = pack(fin[0][0], fin[0][1]);
    po[3][1] = pack(fin[1][0], fin[1][1]);
    store(7);
    store(8);
    TS(9);
  }
  asm volatile(".Lpf_end:");
}
}

extern "C" void kernel_launch(void* const* d_in, const int* in_sizes, int n_in, void* d_out, int out_size, void* d_ws,
                              size_t ws_size, hipStream_t stream) {
  (void)in_sizes; (void)n_in; (void)out_size; (void)d_ws; (void)ws_size;
  (void)hipFuncSetAttribute((const void*)ln_linear_kernel, hipFuncAttributeMaxDynamicSharedMemorySize, LDS_BYTES);
  ln_linear_kernel<<<256, 512, LDS_BYTES, stream>>>((const uint16_t*)d_in[0], (const uint16_t*)d_in[1], (const uint16_t*)d_in[2],
                                                    (const uint16_t*)d_in[3], (uint16_t*)d_out
  );
}
```

```cpp
#include <hip/hip_runtime.h>
#include <stdint.h>

typedef short s16x4 __attribute__((ext_vector_type(4)));
typedef __bf16 bf16x2 __attribute__((ext_vector_type(2)));
typedef __bf16 bf16x8 __attribute__((ext_vector_type(8)));
typedef float f32x2 __attribute__((ext_vector_type(2)));
typedef float f32x4 __attribute__((ext_vector_type(4)));
typedef unsigned u32x2 __attribute__((ext_vector_type(2)));
typedef unsigned u32x4 __attribute__((ext_vector_type(4)));
typedef unsigned u32x8 __attribute__((ext_vector_type(8)));
#define LDS_AS __attribute__((address_space(3)))

#define X_POLICY ""
#define S_POLICY " sc1"
#define H_POLICY ""
#define LPRIO 2
#define NE 3
#define GB_PIECE 5
#define GQ_POS 1
#define ST_LIST 19, 20, 21, 22, 23, 24, 25
#define STAMPS 0

namespace {
constexpr int D = 576;
constexpr int ROWB = D * 2;
constexpr int SLOT = 24576;
constexpr int XRES = 3 * SLOT;
constexpr int GBB = 6 * SLOT;
constexpr int FLAG = GBB + 8192;
constexpr int SINK = FLAG + 1024;
constexpr int LDS_BYTES = SINK + 1024;

constexpr int store_at(int u) {
  constexpr int at[7] = {ST_LIST};
  for (int k = 0; k < 7; ++k) if (at[k] == u) return k;
  return -1;
}
template <int V> struct IC { static constexpr int v = V; };
template <int I, int N, class F> __device__ __forceinline__ void static_for(F&& f) {
  if constexpr (I < N) { f(IC<I>{}); static_for<I + 1, N>(f); }
}
__device__ __forceinline__ u32x4 make_srd(unsigned lo, unsigned hi, unsigned bytes) { return u32x4{lo, hi & 0xffffu, bytes, 0x00020000u}; }
template <int N> __device__ __forceinline__ void wait_vm() { asm volatile("s_waitcnt vmcnt(%0)" ::"i"(N) : "memory"); }
template <int N> __device__ __forceinline__ void wait_lgkm() { asm volatile("s_waitcnt lgkmcnt(%0)" ::"i"(N) : "memory"); }
__device__ __forceinline__ void barrier() { asm volatile("s_barrier" ::: "memory"); }
__device__ __forceinline__ void fence() { __builtin_amdgcn_sched_barrier(0); }
__device__ __forceinline__ void dma16(unsigned lds_addr, unsigned voff, u32x4 srd, unsigned soff) {
  asm volatile("s_mov_b32 m0, %0\n\ts_nop 0\n\tbuffer_load_dwordx4 %1, %2, %3 offen lds" ::"s"(lds_addr), "v"(voff), "s"(srd), "s"(soff) : "memory");
}
__device__ __forceinline__ void dma3(unsigned lds_addr, unsigned voff, u32x4 srd, unsigned soff) {
  unsigned s1, s2;
  asm volatile("s_mov_b32 m0, %2\n\ts_add_u32 %0, %5, 0x9000\n\tbuffer_load_dwordx4 %3, %4, %5 offen lds\n\t"
               "s_add_u32 m0, m0, 0x1000\n\ts_add_u32 %1, %5, 0x12000\n\tbuffer_load_dwordx4 %3, %4, %0 offen lds\n\t"
               "s_add_u32 m0, m0, 0x1000\n\ts_nop 0\n\tbuffer_load_dwordx4 %3, %4, %1 offen lds"
               : "=&s"(s1), "=&s"(s2) : "s"(lds_addr), "v"(voff), "s"(srd), "s"(soff) : "memory", "scc");
}
__device__ __forceinline__ void touch(unsigned lds_addr, unsigned voff, u32x4 srd) {
  asm volatile("s_mov_b32 m0, %0\n\ts_nop 0\n\tbuffer_load_dword %1, %2, 0 offen lds" ::"s"(lds_addr), "v"(voff), "s"(srd) : "memory");
}
__device__ __forceinline__ void st16(u32x4 v, unsigned voff, u32x4 srd, unsigned soff) {
  asm volatile("buffer_store_dwordx4 %0, %1, %2, %3 offen" S_POLICY "\n\ts_nop 1" ::"v"(v), "v"(voff), "s"(srd), "s"(soff) : "memory");
}
__device__ __forceinline__ void st16h(u32x4 v, unsigned voff, u32x4 srd, unsigned soff) {
  asm volatile("buffer_store_dwordx4 %0, %1, %2, %3 offen" H_POLICY "\n\ts_nop 1" ::"v"(v), "v"(voff), "s"(srd), "s"(soff) : "memory");
}
template <int OFF> __device__ __forceinline__ void ldx(u32x4& dst, unsigned voff, unsigned long long base) {
  asm volatile("global_load_dwordx4 %0, %1, %2 offset:%3" X_POLICY : "=v"(dst) : "v"(voff), "s"(base), "i"(OFF));
}
__device__ __forceinline__ void ldg(u32x4& dst, unsigned voff, u32x4 srd) {
  asm volatile("buffer_load_dwordx4 %0, %1, %2, 0 offen" : "=v"(dst) : "v"(voff), "s"(srd));
}
template <int N> __device__ __forceinline__ void wait_vm_for(u32x4& v) { asm volatile("s_waitcnt vmcnt(%1)" : "+v"(v) : "i"(N)); }
__device__ __forceinline__ void ts_raw(unsigned long long& t) { asm volatile("s_memtime %0" : "=s"(t)); }
template <int I> __device__ __forceinline__ void ts_lane(unsigned& tsv, unsigned long long t) { asm volatile("" : "+s"(t)); asm volatile("v_writelane_b32 %0, %1, %2" : "+v"(tsv) : "s"((unsigned)t), "i"(I)); }
template <int I> __device__ __forceinline__ void ts_put(unsigned& tsv) {
  unsigned long long t_; asm volatile("s_memtime %0\n\ts_waitcnt lgkmcnt(0)" : "=s"(t_) :: "memory");
  asm volatile("v_writelane_b32 %0, %1, %2" : "+v"(tsv) : "s"((unsigned)t_), "i"(I));
}
template <int CTRL> __device__ __forceinline__ float dpp_add(float v) {
  return v + __builtin_bit_cast(float, __builtin_amdgcn_update_dpp(0, __builtin_bit_cast(int, v), CTRL, 0xf, 0xf, true));
}
__device__ __forceinline__ float oct_sum(float v) {
  v = dpp_add<0xB1>(v); v = dpp_add<0x4E>(v); v = dpp_add<0x141>(v);
  return v;
}
__device__ __forceinline__ unsigned pack2(f32x2 v) { return __builtin_bit_cast(unsigned, __builtin_convertvector(v, bf16x2)); }
__device__ __forceinline__ f32x2 unpack2(unsigned u) { return f32x2{__uint_as_float(u << 16), __uint_as_float(u & 0xffff0000u)}; }
template <int N> __device__ __forceinline__ void piece_stats(const u32x4& x, float& s, float& q, f32x2 (&y)[4]) {
  float a0, a1, a2, a3, a4, a5, a6, a7;
  const unsigned x0 = x[0], x1 = x[1], x2 = x[2], x3 = x[3];
  asm volatile(
      "s_waitcnt vmcnt(%16)\n\t"
      "v_dot2c_f32_bf16 %0, %14, %10\n\tv_dot2c_f32_bf16 %1, %10, %10\n\t"
      "v_dot2c_f32_bf16 %0, %14, %11\n\tv_dot2c_f32_bf16 %1, %11, %11\n\t"
      "v_dot2c_f32_bf16 %0, %14, %12\n\tv_dot2c_f32_bf16 %1, %12, %12\n\t"
      "v_dot2c_f32_bf16 %0, %14, %13\n\tv_dot2c_f32_bf16 %1, %13, %13\n\t"
      "v_lshlrev_b32 %2, 16, %10\n\tv_and_b32 %3, %15, %10\n\t"
      "v_lshlrev_b32 %4, 16, %11\n\tv_and_b32 %5, %15, %11\n\t"
      "v_lshlrev_b32 %6, 16, %12\n\tv_and_b32 %7, %15, %12\n\t"
      "v_lshlrev_b32 %8, 16, %13\n\tv_and_b32 %9, %15, %13"
      : "+v"(s), "+v"(q), "=&v"(a0), "=&v"(a1), "=&v"(a2), "=&v"(a3), "=&v"(a4), "=&v"(a5), "=&v"(a6), "=&v"(a7)
      : "v"(x0), "v"(x1), "v"(x2), "v"(x3), "s"(0x3f803f80u), "s"(0xffff0000u), "i"(N));
  y[0] = f32x2{a0, a1}; y[1] = f32x2{a2, a3}; y[2] = f32x2{a4, a5}; y[3] = f32x2{a6, a7};
}
__device__ __forceinline__ void count_in(unsigned lds_addr) { asm volatile("s_mov_b64 exec, 1\n\tds_add_u32 %0, %1\n\ts_mov_b64 exec, -1" ::"v"(lds_addr), "v"(1u) : "memory"); }
__device__ __forceinline__ void keep2(unsigned a, unsigned b) { asm volatile("s_waitcnt lgkmcnt(0)" ::"s"(a), "s"(b) : "memory"); }
__device__ __forceinline__ void pinf(float& v) { asm volatile("" : "+v"(v)); }
__device__ __forceinline__ LDS_AS char* opaque(LDS_AS char* p) { asm volatile("" : "+v"(p)); return p; }
template <int MASK> __device__ __forceinline__ float sel8(float v) {
  const int i = __builtin_bit_cast(int, v);
  return __builtin_bit_cast(float, __builtin_amdgcn_update_dpp(i, i, 0x128, 0xf, MASK, false));
}
__device__ __forceinline__ float fold8(float a, float b) {
  float t;
  asm("s_nop 1\n\tv_add_f32_dpp %0, %1, %1 row_ror:8 row_mask:0xf bank_mask:0x3\n\tv_add_f32_dpp %0, %2, %2 row_ror:8 row_mask:0xf bank_mask:0xc" : "=&v"(t) : "v"(a), "v"(b));
  return t;
}

#define TS(i) do {} while (0)
#define TS2(i) do {} while (0)

__global__ __launch_bounds__(512, 2) void ln_linear_kernel(const uint16_t* __restrict__ x, const uint16_t* __restrict__ g,
                                                           const uint16_t* __restrict__ b, const uint16_t* __restrict__ w,
                                                           uint16_t* __restrict__ out
) {
  extern __shared__ __attribute__((aligned(1024))) char smem[];
  LDS_AS char* lds = (LDS_AS char*)smem;
  unsigned long long kx;
  u32x8 ka;
  asm volatile("s_load_dwordx2 %0, %2, 0x0\n\ts_load_dwordx8 %1, %2, 0x8" : "=&s"(kx), "=&s"(ka) : "s"(__builtin_amdgcn_kernarg_segment_ptr()));
  const int tid = threadIdx.x, lane = tid & 63;
  const unsigned bid = blockIdx.x;
  const unsigned row0 = (bid & 7) * 1024 + (bid >> 4) * 64;
  unsigned xvt = (unsigned)(tid >> 3) * ROWB + (unsigned)(tid & 7) * 16, xvm = (unsigned)(tid >> 4) * 2 * ROWB + (unsigned)(tid & 15) * 16;
  unsigned xo = row0 * ROWB;
  const unsigned t0 = __builtin_amdgcn_readfirstlane(tid);
  asm volatile("" : "+v"(xvt), "+v"(xvm));
  asm volatile("" : "+s"(xo));
  unsigned pf0 = 0, pf1 = 0;
  asm volatile("s_waitcnt lgkmcnt(0)" : "+s"(kx), "+s"(ka));
  if (t0 >= 256) {
    *(LDS_AS unsigned*)(lds + FLAG) = 0u;
    unsigned long long pc;
    unsigned len;
    asm volatile("s_getpc_b64 %0\n.Lpf_here:\n\ts_mov_b32 %1, .Lpf_end-.Lpf_here-128" : "=s"(pc), "=s"(len));
    const unsigned o = min((bid >> 3) * 128u + (t0 & 192u) * 64u, len);
    asm volatile("s_waitcnt lgkmcnt(0)\n\ts_load_dword %0, %2, %3\n\ts_load_dword %1, %2, %3 offset:64\n\ts_barrier" : "=&s"(pf0), "=&s"(pf1) : "s"(pc), "s"(o) : "memory");
  }
  u32x4 xr[9], gq;
  {
    const unsigned long long xb = kx + xo;
    ldx<1024>(xr[8], xvt, xb);
    static_for<0, GQ_POS>([&](auto jc) { constexpr int j = decltype(jc)::v; ldx<(j >> 2) * ROWB + (j & 3) * 256>(xr[j], xvm, xb); });
    fence();
    ldg(gq, (unsigned)(tid & 127) * 16u, make_srd(t0 < 128 ? ka[0] : ka[2], t0 < 128 ? ka[1] : ka[3], t0 < 256 ? (unsigned)ROWB : 0u));
    static_for<GQ_POS, 8>([&](auto jc) { constexpr int j = decltype(jc)::v; ldx<(j >> 2) * ROWB + (j & 3) * 256>(xr[j], xvm, xb); });
    fence();
  }
  const int wv = t0 >> 6, wi = wv & 3;
  const unsigned jb = bid >> 3, h = jb & 1;
  const int r7 = lane >> 3, c7 = lane & 7, c15 = lane & 15, rq = lane >> 4;
  const unsigned ldsbase = (unsigned)(uintptr_t)lds;
  const unsigned rowoff = (unsigned)r7 * ROWB;
  if (wv < 4) barrier();
  const u32x4 srdw = make_srd(ka[4], ka[5], (unsigned)D * ROWB), srdo = make_srd(ka[6], ka[7], 8192u * ROWB);
  touch(ldsbase + SINK, (wv >= 4 && wv < 7 && lane < 18) ? (jb * 18u + (unsigned)lane) * ROWB + (wv == 4 ? 512u : wv == 5 ? 0u : 640u) : 0xfffffff0u, srdw);
  TS(13);

  {
    f32x4 gv[5][2], bv[5][2];
    f32x2 y[9][4];
    LDS_AS char* aw[3] = {opaque(lds + XRES + wv * 1024 + (c15 >> 3) * 8192 + (2 * rq) * 128 + ((c7 ^ (2 * rq)) * 16)),
                          opaque(lds + XRES + wv * 1024 + (c15 >> 3) * 8192 + (2 * rq + 1) * 128 + ((c7 ^ (2 * rq + 1)) * 16)),
                          opaque(lds + XRES + 8 * 8192 + wv * 1024 + r7 * 128 + ((c7 ^ r7) * 16))};
    LDS_AS char* gr = opaque(lds + GBB + c15 * 16);
    LDS_AS char* grt = opaque(lds + GBB + 2048 + c7 * 16);
    float sm[3] = {0.f, 0.f, 0.f}, sq[3] = {0.f, 0.f, 0.f};
#pragma unroll
    for (int i = 0; i < 3; ++i) { pinf(sm[i]); pinf(sq[i]); }
    fence();
    static_for<0, 9>([&](auto jc) {
      constexpr int jj = decltype(jc)::v, j = jj == 0 ? 8 : jj - 1;
      piece_stats<9 - jj + (jj <= GQ_POS ? 1 : 0)>(xr[j], sm[j >> 2], sq[j >> 2], y[j]);
      if constexpr (jj == 0) TS(14);
      if constexpr (jj == GQ_POS) {
        wait_vm_for<9 - jj>(gq);
        if (wv < 4) {
          const unsigned g0 = gq[0], g1 = gq[1], g2 = gq[2], g3 = gq[3];
          const f32x2 a0 = unpack2(g0), a1 = unpack2(g1), a2 = unpack2(g2), a3 = unpack2(g3);
          *(LDS_AS f32x4*)(lds + GBB + wi * 2048 + rq * 512 + c15 * 16) = f32x4{a0[0], a0[1], a1[0], a1[1]};
          *(LDS_AS f32x4*)(lds + GBB + wi * 2048 + rq * 512 + c15 * 16 + 256) = f32x4{a2[0], a2[1], a3[0], a3[1]};
          count_in(ldsbase + FLAG);
        }
        fence();
      }
      if constexpr (jj == GB_PIECE) {
        while (__builtin_amdgcn_readfirstlane(*(volatile LDS_AS unsigned*)(lds + FLAG)) < 4u) __builtin_amdgcn_s_sleep(1);
        keep2(pf0, pf1);
      }
      if constexpr (jj >= GB_PIECE) {
#pragma unroll
        for (int kk = jj - GB_PIECE; kk < (jj == 8 ? 5 : jj - GB_PIECE + 1); ++kk) {
          if (kk < 5) {
            const int k = kk == 0 ? 4 : kk - 1;
            LDS_AS char* p = k < 4 ? gr + k * 512 : grt;
#pragma unroll
            for (int e = 0; e < 2; ++e) { gv[k][e] = *(LDS_AS f32x4*)(p + e * 256); bv[k][e] = *(LDS_AS f32x4*)(p + 4096 + e * 256); }
          }
        }
        fence();
      }
    });
    fence();
    TS(3);
    const float mean = oct_sum(fold8(sm[0], sm[1]) + sm[2]) * (1.0f / D);
    const float var = fmaxf(oct_sum(fold8(sq[0], sq[1]) + sq[2]) * (1.0f / D) - mean * mean, 0.f);
    float rs[3], nm[3];
    rs[2] = __builtin_amdgcn_rsqf(var + 1e-5f);
    nm[2] = -mean * rs[2];
    rs[0] = sel8<0xc>(rs[2]); rs[1] = sel8<0x3>(rs[2]);
    nm[0] = sel8<0xc>(nm[2]); nm[1] = sel8<0x3>(nm[2]);
#pragma unroll
    for (int jj = 0; jj < 9; ++jj) {
      const int j = jj == 0 ? 8 : jj - 1, G = j >> 2, k = j == 8 ? 4 : (j & 3);
#pragma unroll
      for (int e = 0; e < 4; ++e) y[j][e] = __builtin_elementwise_fma(y[j][e], f32x2{rs[G], rs[G]}, f32x2{nm[G], nm[G]});
      fence();
#pragma unroll
      for (int e = 0; e < 4; ++e) {
        const f32x4 gg = gv[k][e >> 1], bb = bv[k][e >> 1];
        y[j][e] = __builtin_elementwise_fma(y[j][e], (e & 1) ? f32x2{gg[2], gg[3]} : f32x2{gg[0], gg[1]}, (e & 1) ? f32x2{bb[2], bb[3]} : f32x2{bb[0], bb[1]});
      }
      fence();
      *(LDS_AS u32x4*)(aw[G] + (j == 8 ? 0 : (j & 3) * 16384)) = u32x4{pack2(y[j][0]), pack2(y[j][1]), pack2(y[j][2]), pack2(y[j][3])};
      fence();
    }
  }
  TS(4);

  const unsigned wvoff = rowoff + (unsigned)(((((c7 >> 1) ^ (((r7 >> 1) & 1) | ((tid >> 5) & 2))) << 1) | (c7 & 1)) * 16);
  const unsigned wbase = (unsigned)h * 640u + (unsigned)wi * 8u * ROWB;
  auto request = [&](unsigned hslot, unsigned so) {
    dma3(ldsbase + hslot * (SLOT / 2) + (unsigned)wi * 1024u, wvoff, srdw, so);
  };

  if (wv >= 4) {
    wait_lgkm<0>();
    barrier();
    TS(6);
    __builtin_amdgcn_s_setprio(LPRIO);
    auto req = [&](auto cc) {
      constexpr int c = decltype(cc)::v;
      unsigned wb = wbase, lb = ldsbase + (unsigned)wi * 1024u;
      asm volatile("" : "+s"(wb), "+s"(lb));
      dma3(lb + ((c + 3) % 12) * (SLOT / 2), wvoff, srdw, wb + (c / 6) * 128u + (c % 6) * 96u * ROWB);
    };
    static_for<NE, 3>(req);
    barrier(); TS2(18); static_for<3, 5>(req);
    barrier(); TS2(21); static_for<5, 6>(req);
    wait_vm<12>();
    barrier(); TS2(24); static_for<6, 8>(req);
    static_for<0, 11>([&](auto qc) {
      constexpr int q = decltype(qc)::v;
      wait_vm<(q < 9 ? 12 : q == 9 ? 6 : 0)>();
      barrier();
      TS2(27 + 3 * q);
      if constexpr (q < 8) static_for<2 * q + 8, 2 * q + 10>(req);
    });
    TS(8);
  } else {
    {
      const unsigned hv = (unsigned)(lane >> 2) * ROWB + (unsigned)(((lane & 3) ^ ((lane >> 4) & 2)) * 16);
      const unsigned hs = 512u + (unsigned)h * 64u + (unsigned)wi * 16u * ROWB;
#pragma unroll
      for (int i = 0; i < 9; ++i) dma16(ldsbase + (i * 4 + wi) * 1024, hv, srdw, hs + (unsigned)(i * 64) * ROWB);
#pragma unroll
      for (int c = 0; c < NE; ++c) request(c + 3, wbase + c * 96u * ROWB);
      const unsigned li = (unsigned)tid, kk = (li * 43u) >> 8, ln = li - kk * 6u;
      touch(ldsbase + SINK, li < 108u ? ((unsigned)jb * 18u + kk) * ROWB + (ln + 1u + (ln >= 3u) * 2u) * 128u : 0xfffffff0u, srdw);
    }
    const int m = wi >> 1, n = wi & 1;
    const int fr = lane & 15, fg = lane >> 4;
    bf16x8 xa[2][18];
    LDS_AS char* ard[2][2];
#pragma unroll
    for (int e = 0; e < 2; ++e) {
      const unsigned o = (m * 2 + (fr >> 3)) * 1024 + (fr & 7) * 128 + (((4 * e + fg) ^ (fr & 7)) * 16);
      ard[e][0] = opaque(lds + XRES + o);
      ard[e][1] = opaque(lds + XRES + 5 * 8192 + o);
    }
    LDS_AS char* wrd[2][3];
#pragma unroll
    for (int t = 0; t < 2; ++t)
#pragma unroll
      for (int z = 0; z < 3; ++z)
        wrd[t][z] = opaque(lds + z * 2 * SLOT + (8 * fg + (fr >> 2)) * 128 + (((2 * n + t) ^ (((fr >> 3) & 1) | ((fg & 1) << 1))) * 32) + (fr & 3) * 8);
    LDS_AS char* wh = opaque(lds + (8 * fg + (fr >> 2)) * 64 + ((n ^ (fg & 1)) * 32) + (fr & 3) * 8);
    const unsigned ovoff = (unsigned)fr * ROWB + (unsigned)(2 * (fg & 1) + (fg >> 1)) * 16u;
    const unsigned orow = (unsigned)(row0 + m * 16) * ROWB;
    const unsigned osoff = orow + (unsigned)h * 640u + (unsigned)n * 64u;
    wait_lgkm<0>();
    wait_vm<6 + NE * 3 + 1>();
    TS(7);
    barrier();
    TS(6);

    bf16x8 wf[2][6];
    f32x4 acc[2][2], fin[2][2];
    u32x4 po[4][2];
    auto tr_frag = [&](LDS_AS char* p, int step) {
      const s16x4 lo = __builtin_amdgcn_ds_read_tr16_b64_v4i16((LDS_AS s16x4*)p);
      const s16x4 hi = __builtin_amdgcn_ds_read_tr16_b64_v4i16((LDS_AS s16x4*)(p + step));
      return __builtin_bit_cast(bf16x8, __builtin_shufflevector(lo, hi, 0, 1, 2, 3, 4, 5, 6, 7));
    };
    auto rd = [&](auto uc) {
      constexpr int u = decltype(uc)::v, hsl = u % 12;
      if constexpr (u >= 3) {
#pragma unroll
        for (int s = 0; s < 3; ++s)
#pragma unroll
          for (int t = 0; t < 2; ++t) wf[u & 1][s * 2 + t] = tr_frag(wrd[t][hsl >> 2] + (hsl & 3) * (SLOT / 2) + s * 4096, 512);
      } else {
#pragma unroll
        for (int s = 0; s < 6; ++s) {
          const int ks = u * 6 + s;
          wf[u & 1][s] = tr_frag(wh + ks * 2048, 256);
#pragma unroll
          for (int i = 0; i < 2; ++i) xa[i][ks] = *(LDS_AS bf16x8*)(ard[ks & 1][ks >= 10] + ((ks >> 1) % 5) * 8192 + i * 4096);
        }
      }
    };
    auto mm = [&](auto uc) {
      constexpr int u = decltype(uc)::v;
#pragma unroll
      for (int f = 0; f < 6; ++f) {
        const int ks = u >= 3 ? ((u - 3) % 6) * 3 + f / 2 : u * 6 + f, t = f & 1;
        const bool first = u >= 3 ? ks == 0 : ks < 2;
#pragma unroll
        for (int i = 0; i < 2; ++i) acc[i][t] = __builtin_amdgcn_mfma_f32_16x16x32_bf16(wf[u & 1][f], xa[i][ks], first ? f32x4{0.f, 0.f, 0.f, 0.f} : acc[i][t], 0, 0, 0);
      }
    };
    auto pack = [&](const f32x4& a, const f32x4& c) {
      const auto lo = __builtin_amdgcn_permlane16_swap(pack2(f32x2{a[0], a[1]}), pack2(f32x2{c[0], c[1]}), false, false);
      const auto hi = __builtin_amdgcn_permlane16_swap(pack2(f32x2{a[2], a[3]}), pack2(f32x2{c[2], c[3]}), false, false);
      return u32x4{(unsigned)lo[0], (unsigned)hi[0], (unsigned)lo[1], (unsigned)hi[1]};
    };
    const unsigned hvoff = (unsigned)(fr + (fg & 1) * 32) * ROWB + (unsigned)(fg >> 1) * 16u;
    u32x4 ph;
    auto store = [&](int k) {
      if (k == 0) st16h(ph, hvoff, srdo, orow + 512u + (unsigned)h * 64u + (unsigned)n * 32u);
      else st16(po[(k - 1) / 2][(k - 1) & 1], ovoff, srdo, osoff + ((k - 1) / 2) * 128u + ((k - 1) & 1) * 32u * ROWB);
    };
    rd(IC<0>{});
    fence();
    static_for<0, 27>([&](auto uc) {
      constexpr int u = decltype(uc)::v, v = u - 3;
      constexpr bool pk = v >= 6 && v % 6 < 2;
      if constexpr (u < 3) {
        wait_vm<(u == 0 ? 3 + NE * 3 + 1 : u == 1 ? NE * 3 + 1 : (NE > 2 ? 3 : 0) + 1)>();
        wait_lgkm<0>();
        barrier();
        fence();
      } else if constexpr ((v & 1) && u < 26) {
        if constexpr (v == 1) wait_vm<1>();
        barrier();
        fence();
      }
      if constexpr (u < 26) rd(IC<u + 1>{});
      if constexpr (pk) po[v / 6 - 1][v % 6] = pack(fin[v % 6][0], fin[v % 6][1]);
      if constexpr (u == 3) ph = pack(fin[0][0] + fin[0][1], fin[1][0] + fin[1][1]);
      mm(uc);
      if constexpr (u < 26) {
        static_for<0, 12>([&](auto ic) {
          constexpr int i = decltype(ic)::v;
          __builtin_amdgcn_sched_group_barrier(0x008, 1, 0);
          if constexpr (u < 2) { if constexpr (i < 8) __builtin_amdgcn_sched_group_barrier(0x100, 3, 0); }
          else __builtin_amdgcn_sched_group_barrier(0x100, 1, 0);
          if constexpr ((pk && i >= 2 && i < 8) || (u == 3 && i >= 2)) __builtin_amdgcn_sched_group_barrier(0x002, 1, 0);
        });
      }
      fence();
      if constexpr (store_at(u) >= 0) { store(store_at(u)); fence(); }
      if constexpr (u == 2 || (v >= 0 && v % 6 == 5)) {
#pragma unroll
        for (int i = 0; i < 2; ++i) { fin[i][0] = acc[i][0]; fin[i][1] = acc[i][1]; }
      }
    });
    po[3][0] = pack(fin[0][0], fin[0][1]);
    po[3][1] = pack(fin[1][0], fin[1][1]);
    store(7);
    store(8);
    TS(9);
  }
  asm volatile(".Lpf_end:");
}
}

extern "C" void kernel_launch(void* const* d_in, const int* in_sizes, int n_in, void* d_out, int out_size, void* d_ws,
                              size_t ws_size, hipStream_t stream) {
  (void)in_sizes; (void)n_in; (void)out_size; (void)d_ws; (void)ws_size;
  (void)hipFuncSetAttribute((const void*)ln_linear_kernel, hipFuncAttributeMaxDynamicSharedMemorySize, LDS_BYTES);
  ln_linear_kernel<<<256, 512, LDS_BYTES, stream>>>((const uint16_t*)d_in[0], (const uint16_t*)d_in[1], (const uint16_t*)d_in[2],
                                                    (const uint16_t*)d_in[3], (uint16_t*)d_out
  );
}
```

```cpp
#define PROBE 0
#define PROBE_WAVE 0
#include <hip/hip_runtime.h>
#include <stdint.h>

typedef short s16x4 __attribute__((ext_vector_type(4)));
typedef __bf16 bf16x2 __attribute__((ext_vector_type(2)));
typedef __bf16 bf16x8 __attribute__((ext_vector_type(8)));
typedef float f32x2 __attribute__((ext_vector_type(2)));
typedef float f32x4 __attribute__((ext_vector_type(4)));
typedef unsigned u32x2 __attribute__((ext_vector_type(2)));
typedef unsigned u32x4 __attribute__((ext_vector_type(4)));
typedef unsigned u32x8 __attribute__((ext_vector_type(8)));
#define LDS_AS __attribute__((address_space(3)))

#define X_POLICY ""
#define S_POLICY " sc1"
#define H_POLICY ""
#define LPRIO 2
#define NE 3
#define GB_PIECE 5
#define GQ_POS 1
#define ST_LIST 19, 20, 21, 22, 23, 24, 25
#define STAMPS 0

namespace {
constexpr int D = 576;
constexpr int ROWB = D * 2;
constexpr int SLOT = 24576;
constexpr int XRES = 3 * SLOT;
constexpr int GBB = 6 * SLOT;
constexpr int FLAG = GBB + 8192;
constexpr int SINK = FLAG + 1024;
constexpr int LDS_BYTES = SINK + 1024;

constexpr int store_at(int u) {
  constexpr int at[7] = {ST_LIST};
  for (int k = 0; k < 7; ++k) if (at[k] == u) return k;
  return -1;
}
template <int V> struct IC { static constexpr int v = V; };
template <int I, int N, class F> __device__ __forceinline__ void static_for(F&& f) {
  if constexpr (I < N) { f(IC<I>{}); static_for<I + 1, N>(f); }
}
__device__ __forceinline__ u32x4 make_srd(unsigned lo, unsigned hi, unsigned bytes) { return u32x4{lo, hi & 0xffffu, bytes, 0x00020000u}; }
template <int N> __device__ __forceinline__ void wait_vm() { asm volatile("s_waitcnt vmcnt(%0)" ::"i"(N) : "memory"); }
template <int N> __device__ __forceinline__ void wait_lgkm() { asm volatile("s_waitcnt lgkmcnt(%0)" ::"i"(N) : "memory"); }
__device__ __forceinline__ void barrier() { asm volatile("s_barrier" ::: "memory"); }
__device__ __forceinline__ void fence() { __builtin_amdgcn_sched_barrier(0); }
__device__ __forceinline__ void dma16(unsigned lds_addr, unsigned voff, u32x4 srd, unsigned soff) {
  asm volatile("s_mov_b32 m0, %0\n\ts_nop 0\n\tbuffer_load_dwordx4 %1, %2, %3 offen lds" ::"s"(lds_addr), "v"(voff), "s"(srd), "s"(soff) : "memory");
}
__device__ __forceinline__ void dma3(unsigned lds_addr, unsigned voff, u32x4 srd, unsigned soff) {
  unsigned s1, s2;
  asm volatile("s_mov_b32 m0, %2\n\ts_add_u32 %0, %5, 0x9000\n\tbuffer_load_dwordx4 %3, %4, %5 offen lds\n\t"
               "s_add_u32 m0, m0, 0x1000\n\ts_add_u32 %1, %5, 0x12000\n\tbuffer_load_dwordx4 %3, %4, %0 offen lds\n\t"
               "s_add_u32 m0, m0, 0x1000\n\ts_nop 0\n\tbuffer_load_dwordx4 %3, %4, %1 offen lds"
               : "=&s"(s1), "=&s"(s2) : "s"(lds_addr), "v"(voff), "s"(srd), "s"(soff) : "memory", "scc");
}
__device__ __forceinline__ void touch(unsigned lds_addr, unsigned voff, u32x4 srd) {
  asm volatile("s_mov_b32 m0, %0\n\ts_nop 0\n\tbuffer_load_dword %1, %2, 0 offen lds" ::"s"(lds_addr), "v"(voff), "s"(srd) : "memory");
}
__device__ __forceinline__ void st16(u32x4 v, unsigned voff, u32x4 srd, unsigned soff) {
  asm volatile("buffer_store_dwordx4 %0, %1, %2, %3 offen" S_POLICY "\n\ts_nop 1" ::"v"(v), "v"(voff), "s"(srd), "s"(soff) : "memory");
}
__device__ __forceinline__ void st16h(u32x4 v, unsigned voff, u32x4 srd, unsigned soff) {
  asm volatile("buffer_store_dwordx4 %0, %1, %2, %3 offen" H_POLICY "\n\ts_nop 1" ::"v"(v), "v"(voff), "s"(srd), "s"(soff) : "memory");
}
template <int OFF> __device__ __forceinline__ void ldx(u32x4& dst, unsigned voff, unsigned long long base) {
  asm volatile("global_load_dwordx4 %0, %1, %2 offset:%3" X_POLICY : "=v"(dst) : "v"(voff), "s"(base), "i"(OFF));
}
__device__ __forceinline__ void ldg(u32x4& dst, unsigned voff, u32x4 srd) {
  asm volatile("buffer_load_dwordx4 %0, %1, %2, 0 offen" : "=v"(dst) : "v"(voff), "s"(srd));
}
template <int N> __device__ __forceinline__ void wait_vm_for(u32x4& v) { asm volatile("s_waitcnt vmcnt(%1)" : "+v"(v) : "i"(N)); }
__device__ __forceinline__ void ts_raw(unsigned long long& t) { asm volatile("s_memtime %0" : "=s"(t)); }
template <int I> __device__ __forceinline__ void ts_lane(unsigned& tsv, unsigned long long t) { asm volatile("" : "+s"(t)); asm volatile("v_writelane_b32 %0, %1, %2" : "+v"(tsv) : "s"((unsigned)t), "i"(I)); }
__device__ __forceinline__ void probe_now(unsigned long long& t) { asm volatile("s_memrealtime %0\n\ts_waitcnt lgkmcnt(0)" : "=s"(t) :: "memory"); }
template <int I> __device__ __forceinline__ void ts_put(unsigned& tsv) {
  unsigned long long t_; asm volatile("s_memtime %0\n\ts_waitcnt lgkmcnt(0)" : "=s"(t_) :: "memory");
  asm volatile("v_writelane_b32 %0, %1, %2" : "+v"(tsv) : "s"((unsigned)t_), "i"(I));
}
template <int CTRL> __device__ __forceinline__ float dpp_add(float v) {
  return v + __builtin_bit_cast(float, __builtin_amdgcn_update_dpp(0, __builtin_bit_cast(int, v), CTRL, 0xf, 0xf, true));
}
__device__ __forceinline__ float oct_sum(float v) {
  v = dpp_add<0xB1>(v); v = dpp_add<0x4E>(v); v = dpp_add<0x141>(v);
  return v;
}
__device__ __forceinline__ unsigned pack2(f32x2 v) { return __builtin_bit_cast(unsigned, __builtin_convertvector(v, bf16x2)); }
__device__ __forceinline__ f32x2 unpack2(unsigned u) { return f32x2{__uint_as_float(u << 16), __uint_as_float(u & 0xffff0000u)}; }
template <int N> __device__ __forceinline__ void piece_stats(const u32x4& x, float& s, float& q, f32x2 (&y)[4]) {
  float a0, a1, a2, a3, a4, a5, a6, a7;
  const unsigned x0 = x[0], x1 = x[1], x2 = x[2], x3 = x[3];
  asm volatile(
      "s_waitcnt vmcnt(%16)\n\t"
      "v_dot2c_f32_bf16 %0, %14, %10\n\tv_dot2c_f32_bf16 %1, %10, %10\n\t"
      "v_dot2c_f32_bf16 %0, %14, %11\n\tv_dot2c_f32_bf16 %1, %11, %11\n\t"
      "v_dot2c_f32_bf16 %0, %14, %12\n\tv_dot2c_f32_bf16 %1, %12, %12\n\t"
      "v_dot2c_f32_bf16 %0, %14, %13\n\tv_dot2c_f32_bf16 %1, %13, %13\n\t"
      "v_lshlrev_b32 %2, 16, %10\n\tv_and_b32 %3, %15, %10\n\t"
      "v_lshlrev_b32 %4, 16, %11\n\tv_and_b32 %5, %15, %11\n\t"
      "v_lshlrev_b32 %6, 16, %12\n\tv_and_b32 %7, %15, %12\n\t"
      "v_lshlrev_b32 %8, 16, %13\n\tv_and_b32 %9, %15, %13"
      : "+v"(s), "+v"(q), "=&v"(a0), "=&v"(a1), "=&v"(a2), "=&v"(a3), "=&v"(a4), "=&v"(a5), "=&v"(a6), "=&v"(a7)
      : "v"(x0), "v"(x1), "v"(x2), "v"(x3), "s"(0x3f803f80u), "s"(0xffff0000u), "i"(N));
  y[0] = f32x2{a0, a1}; y[1] = f32x2{a2, a3}; y[2] = f32x2{a4, a5}; y[3] = f32x2{a6, a7};
}
__device__ __forceinline__ void count_in(unsigned lds_addr) { asm volatile("s_mov_b64 exec, 1\n\tds_add_u32 %0, %1\n\ts_mov_b64 exec, -1" ::"v"(lds_addr), "v"(1u) : "memory"); }
__device__ __forceinline__ void keep2(unsigned a, unsigned b) { asm volatile("s_waitcnt lgkmcnt(0)" ::"s"(a), "s"(b) : "memory"); }
__device__ __forceinline__ void pinf(float& v) { asm volatile("" : "+v"(v)); }
__device__ __forceinline__ LDS_AS char* opaque(LDS_AS char* p) { asm volatile("" : "+v"(p)); return p; }
template <int MASK> __device__ __forceinline__ float sel8(float v) {
  const int i = __builtin_bit_cast(int, v);
  return __builtin_bit_cast(float, __builtin_amdgcn_update_dpp(i, i, 0x128, 0xf, MASK, false));
}
__device__ __forceinline__ float fold8(float a, float b) {
  float t;
  asm("s_nop 1\n\tv_add_f32_dpp %0, %1, %1 row_ror:8 row_mask:0xf bank_mask:0x3\n\tv_add_f32_dpp %0, %2, %2 row_ror:8 row_mask:0xf bank_mask:0xc" : "=&v"(t) : "v"(a), "v"(b));
  return t;
}

#define TS(i) do { if constexpr ((i) == PROBE) probe_now(tprobe); } while (0)
#define TS2(i) do {} while (0)

__global__ __launch_bounds__(512, 2) void ln_linear_kernel(const uint16_t* __restrict__ x, const uint16_t* __restrict__ g,
                                                           const uint16_t* __restrict__ b, const uint16_t* __restrict__ w,
                                                           uint16_t* __restrict__ out
) {
  extern __shared__ __attribute__((aligned(1024))) char smem[];
  LDS_AS char* lds = (LDS_AS char*)smem;
  unsigned long long kx;
  u32x8 ka;
  asm volatile("s_load_dwordx2 %0, %2, 0x0\n\ts_load_dwordx8 %1, %2, 0x8" : "=&s"(kx), "=&s"(ka) : "s"(__builtin_amdgcn_kernarg_segment_ptr()));
  unsigned long long tprobe = 0;
  if constexpr (PROBE == 0) probe_now(tprobe);
  const int tid = threadIdx.x, lane = tid & 63;
  const unsigned bid = blockIdx.x;
  const unsigned row0 = (bid & 7) * 1024 + (bid >> 4) * 64;
  unsigned xvt = (unsigned)(tid >> 3) * ROWB + (unsigned)(tid & 7) * 16, xvm = (unsigned)(tid >> 4) * 2 * ROWB + (unsigned)(tid & 15) * 16;
  unsigned xo = row0 * ROWB;
  const unsigned t0 = __builtin_amdgcn_readfirstlane(tid);
  asm volatile("" : "+v"(xvt), "+v"(xvm));
  asm volatile("" : "+s"(xo));
  unsigned pf0 = 0, pf1 = 0;
  asm volatile("s_waitcnt lgkmcnt(0)" : "+s"(kx), "+s"(ka));
  if (t0 >= 256) {
    *(LDS_AS unsigned*)(lds + FLAG) = 0u;
    unsigned long long pc;
    unsigned len;
    asm volatile("s_getpc_b64 %0\n.Lpf_here:\n\ts_mov_b32 %1, .Lpf_end-.Lpf_here-128" : "=s"(pc), "=s"(len));
    const unsigned o = min((bid >> 3) * 128u + (t0 & 192u) * 64u, len);
    asm volatile("s_waitcnt lgkmcnt(0)\n\ts_load_dword %0, %2, %3\n\ts_load_dword %1, %2, %3 offset:64\n\ts_barrier" : "=&s"(pf0), "=&s"(pf1) : "s"(pc), "s"(o) : "memory");
  }
  u32x4 xr[9], gq;
  {
    const unsigned long long xb = kx + xo;
    ldx<1024>(xr[8], xvt, xb);
    static_for<0, GQ_POS>([&](auto jc) { constexpr int j = decltype(jc)::v; ldx<(j >> 2) * ROWB + (j & 3) * 256>(xr[j], xvm, xb); });
    fence();
    ldg(gq, (unsigned)(tid & 127) * 16u, make_srd(t0 < 128 ? ka[0] : ka[2], t0 < 128 ? ka[1] : ka[3], t0 < 256 ? (unsigned)ROWB : 0u));
    static_for<GQ_POS, 8>([&](auto jc) { constexpr int j = decltype(jc)::v; ldx<(j >> 2) * ROWB + (j & 3) * 256>(xr[j], xvm, xb); });
    fence();
  }
  const int wv = t0 >> 6, wi = wv & 3;
  const unsigned jb = bid >> 3, h = jb & 1;
  const int r7 = lane >> 3, c7 = lane & 7, c15 = lane & 15, rq = lane >> 4;
  const unsigned ldsbase = (unsigned)(uintptr_t)lds;
  const unsigned rowoff = (unsigned)r7 * ROWB;
  if (wv < 4) barrier();
  const u32x4 srdw = make_srd(ka[4], ka[5], (unsigned)D * ROWB), srdo = make_srd(ka[6], ka[7], 8192u * ROWB);
  touch(ldsbase + SINK, (wv >= 4 && wv < 7 && lane < 18) ? (jb * 18u + (unsigned)lane) * ROWB + (wv == 4 ? 512u : wv == 5 ? 0u : 640u) : 0xfffffff0u, srdw);
  TS(13);

  {
    f32x4 gv[5][2], bv[5][2];
    f32x2 y[9][4];
    LDS_AS char* aw[3] = {opaque(lds + XRES + wv * 1024 + (c15 >> 3) * 8192 + (2 * rq) * 128 + ((c7 ^ (2 * rq)) * 16)),
                          opaque(lds + XRES + wv * 1024 + (c15 >> 3) * 8192 + (2 * rq + 1) * 128 + ((c7 ^ (2 * rq + 1)) * 16)),
                          opaque(lds + XRES + 8 * 8192 + wv * 1024 + r7 * 128 + ((c7 ^ r7) * 16))};
    LDS_AS char* gr = opaque(lds + GBB + c15 * 16);
    LDS_AS char* grt = opaque(lds + GBB + 2048 + c7 * 16);
    float sm[3] = {0.f, 0.f, 0.f}, sq[3] = {0.f, 0.f, 0.f};
#pragma unroll
    for (int i = 0; i < 3; ++i) { pinf(sm[i]); pinf(sq[i]); }
    fence();
    static_for<0, 9>([&](auto jc) {
      constexpr int jj = decltype(jc)::v, j = jj == 0 ? 8 : jj - 1;
      piece_stats<9 - jj + (jj <= GQ_POS ? 1 : 0)>(xr[j], sm[j >> 2], sq[j >> 2], y[j]);
      if constexpr (jj == 0) TS(14);
      if constexpr (jj == GQ_POS) {
        wait_vm_for<9 - jj>(gq);
        if (wv < 4) {
          const unsigned g0 = gq[0], g1 = gq[1], g2 = gq[2], g3 = gq[3];
          const f32x2 a0 = unpack2(g0), a1 = unpack2(g1), a2 = unpack2(g2), a3 = unpack2(g3);
          *(LDS_AS f32x4*)(lds + GBB + wi * 2048 + rq * 512 + c15 * 16) = f32x4{a0[0], a0[1], a1[0], a1[1]};
          *(LDS_AS f32x4*)(lds + GBB + wi * 2048 + rq * 512 + c15 * 16 + 256) = f32x4{a2[0], a2[1], a3[0], a3[1]};
          count_in(ldsbase + FLAG);
        }
        fence();
      }
      if constexpr (jj == GB_PIECE) {
        while (__builtin_amdgcn_readfirstlane(*(volatile LDS_AS unsigned*)(lds + FLAG)) < 4u) __builtin_amdgcn_s_sleep(1);
        keep2(pf0, pf1);
      }
      if constexpr (jj >= GB_PIECE) {
#pragma unroll
        for (int kk = jj - GB_PIECE; kk < (jj == 8 ? 5 : jj - GB_PIECE + 1); ++kk) {
          if (kk < 5) {
            const int k = kk == 0 ? 4 : kk - 1;
            LDS_AS char* p = k < 4 ? gr + k * 512 : grt;
#pragma unroll
            for (int e = 0; e < 2; ++e) { gv[k][e] = *(LDS_AS f32x4*)(p + e * 256); bv[k][e] = *(LDS_AS f32x4*)(p + 4096 + e * 256); }
          }
        }
        fence();
      }
    });
    fence();
    TS(3);
    const float mean = oct_sum(fold8(sm[0], sm[1]) + sm[2]) * (1.0f / D);
    const float var = fmaxf(oct_sum(fold8(sq[0], sq[1]) + sq[2]) * (1.0f / D) - mean * mean, 0.f);
    float rs[3], nm[3];
    rs[2] = __builtin_amdgcn_rsqf(var + 1e-5f);
    nm[2] = -mean * rs[2];
    rs[0] = sel8<0xc>(rs[2]); rs[1] = sel8<0x3>(rs[2]);
    nm[0] = sel8<0xc>(nm[2]); nm[1] = sel8<0x3>(nm[2]);
#pragma unroll
    for (int jj = 0; jj < 9; ++jj) {
      const int j = jj == 0 ? 8 : jj - 1, G = j >> 2, k = j == 8 ? 4 : (j & 3);
#pragma unroll
      for (int e = 0; e < 4; ++e) y[j][e] = __builtin_elementwise_fma(y[j][e], f32x2{rs[G], rs[G]}, f32x2{nm[G], nm[G]});
      fence();
#pragma unroll
      for (int e = 0; e < 4; ++e) {
        const f32x4 gg = gv[k][e >> 1], bb = bv[k][e >> 1];
        y[j][e] = __builtin_elementwise_fma(y[j][e], (e & 1) ? f32x2{gg[2], gg[3]} : f32x2{gg[0], gg[1]}, (e & 1) ? f32x2{bb[2], bb[3]} : f32x2{bb[0], bb[1]});
      }
      fence();
      *(LDS_AS u32x4*)(aw[G] + (j == 8 ? 0 : (j & 3) * 16384)) = u32x4{pack2(y[j][0]), pack2(y[j][1]), pack2(y[j][2]), pack2(y[j][3])};
      fence();
    }
  }
  TS(4);

  const unsigned wvoff = rowoff + (unsigned)(((((c7 >> 1) ^ (((r7 >> 1) & 1) | ((tid >> 5) & 2))) << 1) | (c7 & 1)) * 16);
  const unsigned wbase = (unsigned)h * 640u + (unsigned)wi * 8u * ROWB;
  auto request = [&](unsigned hslot, unsigned so) {
    dma3(ldsbase + hslot * (SLOT / 2) + (unsigned)wi * 1024u, wvoff, srdw, so);
  };

  if (wv >= 4) {
    wait_lgkm<0>();
    barrier();
    TS(6);
    __builtin_amdgcn_s_setprio(LPRIO);
    auto req = [&](auto cc) {
      constexpr int c = decltype(cc)::v;
      unsigned wb = wbase, lb = ldsbase + (unsigned)wi * 1024u;
      asm volatile("" : "+s"(wb), "+s"(lb));
      dma3(lb + ((c + 3) % 12) * (SLOT / 2), wvoff, srdw, wb + (c / 6) * 128u + (c % 6) * 96u * ROWB);
    };
    static_for<NE, 3>(req);
    barrier(); TS2(18); static_for<3, 5>(req);
    barrier(); TS2(21); static_for<5, 6>(req);
    wait_vm<12>();
    barrier(); TS2(24); static_for<6, 8>(req);
    static_for<0, 11>([&](auto qc) {
      constexpr int q = decltype(qc)::v;
      wait_vm<(q < 9 ? 12 : q == 9 ? 6 : 0)>();
      barrier();
      TS2(27 + 3 * q);
      if constexpr (q < 8) static_for<2 * q + 8, 2 * q + 10>(req);
    });
    TS(8);
  } else {
    {
      const unsigned hv = (unsigned)(lane >> 2) * ROWB + (unsigned)(((lane & 3) ^ ((lane >> 4) & 2)) * 16);
      const unsigned hs = 512u + (unsigned)h * 64u + (unsigned)wi * 16u * ROWB;
#pragma unroll
      for (int i = 0; i < 9; ++i) dma16(ldsbase + (i * 4 + wi) * 1024, hv, srdw, hs + (unsigned)(i * 64) * ROWB);
#pragma unroll
      for (int c = 0; c < NE; ++c) request(c + 3, wbase + c * 96u * ROWB);
      const unsigned li = (unsigned)tid, kk = (li * 43u) >> 8, ln = li - kk * 6u;
      touch(ldsbase + SINK, li < 108u ? ((unsigned)jb * 18u + kk) * ROWB + (ln + 1u + (ln >= 3u) * 2u) * 128u : 0xfffffff0u, srdw);
    }
    const int m = wi >> 1, n = wi & 1;
    const int fr = lane & 15, fg = lane >> 4;
    bf16x8 xa[2][18];
    LDS_AS char* ard[2][2];
#pragma unroll
    for (int e = 0; e < 2; ++e) {
      const unsigned o = (m * 2 + (fr >> 3)) * 1024 + (fr & 7) * 128 + (((4 * e + fg) ^ (fr & 7)) * 16);
      ard[e][0] = opaque(lds + XRES + o);
      ard[e][1] = opaque(lds + XRES + 5 * 8192 + o);
    }
    LDS_AS char* wrd[2][3];
#pragma unroll
    for (int t = 0; t < 2; ++t)
#pragma unroll
      for (int z = 0; z < 3; ++z)
        wrd[t][z] = opaque(lds + z * 2 * SLOT + (8 * fg + (fr >> 2)) * 128 + (((2 * n + t) ^ (((fr >> 3) & 1) | ((fg & 1) << 1))) * 32) + (fr & 3) * 8);
    LDS_AS char* wh = opaque(lds + (8 * fg + (fr >> 2)) * 64 + ((n ^ (fg & 1)) * 32) + (fr & 3) * 8);
    const unsigned ovoff = (unsigned)fr * ROWB + (unsigned)(2 * (fg & 1) + (fg >> 1)) * 16u;
    const unsigned orow = (unsigned)(row0 + m * 16) * ROWB;
    const unsigned osoff = orow + (unsigned)h * 640u + (unsigned)n * 64u;
    wait_lgkm<0>();
    wait_vm<6 + NE * 3 + 1>();
    TS(7);
    barrier();
    TS(6);

    bf16x8 wf[2][6];
    f32x4 acc[2][2], fin[2][2];
    u32x4 po[4][2];
    auto tr_frag = [&](LDS_AS char* p, int step) {
      const s16x4 lo = __builtin_amdgcn_ds_read_tr16_b64_v4i16((LDS_AS s16x4*)p);
      const s16x4 hi = __builtin_amdgcn_ds_read_tr16_b64_v4i16((LDS_AS s16x4*)(p + step));
      return __builtin_bit_cast(bf16x8, __builtin_shufflevector(lo, hi, 0, 1, 2, 3, 4, 5, 6, 7));
    };
    auto rd = [&](auto uc) {
      constexpr int u = decltype(uc)::v, hsl = u % 12;
      if constexpr (u >= 3) {
#pragma unroll
        for (int s = 0; s < 3; ++s)
#pragma unroll
          for (int t = 0; t < 2; ++t) wf[u & 1][s * 2 + t] = tr_frag(wrd[t][hsl >> 2] + (hsl & 3) * (SLOT / 2) + s * 4096, 512);
      } else {
#pragma unroll
        for (int s = 0; s < 6; ++s) {
          const int ks = u * 6 + s;
          wf[u & 1][s] = tr_frag(wh + ks * 2048, 256);
#pragma unroll
          for (int i = 0; i < 2; ++i) xa[i][ks] = *(LDS_AS bf16x8*)(ard[ks & 1][ks >= 10] + ((ks >> 1) % 5) * 8192 + i * 4096);
        }
      }
    };
    auto mm = [&](auto uc) {
      constexpr int u = decltype(uc)::v;
#pragma unroll
      for (int f = 0; f < 6; ++f) {
        const int ks = u >= 3 ? ((u - 3) % 6) * 3 + f / 2 : u * 6 + f, t = f & 1;
        const bool first = u >= 3 ? ks == 0 : ks < 2;
#pragma unroll
        for (int i = 0; i < 2; ++i) acc[i][t] = __builtin_amdgcn_mfma_f32_16x16x32_bf16(wf[u & 1][f], xa[i][ks], first ? f32x4{0.f, 0.f, 0.f, 0.f} : acc[i][t], 0, 0, 0);
      }
    };
    auto pack = [&](const f32x4& a, const f32x4& c) {
      const auto lo = __builtin_amdgcn_permlane16_swap(pack2(f32x2{a[0], a[1]}), pack2(f32x2{c[0], c[1]}), false, false);
      const auto hi = __builtin_amdgcn_permlane16_swap(pack2(f32x2{a[2], a[3]}), pack2(f32x2{c[2], c[3]}), false, false);
      return u32x4{(unsigned)lo[0], (unsigned)hi[0], (unsigned)lo[1], (unsigned)hi[1]};
    };
    const unsigned hvoff = (unsigned)(fr + (fg & 1) * 32) * ROWB + (unsigned)(fg >> 1) * 16u;
    u32x4 ph;
    auto store = [&](int k) {
      if (k == 0) st16h(ph, hvoff, srdo, orow + 512u + (unsigned)h * 64u + (unsigned)n * 32u);
      else st16(po[(k - 1) / 2][(k - 1) & 1], ovoff, srdo, osoff + ((k - 1) / 2) * 128u + ((k - 1) & 1) * 32u * ROWB);
    };
    rd(IC<0>{});
    fence();
    static_for<0, 27>([&](auto uc) {
      constexpr int u = decltype(uc)::v, v = u - 3;
      constexpr bool pk = v >= 6 && v % 6 < 2;
      if constexpr (u < 3) {
        wait_vm<(u == 0 ? 3 + NE * 3 + 1 : u == 1 ? NE * 3 + 1 : (NE > 2 ? 3 : 0) + 1)>();
        wait_lgkm<0>();
        barrier();
        fence();
      } else if constexpr ((v & 1) && u < 26) {
        if constexpr (v == 1) wait_vm<1>();
        barrier();
        fence();
      }
      if constexpr (u < 26) rd(IC<u + 1>{});
      if constexpr (pk) po[v / 6 - 1][v % 6] = pack(fin[v % 6][0], fin[v % 6][1]);
      if constexpr (u == 3) ph = pack(fin[0][0] + fin[0][1], fin[1][0] + fin[1][1]);
      mm(uc);
      if constexpr (u < 26) {
        static_for<0, 12>([&](auto ic) {
          constexpr int i = decltype(ic)::v;
          __builtin_amdgcn_sched_group_barrier(0x008, 1, 0);
          if constexpr (u < 2) { if constexpr (i < 8) __builtin_amdgcn_sched_group_barrier(0x100, 3, 0); }
          else __builtin_amdgcn_sched_group_barrier(0x100, 1, 0);
          if constexpr ((pk && i >= 2 && i < 8) || (u == 3 && i >= 2)) __builtin_amdgcn_sched_group_barrier(0x002, 1, 0);
        });
      }
      fence();
      if constexpr (store_at(u) >= 0) { store(store_at(u)); fence(); }
      if constexpr (u == 2 || (v >= 0 && v % 6 == 5)) {
#pragma unroll
        for (int i = 0; i < 2; ++i) { fin[i][0] = acc[i][0]; fin[i][1] = acc[i][1]; }
      }
    });
    po[3][0] = pack(fin[0][0], fin[0][1]);
    po[3][1] = pack(fin[1][0], fin[1][1]);
    store(7);
    store(8);
    TS(9);
  }
  if constexpr (PROBE == 11) { asm volatile("s_waitcnt vmcnt(0)" ::: "memory"); probe_now(tprobe); }
  if (wv == PROBE_WAVE) { while (__builtin_amdgcn_s_memrealtime() < tprobe + 4000ull) __builtin_amdgcn_s_sleep(1); }
  asm volatile(".Lpf_end:");
}
}

extern "C" void kernel_launch(void* const* d_in, const int* in_sizes, int n_in, void* d_out, int out_size, void* d_ws,
                              size_t ws_size, hipStream_t stream) {
  (void)in_sizes; (void)n_in; (void)out_size; (void)d_ws; (void)ws_size;
  (void)hipFuncSetAttribute((const void*)ln_linear_kernel, hipFuncAttributeMaxDynamicSharedMemorySize, LDS_BYTES);
  ln_linear_kernel<<<256, 512, LDS_BYTES, stream>>>((const uint16_t*)d_in[0], (const uint16_t*)d_in[1], (const uint16_t*)d_in[2],
                                                    (const uint16_t*)d_in[3], (uint16_t*)d_out
  );
}
```

```cpp
#include <hip/hip_runtime.h>
#include <stdint.h>

typedef short s16x4 __attribute__((ext_vector_type(4)));
typedef __bf16 bf16x2 __attribute__((ext_vector_type(2)));
typedef __bf16 bf16x8 __attribute__((ext_vector_type(8)));
typedef float f32x2 __attribute__((ext_vector_type(2)));
typedef float f32x4 __attribute__((ext_vector_type(4)));
typedef unsigned u32x4 __attribute__((ext_vector_type(4)));
typedef unsigned u32x8 __attribute__((ext_vector_type(8)));
#define LDS_AS __attribute__((address_space(3)))

namespace {
constexpr int D = 576;
constexpr int ROWS = 8192;
constexpr int ROWB = D * 2;
constexpr int HSLOT = 12288;
constexpr int XRES = 6 * HSLOT;
constexpr int GBB = 12 * HSLOT;
constexpr int FLAG = GBB + 8192;
constexpr int SINK = FLAG + 1024;
constexpr int LDS_BYTES = SINK + 1024;
constexpr int GQ_POS = 1;
constexpr int GB_PIECE = 5;
constexpr int ST_FROM = 19;
constexpr unsigned OOB = 0xfffffff0u;

template <int V> struct IC { static constexpr int v = V; };
template <int I, int N, class F> __device__ __forceinline__ void static_for(F&& f) {
  if constexpr (I < N) { f(IC<I>{}); static_for<I + 1, N>(f); }
}
__device__ __forceinline__ u32x4 make_srd(unsigned lo, unsigned hi, unsigned bytes) { return u32x4{lo, hi & 0xffffu, bytes, 0x00020000u}; }
template <int N> __device__ __forceinline__ void wait_vm() { asm volatile("s_waitcnt vmcnt(%0)" ::"i"(N) : "memory"); }
template <int N> __device__ __forceinline__ void wait_vm_for(u32x4& v) { asm volatile("s_waitcnt vmcnt(%1)" : "+v"(v) : "i"(N)); }
__device__ __forceinline__ void wait_lgkm0() { asm volatile("s_waitcnt lgkmcnt(0)" ::: "memory"); }
__device__ __forceinline__ void barrier() { asm volatile("s_barrier" ::: "memory"); }
__device__ __forceinline__ void fence() { __builtin_amdgcn_sched_barrier(0); }
__device__ __forceinline__ void dma16(unsigned lds_addr, unsigned voff, u32x4 srd, unsigned soff) {
  asm volatile("s_mov_b32 m0, %0\n\ts_nop 0\n\tbuffer_load_dwordx4 %1, %2, %3 offen lds" ::"s"(lds_addr), "v"(voff), "s"(srd), "s"(soff) : "memory");
}
__device__ __forceinline__ void dma3(unsigned lds_addr, unsigned voff, u32x4 srd, unsigned soff) {
  unsigned s1, s2;
  asm volatile("s_mov_b32 m0, %2\n\ts_add_u32 %0, %5, 0x9000\n\tbuffer_load_dwordx4 %3, %4, %5 offen lds\n\t"
               "s_add_u32 m0, m0, 0x1000\n\ts_add_u32 %1, %5, 0x12000\n\tbuffer_load_dwordx4 %3, %4, %0 offen lds\n\t"
               "s_add_u32 m0, m0, 0x1000\n\ts_nop 0\n\tbuffer_load_dwordx4 %3, %4, %1 offen lds"
               : "=&s"(s1), "=&s"(s2) : "s"(lds_addr), "v"(voff), "s"(srd), "s"(soff) : "memory", "scc");
}
__device__ __forceinline__ void touch(unsigned lds_addr, unsigned voff, u32x4 srd) {
  asm volatile("s_mov_b32 m0, %0\n\ts_nop 0\n\tbuffer_load_dword %1, %2, 0 offen lds" ::"s"(lds_addr), "v"(voff), "s"(srd) : "memory");
}
__device__ __forceinline__ void st16_through(u32x4 v, unsigned voff, u32x4 srd, unsigned soff) {
  asm volatile("buffer_store_dwordx4 %0, %1, %2, %3 offen sc1\n\ts_nop 1" ::"v"(v), "v"(voff), "s"(srd), "s"(soff) : "memory");
}
__device__ __forceinline__ void st16(u32x4 v, unsigned voff, u32x4 srd, unsigned soff) {
  asm volatile("buffer_store_dwordx4 %0, %1, %2, %3 offen\n\ts_nop 1" ::"v"(v), "v"(voff), "s"(srd), "s"(soff) : "memory");
}
template <int OFF> __device__ __forceinline__ void ldx(u32x4& dst, unsigned voff, unsigned long long base) {
  asm volatile("global_load_dwordx4 %0, %1, %2 offset:%3" : "=v"(dst) : "v"(voff), "s"(base), "i"(OFF));
}
__device__ __forceinline__ void ldg(u32x4& dst, unsigned voff, u32x4 srd) {
  asm volatile("buffer_load_dwordx4 %0, %1, %2, 0 offen" : "=v"(dst) : "v"(voff), "s"(srd));
}
template <int CTRL> __device__ __forceinline__ float dpp_add(float v) {
  return v + __builtin_bit_cast(float, __builtin_amdgcn_update_dpp(0, __builtin_bit_cast(int, v), CTRL, 0xf, 0xf, true));
}
__device__ __forceinline__ float oct_sum(float v) {
  v = dpp_add<0xB1>(v); v = dpp_add<0x4E>(v); v = dpp_add<0x141>(v);
  return v;
}
__device__ __forceinline__ unsigned pack2(f32x2 v) { return __builtin_bit_cast(unsigned, __builtin_convertvector(v, bf16x2)); }
__device__ __forceinline__ f32x2 unpack2(unsigned u) { return f32x2{__uint_as_float(u << 16), __uint_as_float(u & 0xffff0000u)}; }
template <int N> __device__ __forceinline__ void piece_stats(const u32x4& x, float& s, float& q, f32x2 (&y)[4]) {
  float a0, a1, a2, a3, a4, a5, a6, a7;
  const unsigned x0 = x[0], x1 = x[1], x2 = x[2], x3 = x[3];
  asm volatile(
      "s_waitcnt vmcnt(%16)\n\t"
      "v_dot2c_f32_bf16 %0, %14, %10\n\tv_dot2c_f32_bf16 %1, %10, %10\n\t"
      "v_dot2c_f32_bf16 %0, %14, %11\n\tv_dot2c_f32_bf16 %1, %11, %11\n\t"
      "v_dot2c_f32_bf16 %0, %14, %12\n\tv_dot2c_f32_bf16 %1, %12, %12\n\t"
      "v_dot2c_f32_bf16 %0, %14, %13\n\tv_dot2c_f32_bf16 %1, %13, %13\n\t"
      "v_lshlrev_b32 %2, 16, %10\n\tv_and_b32 %3, %15, %10\n\t"
      "v_lshlrev_b32 %4, 16, %11\n\tv_and_b32 %5, %15, %11\n\t"
      "v_lshlrev_b32 %6, 16, %12\n\tv_and_b32 %7, %15, %12\n\t"
      "v_lshlrev_b32 %8, 16, %13\n\tv_and_b32 %9, %15, %13"
      : "+v"(s), "+v"(q), "=&v"(a0), "=&v"(a1), "=&v"(a2), "=&v"(a3), "=&v"(a4), "=&v"(a5), "=&v"(a6), "=&v"(a7)
      : "v"(x0), "v"(x1), "v"(x2), "v"(x3), "s"(0x3f803f80u), "s"(0xffff0000u), "i"(N));
  y[0] = f32x2{a0, a1}; y[1] = f32x2{a2, a3}; y[2] = f32x2{a4, a5}; y[3] = f32x2{a6, a7};
}
__device__ __forceinline__ void count_in(unsigned lds_addr) {
  asm volatile("s_mov_b64 exec, 1\n\tds_add_u32 %0, %1\n\ts_mov_b64 exec, -1" ::"v"(lds_addr), "v"(1u) : "memory");
}
__device__ __forceinline__ void retire2(unsigned a, unsigned b) { asm volatile("s_waitcnt lgkmcnt(0)" ::"s"(a), "s"(b) : "memory"); }
__device__ __forceinline__ void pinf(float& v) { asm volatile("" : "+v"(v)); }
__device__ __forceinline__ LDS_AS char* opaque(LDS_AS char* p) { asm volatile("" : "+v"(p)); return p; }
template <int MASK> __device__ __forceinline__ float sel8(float v) {
  const int i = __builtin_bit_cast(int, v);
  return __builtin_bit_cast(float, __builtin_amdgcn_update_dpp(i, i, 0x128, 0xf, MASK, false));
}
__device__ __forceinline__ float fold8(float a, float b) {
  float t;
  asm("s_nop 1\n\tv_add_f32_dpp %0, %1, %1 row_ror:8 row_mask:0xf bank_mask:0x3\n\tv_add_f32_dpp %0, %2, %2 row_ror:8 row_mask:0xf bank_mask:0xc" : "=&v"(t) : "v"(a), "v"(b));
  return t;
}

__global__ __launch_bounds__(512, 2) void ln_linear_kernel(const uint16_t* __restrict__ x, const uint16_t* __restrict__ g,
                                                           const uint16_t* __restrict__ b, const uint16_t* __restrict__ w,
                                                           uint16_t* __restrict__ out) {
  extern __shared__ __attribute__((aligned(1024))) char smem[];
  LDS_AS char* lds = (LDS_AS char*)smem;
  unsigned long long kx;
  u32x8 ka;
  asm volatile("s_load_dwordx2 %0, %2, 0x0\n\ts_load_dwordx8 %1, %2, 0x8" : "=&s"(kx), "=&s"(ka) : "s"(__builtin_amdgcn_kernarg_segment_ptr()));
  const int tid = threadIdx.x, lane = tid & 63;
  const unsigned bid = blockIdx.x;
  const unsigned row0 = (bid & 7) * 1024 + (bid >> 4) * 64;
  unsigned xvt = (unsigned)(tid >> 3) * ROWB + (unsigned)(tid & 7) * 16, xvm = (unsigned)(tid >> 4) * 2 * ROWB + (unsigned)(tid & 15) * 16;
  unsigned xo = row0 * ROWB;
  const unsigned t0 = __builtin_amdgcn_readfirstlane(tid);
  asm volatile("" : "+v"(xvt), "+v"(xvm));
  asm volatile("" : "+s"(xo));
  unsigned pf0 = 0, pf1 = 0;
  asm volatile("s_waitcnt lgkmcnt(0)" : "+s"(kx), "+s"(ka));
  if (t0 >= 256) {
    *(LDS_AS unsigned*)(lds + FLAG) = 0u;
    unsigned long long pc;
    unsigned len;
    asm volatile("s_getpc_b64 %0\n.Lpf_here:\n\ts_mov_b32 %1, .Lpf_end-.Lpf_here-128" : "=s"(pc), "=s"(len));
    const unsigned o = min((bid >> 3) * 128u + (t0 & 192u) * 64u, len);
    asm volatile("s_waitcnt lgkmcnt(0)\n\ts_load_dword %0, %2, %3\n\ts_load_dword %1, %2, %3 offset:64\n\ts_barrier" : "=&s"(pf0), "=&s"(pf1) : "s"(pc), "s"(o) : "memory");
  }
  u32x4 xr[9], gq;
  {
    const unsigned long long xb = kx + xo;
    ldx<1024>(xr[8], xvt, xb);
    static_for<0, GQ_POS>([&](auto jc) { constexpr int j = decltype(jc)::v; ldx<(j >> 2) * ROWB + (j & 3) * 256>(xr[j], xvm, xb); });
    fence();
    ldg(gq, (unsigned)(tid & 127) * 16u, make_srd(t0 < 128 ? ka[0] : ka[2], t0 < 128 ? ka[1] : ka[3], t0 < 256 ? (unsigned)ROWB : 0u));
    static_for<GQ_POS, 8>([&](auto jc) { constexpr int j = decltype(jc)::v; ldx<(j >> 2) * ROWB + (j & 3) * 256>(xr[j], xvm, xb); });
    fence();
  }
  const int wv = t0 >> 6, wi = wv & 3;
  const unsigned jb = bid >> 3, h = jb & 1;
  const int r7 = lane >> 3, c7 = lane & 7, c15 = lane & 15, rq = lane >> 4;
  const unsigned ldsbase = (unsigned)(uintptr_t)lds;
  if (wv < 4) barrier();
  const u32x4 srdw = make_srd(ka[4], ka[5], (unsigned)D * ROWB), srdo = make_srd(ka[6], ka[7], (unsigned)ROWS * ROWB);
  touch(ldsbase + SINK, (wv >= 4 && wv < 7 && lane < 18) ? (jb * 18u + (unsigned)lane) * ROWB + (wv == 4 ? 512u : wv == 5 ? 0u : 640u) : OOB, srdw);

  {
    f32x4 gv[5][2], bv[5][2];
    f32x2 y[9][4];
    LDS_AS char* aw[3] = {opaque(lds + XRES + wv * 1024 + (c15 >> 3) * 8192 + (2 * rq) * 128 + ((c7 ^ (2 * rq)) * 16)),
                          opaque(lds + XRES + wv * 1024 + (c15 >> 3) * 8192 + (2 * rq + 1) * 128 + ((c7 ^ (2 * rq + 1)) * 16)),
                          opaque(lds + XRES + 8 * 8192 + wv * 1024 + r7 * 128 + ((c7 ^ r7) * 16))};
    LDS_AS char* gr = opaque(lds + GBB + c15 * 16);
    LDS_AS char* grt = opaque(lds + GBB + 2048 + c7 * 16);
    float sm[3] = {0.f, 0.f, 0.f}, sq[3] = {0.f, 0.f, 0.f};
#pragma unroll
    for (int i = 0; i < 3; ++i) { pinf(sm[i]); pinf(sq[i]); }
    fence();
    static_for<0, 9>([&](auto jc) {
      constexpr int jj = decltype(jc)::v, j = jj == 0 ? 8 : jj - 1;
      piece_stats<9 - jj + (jj <= GQ_POS ? 1 : 0)>(xr[j], sm[j >> 2], sq[j >> 2], y[j]);
      if constexpr (jj == GQ_POS) {
        wait_vm_for<9 - jj>(gq);
        if (wv < 4) {
          const unsigned g0 = gq[0], g1 = gq[1], g2 = gq[2], g3 = gq[3];
          const f32x2 a0 = unpack2(g0), a1 = unpack2(g1), a2 = unpack2(g2), a3 = unpack2(g3);
          *(LDS_AS f32x4*)(lds + GBB + wi * 2048 + rq * 512 + c15 * 16) = f32x4{a0[0], a0[1], a1[0], a1[1]};
          *(LDS_AS f32x4*)(lds + GBB + wi * 2048 + rq * 512 + c15 * 16 + 256) = f32x4{a2[0], a2[1], a3[0], a3[1]};
          count_in(ldsbase + FLAG);
        }
        fence();
      }
      if constexpr (jj == GB_PIECE) {
        while (__builtin_amdgcn_readfirstlane(*(volatile LDS_AS unsigned*)(lds + FLAG)) < 4u) __builtin_amdgcn_s_sleep(1);
        retire2(pf0, pf1);
      }
      if constexpr (jj >= GB_PIECE) {
#pragma unroll
        for (int kk = jj - GB_PIECE; kk < (jj == 8 ? 5 : jj - GB_PIECE + 1); ++kk) {
          const int k = kk == 0 ? 4 : kk - 1;
          LDS_AS char* p = k < 4 ? gr + k * 512 : grt;
#pragma unroll
          for (int e = 0; e < 2; ++e) { gv[k][e] = *(LDS_AS f32x4*)(p + e * 256); bv[k][e] = *(LDS_AS f32x4*)(p + 4096 + e * 256); }
        }
        fence();
      }
    });
    fence();
    const float mean = oct_sum(fold8(sm[0], sm[1]) + sm[2]) * (1.0f / D);
    const float var = fmaxf(oct_sum(fold8(sq[0], sq[1]) + sq[2]) * (1.0f / D) - mean * mean, 0.f);
    float rs[3], nm[3];
    rs[2] = __builtin_amdgcn_rsqf(var + 1e-5f);
    nm[2] = -mean * rs[2];
    rs[0] = sel8<0xc>(rs[2]); rs[1] = sel8<0x3>(rs[2]);
    nm[0] = sel8<0xc>(nm[2]); nm[1] = sel8<0x3>(nm[2]);
#pragma unroll
    for (int jj = 0; jj < 9; ++jj) {
      const int j = jj == 0 ? 8 : jj - 1, G = j >> 2, k = j == 8 ? 4 : (j & 3);
#pragma unroll
      for (int e = 0; e < 4; ++e) y[j][e] = __builtin_elementwise_fma(y[j][e], f32x2{rs[G], rs[G]}, f32x2{nm[G], nm[G]});
      fence();
#pragma unroll
      for (int e = 0; e < 4; ++e) {
        const f32x4 gg = gv[k][e >> 1], bb = bv[k][e >> 1];
        y[j][e] = __builtin_elementwise_fma(y[j][e], (e & 1) ? f32x2{gg[2], gg[3]} : f32x2{gg[0], gg[1]}, (e & 1) ? f32x2{bb[2], bb[3]} : f32x2{bb[0], bb[1]});
      }
      fence();
      *(LDS_AS u32x4*)(aw[G] + (j == 8 ? 0 : (j & 3) * 16384)) = u32x4{pack2(y[j][0]), pack2(y[j][1]), pack2(y[j][2]), pack2(y[j][3])};
      fence();
    }
  }

  const unsigned wvoff = (unsigned)r7 * ROWB + (unsigned)(((((c7 >> 1) ^ (((r7 >> 1) & 1) | ((tid >> 5) & 2))) << 1) | (c7 & 1)) * 16);
  const unsigned wbase = h * 640u + (unsigned)wi * 8u * ROWB;

  if (wv >= 4) {
    wait_lgkm0();
    barrier();
    __builtin_amdgcn_s_setprio(2);
    auto req = [&](auto cc) {
      constexpr int c = decltype(cc)::v;
      unsigned wb = wbase, lb = ldsbase + (unsigned)wi * 1024u;
      asm volatile("" : "+s"(wb), "+s"(lb));
      dma3(lb + ((c + 3) % 12) * HSLOT, wvoff, srdw, wb + (c / 6) * 128u + (c % 6) * 96u * ROWB);
    };
    barrier(); static_for<3, 5>(req);
    barrier(); static_for<5, 6>(req);
    barrier(); static_for<6, 8>(req);
    static_for<0, 11>([&](auto qc) {
      constexpr int q = decltype(qc)::v;
      wait_vm<(q < 9 ? 12 : q == 9 ? 6 : 0)>();
      barrier();
      if constexpr (q < 8) static_for<2 * q + 8, 2 * q + 10>(req);
    });
  } else {
    {
      const unsigned hv = (unsigned)(lane >> 2) * ROWB + (unsigned)(((lane & 3) ^ ((lane >> 4) & 2)) * 16);
      const unsigned hs = 512u + h * 64u + (unsigned)wi * 16u * ROWB;
#pragma unroll
      for (int i = 0; i < 9; ++i) dma16(ldsbase + (i * 4 + wi) * 1024, hv, srdw, hs + (unsigned)(i * 64) * ROWB);
#pragma unroll
      for (int c = 0; c < 3; ++c) dma3(ldsbase + (c + 3) * HSLOT + (unsigned)wi * 1024u, wvoff, srdw, wbase + c * 96u * ROWB);
      const unsigned li = (unsigned)tid, kk = (li * 43u) >> 8, ln = li - kk * 6u;
      touch(ldsbase + SINK, li < 108u ? (jb * 18u + kk) * ROWB + (ln + 1u + (ln >= 3u) * 2u) * 128u : OOB, srdw);
    }
    const int m = wi >> 1, n = wi & 1;
    const int fr = lane & 15, fg = lane >> 4;
    bf16x8 xa[2][18];
    LDS_AS char* ard[2][2];
#pragma unroll
    for (int e = 0; e < 2; ++e) {
      const unsigned o = (m * 2 + (fr >> 3)) * 1024 + (fr & 7) * 128 + (((4 * e + fg) ^ (fr & 7)) * 16);
      ard[e][0] = opaque(lds + XRES + o);
      ard[e][1] = opaque(lds + XRES + 5 * 8192 + o);
    }
    LDS_AS char* wrd[2][3];
#pragma unroll
    for (int t = 0; t < 2; ++t)
#pragma unroll
      for (int z = 0; z < 3; ++z)
        wrd[t][z] = opaque(lds + z * 4 * HSLOT + (8 * fg + (fr >> 2)) * 128 + (((2 * n + t) ^ (((fr >> 3) & 1) | ((fg & 1) << 1))) * 32) + (fr & 3) * 8);
    LDS_AS char* wh = opaque(lds + (8 * fg + (fr >> 2)) * 64 + ((n ^ (fg & 1)) * 32) + (fr & 3) * 8);
    const unsigned ovoff = (unsigned)fr * ROWB + (unsigned)(2 * (fg & 1) + (fg >> 1)) * 16u;
    const unsigned orow = (row0 + m * 16) * ROWB;
    const unsigned osoff = orow + h * 640u + (unsigned)n * 64u;
    wait_lgkm0();
    wait_vm<16>();
    barrier();

    bf16x8 wf[2][6];
    f32x4 acc[2][2], fin[2][2];
    u32x4 po[4][2], ph;
    auto tr_frag = [&](LDS_AS char* p, int step) {
      const s16x4 lo = __builtin_amdgcn_ds_read_tr16_b64_v4i16((LDS_AS s16x4*)p);
      const s16x4 hi = __builtin_amdgcn_ds_read_tr16_b64_v4i16((LDS_AS s16x4*)(p + step));
      return __builtin_bit_cast(bf16x8, __builtin_shufflevector(lo, hi, 0, 1, 2, 3, 4, 5, 6, 7));
    };
    auto rd = [&](auto uc) {
      constexpr int u = decltype(uc)::v, hsl = u % 12;
      if constexpr (u >= 3) {
#pragma unroll
        for (int s = 0; s < 3; ++s)
#pragma unroll
          for (int t = 0; t < 2; ++t) wf[u & 1][s * 2 + t] = tr_frag(wrd[t][hsl >> 2] + (hsl & 3) * HSLOT + s * 4096, 512);
      } else {
#pragma unroll
        for (int s = 0; s < 6; ++s) {
          const int ks = u * 6 + s;
          wf[u & 1][s] = tr_frag(wh + ks * 2048, 256);
#pragma unroll
          for (int i = 0; i < 2; ++i) xa[i][ks] = *(LDS_AS bf16x8*)(ard[ks & 1][ks >= 10] + ((ks >> 1) % 5) * 8192 + i * 4096);
        }
      }
    };
    auto mm = [&](auto uc) {
      constexpr int u = decltype(uc)::v;
#pragma unroll
      for (int f = 0; f < 6; ++f) {
        const int ks = u >= 3 ? ((u - 3) % 6) * 3 + f / 2 : u * 6 + f, t = f & 1;
        const bool first = u >= 3 ? ks == 0 : ks < 2;
#pragma unroll
        for (int i = 0; i < 2; ++i)
          acc[i][t] = __builtin_amdgcn_mfma_f32_16x16x32_bf16(wf[u & 1][f], xa[i][ks], first ? f32x4{0.f, 0.f, 0.f, 0.f} : acc[i][t], 0, 0, 0);
      }
    };
    auto pack = [&](const f32x4& a, const f32x4& c) {
      const auto lo = __builtin_amdgcn_permlane16_swap(pack2(f32x2{a[0], a[1]}), pack2(f32x2{c[0], c[1]}), false, false);
      const auto hi = __builtin_amdgcn_permlane16_swap(pack2(f32x2{a[2], a[3]}), pack2(f32x2{c[2], c[3]}), false, false);
      return u32x4{(unsigned)lo[0], (unsigned)hi[0], (unsigned)lo[1], (unsigned)hi[1]};
    };
    const unsigned hvoff = (unsigned)(fr + (fg & 1) * 32) * ROWB + (unsigned)(fg >> 1) * 16u;
    auto store = [&](int k) {
      if (k == 0) st16(ph, hvoff, srdo, orow + 512u + h * 64u + (unsigned)n * 32u);
      else st16_through(po[(k - 1) / 2][(k - 1) & 1], ovoff, srdo, osoff + ((k - 1) / 2) * 128u + ((k - 1) & 1) * 32u * ROWB);
    };
    rd(IC<0>{});
    fence();
    static_for<0, 27>([&](auto uc) {
      constexpr int u = decltype(uc)::v, v = u - 3;
      constexpr bool pk = v >= 6 && v % 6 < 2;
      if constexpr (u < 3) {
        wait_vm<(u == 0 ? 13 : u == 1 ? 10 : 4)>();
        wait_lgkm0();
        barrier();
        fence();
      } else if constexpr ((v & 1) && u < 26) {
        if constexpr (v == 1) wait_vm<1>();
        barrier();
        fence();
      }
      if constexpr (u < 26) rd(IC<u + 1>{});
      if constexpr (pk) po[v / 6 - 1][v % 6] = pack(fin[v % 6][0], fin[v % 6][1]);
      if constexpr (u == 3) ph = pack(fin[0][0] + fin[0][1], fin[1][0] + fin[1][1]);
      mm(uc);
      if constexpr (u < 26) {
        static_for<0, 12>([&](auto ic) {
          constexpr int i = decltype(ic)::v;
          __builtin_amdgcn_sched_group_barrier(0x008, 1, 0);
          if constexpr (u < 2) { if constexpr (i < 8) __builtin_amdgcn_sched_group_barrier(0x100, 3, 0); }
          else __builtin_amdgcn_sched_group_barrier(0x100, 1, 0);
          if constexpr ((pk && i >= 2 && i < 8) || (u == 3 && i >= 2)) __builtin_amdgcn_sched_group_barrier(0x002, 1, 0);
        });
      }
      fence();
      if constexpr (u >= ST_FROM && u < ST_FROM + 7) { store(u - ST_FROM); fence(); }
      if constexpr (u == 2 || (v >= 0 && v % 6 == 5)) {
#pragma unroll
        for (int i = 0; i < 2; ++i) { fin[i][0] = acc[i][0]; fin[i][1] = acc[i][1]; }
      }
    });
    po[3][0] = pack(fin[0][0], fin[0][1]);
    po[3][1] = pack(fin[1][0], fin[1][1]);
    store(7);
    store(8);
  }
  asm volatile(".Lpf_end:");
}
}

extern "C" void kernel_launch(void* const* d_in, const int* in_sizes, int n_in, void* d_out, int out_size, void* d_ws,
                              size_t ws_size, hipStream_t stream) {
  (void)in_sizes; (void)n_in; (void)out_size; (void)d_ws; (void)ws_size;
  (void)hipFuncSetAttribute((const void*)ln_linear_kernel, hipFuncAttributeMaxDynamicSharedMemorySize, LDS_BYTES);
  ln_linear_kernel<<<256, 512, LDS_BYTES, stream>>>((const uint16_t*)d_in[0], (const uint16_t*)d_in[1], (const uint16_t*)d_in[2],
                                                    (const uint16_t*)d_in[3], (uint16_t*)d_out);
}
```

```cpp
#define PROBE 0
#define PROBE_WAVE 0
#include <hip/hip_runtime.h>
#include <stdint.h>

typedef short s16x4 __attribute__((ext_vector_type(4)));
typedef __bf16 bf16x2 __attribute__((ext_vector_type(2)));
typedef __bf16 bf16x8 __attribute__((ext_vector_type(8)));
typedef float f32x2 __attribute__((ext_vector_type(2)));
typedef float f32x4 __attribute__((ext_vector_type(4)));
typedef unsigned u32x2 __attribute__((ext_vector_type(2)));
typedef unsigned u32x4 __attribute__((ext_vector_type(4)));
typedef unsigned u32x8 __attribute__((ext_vector_type(8)));
#define LDS_AS __attribute__((address_space(3)))

#define X_POLICY ""
#define S_POLICY " sc1"
#define H_POLICY ""
#define LPRIO 2
#define NE 3
#define GB_PIECE 5
#define GQ_POS 1
#define ST_LIST 19, 20, 21, 22, 23, 24, 25
#define STAMPS 0

namespace {
constexpr int D = 576;
constexpr int ROWB = D * 2;
constexpr int SLOT = 24576;
constexpr int XRES = 3 * SLOT;
constexpr int GBB = 6 * SLOT;
constexpr int FLAG = GBB + 8192;
constexpr int SINK = FLAG + 1024;
constexpr int LDS_BYTES = SINK + 1024;

constexpr int store_at(int u) {
  constexpr int at[7] = {ST_LIST};
  for (int k = 0; k < 7; ++k) if (at[k] == u) return k;
  return -1;
}
template <int V> struct IC { static constexpr int v = V; };
template <int I, int N, class F> __device__ __forceinline__ void static_for(F&& f) {
  if constexpr (I < N) { f(IC<I>{}); static_for<I + 1, N>(f); }
}
__device__ __forceinline__ u32x4 make_srd(unsigned lo, unsigned hi, unsigned bytes) { return u32x4{lo, hi & 0xffffu, bytes, 0x00020000u}; }
template <int N> __device__ __forceinline__ void wait_vm() { asm volatile("s_waitcnt vmcnt(%0)" ::"i"(N) : "memory"); }
template <int N> __device__ __forceinline__ void wait_lgkm() { asm volatile("s_waitcnt lgkmcnt(%0)" ::"i"(N) : "memory"); }
__device__ __forceinline__ void barrier() { asm volatile("s_barrier" ::: "memory"); }
__device__ __forceinline__ void fence() { __builtin_amdgcn_sched_barrier(0); }
__device__ __forceinline__ void dma16(unsigned lds_addr, unsigned voff, u32x4 srd, unsigned soff) {
  asm volatile("s_mov_b32 m0, %0\n\ts_nop 0\n\tbuffer_load_dwordx4 %1, %2, %3 offen lds" ::"s"(lds_addr), "v"(voff), "s"(srd), "s"(soff) : "memory");
}
__device__ __forceinline__ void dma3(unsigned lds_addr, unsigned voff, u32x4 srd, unsigned soff) {
  unsigned s1, s2;
  asm volatile("s_mov_b32 m0, %2\n\ts_add_u32 %0, %5, 0x9000\n\tbuffer_load_dwordx4 %3, %4, %5 offen lds\n\t"
               "s_add_u32 m0, m0, 0x1000\n\ts_add_u32 %1, %5, 0x12000\n\tbuffer_load_dwordx4 %3, %4, %0 offen lds\n\t"
               "s_add_u32 m0, m0, 0x1000\n\ts_nop 0\n\tbuffer_load_dwordx4 %3, %4, %1 offen lds"
               : "=&s"(s1), "=&s"(s2) : "s"(lds_addr), "v"(voff), "s"(srd), "s"(soff) : "memory", "scc");
}
__device__ __forceinline__ void touch(unsigned lds_addr, unsigned voff, u32x4 srd) {
  asm volatile("s_mov_b32 m0, %0\n\ts_nop 0\n\tbuffer_load_dword %1, %2, 0 offen lds" ::"s"(lds_addr), "v"(voff), "s"(srd) : "memory");
}
__device__ __forceinline__ void st16(u32x4 v, unsigned voff, u32x4 srd, unsigned soff) {
  asm volatile("buffer_store_dwordx4 %0, %1, %2, %3 offen" S_POLICY "\n\ts_nop 1" ::"v"(v), "v"(voff), "s"(srd), "s"(soff) : "memory");
}
__device__ __forceinline__ void st16h(u32x4 v, unsigned voff, u32x4 srd, unsigned soff) {
  asm volatile("buffer_store_dwordx4 %0, %1, %2, %3 offen" H_POLICY "\n\ts_nop 1" ::"v"(v), "v"(voff), "s"(srd), "s"(soff) : "memory");
}
template <int OFF> __device__ __forceinline__ void ldx(u32x4& dst, unsigned voff, unsigned long long base) {
  asm volatile("global_load_dwordx4 %0, %1, %2 offset:%3" X_POLICY : "=v"(dst) : "v"(voff), "s"(base), "i"(OFF));
}
__device__ __forceinline__ void ldg(u32x4& dst, unsigned voff, u32x4 srd) {
  asm volatile("buffer_load_dwordx4 %0, %1, %2, 0 offen" : "=v"(dst) : "v"(voff), "s"(srd));
}
template <int N> __device__ __forceinline__ void wait_vm_for(u32x4& v) { asm volatile("s_waitcnt vmcnt(%1)" : "+v"(v) : "i"(N)); }
__device__ __forceinline__ void ts_raw(unsigned long long& t) { asm volatile("s_memtime %0" : "=s"(t)); }
template <int I> __device__ __forceinline__ void ts_lane(unsigned& tsv, unsigned long long t) { asm volatile("" : "+s"(t)); asm volatile("v_writelane_b32 %0, %1, %2" : "+v"(tsv) : "s"((unsigned)t), "i"(I)); }
__device__ __forceinline__ void probe_now(unsigned long long& t) { asm volatile("s_memrealtime %0\n\ts_waitcnt lgkmcnt(0)" : "=s"(t) :: "memory"); }
template <int I> __device__ __forceinline__ void ts_put(unsigned& tsv) {
  unsigned long long t_; asm volatile("s_memtime %0\n\ts_waitcnt lgkmcnt(0)" : "=s"(t_) :: "memory");
  asm volatile("v_writelane_b32 %0, %1, %2" : "+v"(tsv) : "s"((unsigned)t_), "i"(I));
}
template <int CTRL> __device__ __forceinline__ float dpp_add(float v) {
  return v + __builtin_bit_cast(float, __builtin_amdgcn_update_dpp(0, __builtin_bit_cast(int, v), CTRL, 0xf, 0xf, true));
}
__device__ __forceinline__ float oct_sum(float v) {
  v = dpp_add<0xB1>(v); v = dpp_add<0x4E>(v); v = dpp_add<0x141>(v);
  return v;
}
__device__ __forceinline__ unsigned pack2(f32x2 v) { return __builtin_bit_cast(unsigned, __builtin_convertvector(v, bf16x2)); }
__device__ __forceinline__ f32x2 unpack2(unsigned u) { return f32x2{__uint_as_float(u << 16), __uint_as_float(u & 0xffff0000u)}; }
template <int N> __device__ __forceinline__ void piece_stats(const u32x4& x, float& s, float& q, f32x2 (&y)[4]) {
  float a0, a1, a2, a3, a4, a5, a6, a7;
  const unsigned x0 = x[0], x1 = x[1], x2 = x[2], x3 = x[3];
  asm volatile(
      "s_waitcnt vmcnt(%16)\n\t"
      "v_dot2c_f32_bf16 %0, %14, %10\n\tv_dot2c_f32_bf16 %1, %10, %10\n\t"
      "v_dot2c_f32_bf16 %0, %14, %11\n\tv_dot2c_f32_bf16 %1, %11, %11\n\t"
      "v_dot2c_f32_bf16 %0, %14, %12\n\tv_dot2c_f32_bf16 %1, %12, %12\n\t"
      "v_dot2c_f32_bf16 %0, %14, %13\n\tv_dot2c_f32_bf16 %1, %13, %13\n\t"
      "v_lshlrev_b32 %2, 16, %10\n\tv_and_b32 %3, %15, %10\n\t"
      "v_lshlrev_b32 %4, 16, %11\n\tv_and_b32 %5, %15, %11\n\t"
      "v_lshlrev_b32 %6, 16, %12\n\tv_and_b32 %7, %15, %12\n\t"
      "v_lshlrev_b32 %8, 16, %13\n\tv_and_b32 %9, %15, %13"
      : "+v"(s), "+v"(q), "=&v"(a0), "=&v"(a1), "=&v"(a2), "=&v"(a3), "=&v"(a4), "=&v"(a5), "=&v"(a6), "=&v"(a7)
      : "v"(x0), "v"(x1), "v"(x2), "v"(x3), "s"(0x3f803f80u), "s"(0xffff0000u), "i"(N));
  y[0] = f32x2{a0, a1}; y[1] = f32x2{a2, a3}; y[2] = f32x2{a4, a5}; y[3] = f32x2{a6, a7};
}
__device__ __forceinline__ void count_in(unsigned lds_addr) { asm volatile("s_mov_b64 exec, 1\n\tds_add_u32 %0, %1\n\ts_mov_b64 exec, -1" ::"v"(lds_addr), "v"(1u) : "memory"); }
__device__ __forceinline__ void keep2(unsigned a, unsigned b) { asm volatile("s_waitcnt lgkmcnt(0)" ::"s"(a), "s"(b) : "memory"); }
__device__ __forceinline__ void pinf(float& v) { asm volatile("" : "+v"(v)); }
__device__ __forceinline__ LDS_AS char* opaque(LDS_AS char* p) { asm volatile("" : "+v"(p)); return p; }
template <int MASK> __device__ __forceinline__ float sel8(float v) {
  const int i = __builtin_bit_cast(int, v);
  return __builtin_bit_cast(float, __builtin_amdgcn_update_dpp(i, i, 0x128, 0xf, MASK, false));
}
__device__ __forceinline__ float fold8(float a, float b) {
  float t;
  asm("s_nop 1\n\tv_add_f32_dpp %0, %1, %1 row_ror:8 row_mask:0xf bank_mask:0x3\n\tv_add_f32_dpp %0, %2, %2 row_ror:8 row_mask:0xf bank_mask:0xc" : "=&v"(t) : "v"(a), "v"(b));
  return t;
}

#define TS(i) do { if constexpr ((i) == PROBE) probe_now(tprobe); } while (0)
#define TS2(i) do {} while (0)

__global__ __launch_bounds__(512, 2) void ln_linear_kernel(const uint16_t* __restrict__ x, const uint16_t* __restrict__ g,
                                                           const uint16_t* __restrict__ b, const uint16_t* __restrict__ w,
                                                           uint16_t* __restrict__ out
) {
  extern __shared__ __attribute__((aligned(1024))) char smem[];
  LDS_AS char* lds = (LDS_AS char*)smem;
  unsigned long long kx;
  u32x8 ka;
  asm volatile("s_load_dwordx2 %0, %2, 0x0\n\ts_load_dwordx8 %1, %2, 0x8" : "=&s"(kx), "=&s"(ka) : "s"(__builtin_amdgcn_kernarg_segment_ptr()));
  unsigned long long tprobe = 0;
  if constexpr (PROBE == 0) probe_now(tprobe);
  const int tid = threadIdx.x, lane = tid & 63;
  const unsigned bid = blockIdx.x;
  const unsigned row0 = (bid & 7) * 1024 + (bid >> 4) * 64;
  unsigned xvt = (unsigned)(tid >> 3) * ROWB + (unsigned)(tid & 7) * 16, xvm = (unsigned)(tid >> 4) * 2 * ROWB + (unsigned)(tid & 15) * 16;
  unsigned xo = row0 * ROWB;
  const unsigned t0 = __builtin_amdgcn_readfirstlane(tid);
  asm volatile("" : "+v"(xvt), "+v"(xvm));
  asm volatile("" : "+s"(xo));
  unsigned pf0 = 0, pf1 = 0;
  asm volatile("s_waitcnt lgkmcnt(0)" : "+s"(kx), "+s"(ka));
  if (t0 >= 256) {
    *(LDS_AS unsigned*)(lds + FLAG) = 0u;
    unsigned long long pc;
    unsigned len;
    asm volatile("s_getpc_b64 %0\n.Lpf_here:\n\ts_mov_b32 %1, .Lpf_end-.Lpf_here-128" : "=s"(pc), "=s"(len));
    const unsigned o = min((bid >> 3) * 128u + (t0 & 192u) * 64u, len);
    asm volatile("s_waitcnt lgkmcnt(0)\n\ts_load_dword %0, %2, %3\n\ts_load_dword %1, %2, %3 offset:64\n\ts_barrier" : "=&s"(pf0), "=&s"(pf1) : "s"(pc), "s"(o) : "memory");
  }
  u32x4 xr[9], gq;
  {
    const unsigned long long xb = kx + xo;
    ldx<1024>(xr[8], xvt, xb);
    static_for<0, GQ_POS>([&](auto jc) { constexpr int j = decltype(jc)::v; ldx<(j >> 2) * ROWB + (j & 3) * 256>(xr[j], xvm, xb); });
    fence();
    ldg(gq, (unsigned)(tid & 127) * 16u, make_srd(t0 < 128 ? ka[0] : ka[2], t0 < 128 ? ka[1] : ka[3], t0 < 256 ? (unsigned)ROWB : 0u));
    static_for<GQ_POS, 8>([&](auto jc) { constexpr int j = decltype(jc)::v; ldx<(j >> 2) * ROWB + (j & 3) * 256>(xr[j], xvm, xb); });
    fence();
  }
  const int wv = t0 >> 6, wi = wv & 3;
  const unsigned jb = bid >> 3, h = jb & 1;
  const int r7 = lane >> 3, c7 = lane & 7, c15 = lane & 15, rq = lane >> 4;
  const unsigned ldsbase = (unsigned)(uintptr_t)lds;
  const unsigned rowoff = (unsigned)r7 * ROWB;
  if (wv < 4) barrier();
  const u32x4 srdw = make_srd(ka[4], ka[5], (unsigned)D * ROWB), srdo = make_srd(ka[6], ka[7], 8192u * ROWB);
  touch(ldsbase + SINK, (wv >= 4 && wv < 7 && lane < 18) ? (jb * 18u + (unsigned)lane) * ROWB + (wv == 4 ? 512u : wv == 5 ? 0u : 640u) : 0xfffffff0u, srdw);
  TS(13);

  {
    f32x4 gv[5][2], bv[5][2];
    f32x2 y[9][4];
    LDS_AS char* aw[3] = {opaque(lds + XRES + wv * 1024 + (c15 >> 3) * 8192 + (2 * rq) * 128 + ((c7 ^ (2 * rq)) * 16)),
                          opaque(lds + XRES + wv * 1024 + (c15 >> 3) * 8192 + (2 * rq + 1) * 128 + ((c7 ^ (2 * rq + 1)) * 16)),
                          opaque(lds + XRES + 8 * 8192 + wv * 1024 + r7 * 128 + ((c7 ^ r7) * 16))};
    LDS_AS char* gr = opaque(lds + GBB + c15 * 16);
    LDS_AS char* grt = opaque(lds + GBB + 2048 + c7 * 16);
    float sm[3] = {0.f, 0.f, 0.f}, sq[3] = {0.f, 0.f, 0.f};
#pragma unroll
    for (int i = 0; i < 3; ++i) { pinf(sm[i]); pinf(sq[i]); }
    fence();
    static_for<0, 9>([&](auto jc) {
      constexpr int jj = decltype(jc)::v, j = jj == 0 ? 8 : jj - 1;
      piece_stats<9 - jj + (jj <= GQ_POS ? 1 : 0)>(xr[j], sm[j >> 2], sq[j >> 2], y[j]);
      if constexpr (jj == 0) TS(14);
      if constexpr (jj == GQ_POS) {
        wait_vm_for<9 - jj>(gq);
        if (wv < 4) {
          const unsigned g0 = gq[0], g1 = gq[1], g2 = gq[2], g3 = gq[3];
          const f32x2 a0 = unpack2(g0), a1 = unpack2(g1), a2 = unpack2(g2), a3 = unpack2(g3);
          *(LDS_AS f32x4*)(lds + GBB + wi * 2048 + rq * 512 + c15 * 16) = f32x4{a0[0], a0[1], a1[0], a1[1]};
          *(LDS_AS f32x4*)(lds + GBB + wi * 2048 + rq * 512 + c15 * 16 + 256) = f32x4{a2[0], a2[1], a3[0], a3[1]};
          count_in(ldsbase + FLAG);
        }
        fence();
      }
      if constexpr (jj == GB_PIECE) {
        while (__builtin_amdgcn_readfirstlane(*(volatile LDS_AS unsigned*)(lds + FLAG)) < 4u) __builtin_amdgcn_s_sleep(1);
        keep2(pf0, pf1);
      }
      if constexpr (jj >= GB_PIECE) {
#pragma unroll
        for (int kk = jj - GB_PIECE; kk < (jj == 8 ? 5 : jj - GB_PIECE + 1); ++kk) {
          if (kk < 5) {
            const int k = kk == 0 ? 4 : kk - 1;
            LDS_AS char* p = k < 4 ? gr + k * 512 : grt;
#pragma unroll
            for (int e = 0; e < 2; ++e) { gv[k][e] = *(LDS_AS f32x4*)(p + e * 256); bv[k][e] = *(LDS_AS f32x4*)(p + 4096 + e * 256); }
          }
        }
        fence();
      }
    });
    fence();
    TS(3);
    const float mean = oct_sum(fold8(sm[0], sm[1]) + sm[2]) * (1.0f / D);
    const float var = fmaxf(oct_sum(fold8(sq[0], sq[1]) + sq[2]) * (1.0f / D) - mean * mean, 0.f);
    float rs[3], nm[3];
    rs[2] = __builtin_amdgcn_rsqf(var + 1e-5f);
    nm[2] = -mean * rs[2];
    rs[0] = sel8<0xc>(rs[2]); rs[1] = sel8<0x3>(rs[2]);
    nm[0] = sel8<0xc>(nm[2]); nm[1] = sel8<0x3>(nm[2]);
#pragma unroll
    for (int jj = 0; jj < 9; ++jj) {
      const int j = jj == 0 ? 8 : jj - 1, G = j >> 2, k = j == 8 ? 4 : (j & 3);
#pragma unroll
      for (int e = 0; e < 4; ++e) y[j][e] = __builtin_elementwise_fma(y[j][e], f32x2{rs[G], rs[G]}, f32x2{nm[G], nm[G]});
      fence();
#pragma unroll
      for (int e = 0; e < 4; ++e) {
        const f32x4 gg = gv[k][e >> 1], bb = bv[k][e >> 1];
        y[j][e] = __builtin_elementwise_fma(y[j][e], (e & 1) ? f32x2{gg[2], gg[3]} : f32x2{gg[0], gg[1]}, (e & 1) ? f32x2{bb[2], bb[3]} : f32x2{bb[0], bb[1]});
      }
      fence();
      *(LDS_AS u32x4*)(aw[G] + (j == 8 ? 0 : (j & 3) * 16384)) = u32x4{pack2(y[j][0]), pack2(y[j][1]), pack2(y[j][2]), pack2(y[j][3])};
      fence();
    }
  }
  TS(4);

  const unsigned wvoff = rowoff + (unsigned)(((((c7 >> 1) ^ (((r7 >> 1) & 1) | ((tid >> 5) & 2))) << 1) | (c7 & 1)) * 16);
  const unsigned wbase = (unsigned)h * 640u + (unsigned)wi * 8u * ROWB;
  auto request = [&](unsigned hslot, unsigned so) {
    dma3(ldsbase + hslot * (SLOT / 2) + (unsigned)wi * 1024u, wvoff, srdw, so);
  };

  if (wv >= 4) {
    wait_lgkm<0>();
    barrier();
    TS(6);
    __builtin_amdgcn_s_setprio(LPRIO);
    auto req = [&](auto cc) {
      constexpr int c = decltype(cc)::v;
      unsigned wb = wbase, lb = ldsbase + (unsigned)wi * 1024u;
      asm volatile("" : "+s"(wb), "+s"(lb));
      dma3(lb + ((c + 3) % 12) * (SLOT / 2), wvoff, srdw, wb + (c / 6) * 128u + (c % 6) * 96u * ROWB);
    };
    static_for<NE, 3>(req);
    barrier(); TS2(18); static_for<3, 5>(req);
    barrier(); TS2(21); static_for<5, 6>(req);
    wait_vm<12>();
    barrier(); TS2(24); static_for<6, 8>(req);
    static_for<0, 11>([&](auto qc) {
      constexpr int q = decltype(qc)::v;
      wait_vm<(q < 9 ? 12 : q == 9 ? 6 : 0)>();
      barrier();
      TS2(27 + 3 * q);
      if constexpr (q < 8) static_for<2 * q + 8, 2 * q + 10>(req);
    });
    TS(8);
  } else {
    {
      const unsigned hv = (unsigned)(lane >> 2) * ROWB + (unsigned)(((lane & 3) ^ ((lane >> 4) & 2)) * 16);
      const unsigned hs = 512u + (unsigned)h * 64u + (unsigned)wi * 16u * ROWB;
#pragma unroll
      for (int i = 0; i < 9; ++i) dma16(ldsbase + (i * 4 + wi) * 1024, hv, srdw, hs + (unsigned)(i * 64) * ROWB);
#pragma unroll
      for (int c = 0; c < NE; ++c) request(c + 3, wbase + c * 96u * ROWB);
      const unsigned li = (unsigned)tid, kk = (li * 43u) >> 8, ln = li - kk * 6u;
      touch(ldsbase + SINK, li < 108u ? ((unsigned)jb * 18u + kk) * ROWB + (ln + 1u + (ln >= 3u) * 2u) * 128u : 0xfffffff0u, srdw);
    }
    const int m = wi >> 1, n = wi & 1;
    const int fr = lane & 15, fg = lane >> 4;
    bf16x8 xa[2][18];
    LDS_AS char* ard[2][2];
#pragma unroll
    for (int e = 0; e < 2; ++e) {
      const unsigned o = (m * 2 + (fr >> 3)) * 1024 + (fr & 7) * 128 + (((4 * e + fg) ^ (fr & 7)) * 16);
      ard[e][0] = opaque(lds + XRES + o);
      ard[e][1] = opaque(lds + XRES + 5 * 8192 + o);
    }
    LDS_AS char* wrd[2][3];
#pragma unroll
    for (int t = 0; t < 2; ++t)
#pragma unroll
      for (int z = 0; z < 3; ++z)
        wrd[t][z] = opaque(lds + z * 2 * SLOT + (8 * fg + (fr >> 2)) * 128 + (((2 * n + t) ^ (((fr >> 3) & 1) | ((fg & 1) << 1))) * 32) + (fr & 3) * 8);
    LDS_AS char* wh = opaque(lds + (8 * fg + (fr >> 2)) * 64 + ((n ^ (fg & 1)) * 32) + (fr & 3) * 8);
    const unsigned ovoff = (unsigned)fr * ROWB + (unsigned)(2 * (fg & 1) + (fg >> 1)) * 16u;
    const unsigned orow = (unsigned)(row0 + m * 16) * ROWB;
    const unsigned osoff = orow + (unsigned)h * 640u + (unsigned)n * 64u;
    wait_lgkm<0>();
    wait_vm<6 + NE * 3 + 1>();
    TS(7);
    barrier();
    TS(6);

    bf16x8 wf[2][6];
    f32x4 acc[2][2], fin[2][2];
    u32x4 po[4][2];
    auto tr_frag = [&](LDS_AS char* p, int step) {
      const s16x4 lo = __builtin_amdgcn_ds_read_tr16_b64_v4i16((LDS_AS s16x4*)p);
      const s16x4 hi = __builtin_amdgcn_ds_read_tr16_b64_v4i16((LDS_AS s16x4*)(p + step));
      return __builtin_bit_cast(bf16x8, __builtin_shufflevector(lo, hi, 0, 1, 2, 3, 4, 5, 6, 7));
    };
    auto rd = [&](auto uc) {
      constexpr int u = decltype(uc)::v, hsl = u % 12;
      if constexpr (u >= 3) {
#pragma unroll
        for (int s = 0; s < 3; ++s)
#pragma unroll
          for (int t = 0; t < 2; ++t) wf[u & 1][s * 2 + t] = tr_frag(wrd[t][hsl >> 2] + (hsl & 3) * (SLOT / 2) + s * 4096, 512);
      } else {
#pragma unroll
        for (int s = 0; s < 6; ++s) {
          const int ks = u * 6 + s;
          wf[u & 1][s] = tr_frag(wh + ks * 2048, 256);
#pragma unroll
          for (int i = 0; i < 2; ++i) xa[i][ks] = *(LDS_AS bf16x8*)(ard[ks & 1][ks >= 10] + ((ks >> 1) % 5) * 8192 + i * 4096);
        }
      }
    };
    auto mm = [&](auto uc) {
      constexpr int u = decltype(uc)::v;
#pragma unroll
      for (int f = 0; f < 6; ++f) {
        const int ks = u >= 3 ? ((u - 3) % 6) * 3 + f / 2 : u * 6 + f, t = f & 1;
        const bool first = u >= 3 ? ks == 0 : ks < 2;
#pragma unroll
        for (int i = 0; i < 2; ++i) acc[i][t] = __builtin_amdgcn_mfma_f32_16x16x32_bf16(wf[u & 1][f], xa[i][ks], first ? f32x4{0.f, 0.f, 0.f, 0.f} : acc[i][t], 0, 0, 0);
      }
    };
    auto pack = [&](const f32x4& a, const f32x4& c) {
      const auto lo = __builtin_amdgcn_permlane16_swap(pack2(f32x2{a[0], a[1]}), pack2(f32x2{c[0], c[1]}), false, false);
      const auto hi = __builtin_amdgcn_permlane16_swap(pack2(f32x2{a[2], a[3]}), pack2(f32x2{c[2], c[3]}), false, false);
      return u32x4{(unsigned)lo[0], (unsigned)hi[0], (unsigned)lo[1], (unsigned)hi[1]};
    };
    const unsigned hvoff = (unsigned)(fr + (fg & 1) * 32) * ROWB + (unsigned)(fg >> 1) * 16u;
    u32x4 ph;
    auto store = [&](int k) {
      if (k == 0) st16h(ph, hvoff, srdo, orow + 512u + (unsigned)h * 64u + (unsigned)n * 32u);
      else st16(po[(k - 1) / 2][(k - 1) & 1], ovoff, srdo, osoff + ((k - 1) / 2) * 128u + ((k - 1) & 1) * 32u * ROWB);
    };
    rd(IC<0>{});
    fence();
    static_for<0, 27>([&](auto uc) {
      constexpr int u = decltype(uc)::v, v = u - 3;
      constexpr bool pk = v >= 6 && v % 6 < 2;
      if constexpr (u < 3) {
        wait_vm<(u == 0 ? 3 + NE * 3 + 1 : u == 1 ? NE * 3 + 1 : (NE > 2 ? 3 : 0) + 1)>();
        wait_lgkm<0>();
        barrier();
        fence();
      } else if constexpr ((v & 1) && u < 26) {
        if constexpr (v == 1) wait_vm<1>();
        barrier();
        fence();
      }
      if constexpr (u < 26) rd(IC<u + 1>{});
      if constexpr (pk) po[v / 6 - 1][v % 6] = pack(fin[v % 6][0], fin[v % 6][1]);
      if constexpr (u == 3) ph = pack(fin[0][0] + fin[0][1], fin[1][0] + fin[1][1]);
      mm(uc);
      if constexpr (u < 26) {
        static_for<0, 12>([&](auto ic) {
          constexpr int i = decltype(ic)::v;
          __builtin_amdgcn_sched_group_barrier(0x008, 1, 0);
          if constexpr (u < 2) { if constexpr (i < 8) __builtin_amdgcn_sched_group_barrier(0x100, 3, 0); }
          else __builtin_amdgcn_sched_group_barrier(0x100, 1, 0);
          if constexpr ((pk && i >= 2 && i < 8) || (u == 3 && i >= 2)) __builtin_amdgcn_sched_group_barrier(0x002, 1, 0);
        });
      }
      fence();
      if constexpr (u == 3) TS(12);
      if constexpr (u == 18) TS(10);
      if constexpr (store_at(u) >= 0) { store(store_at(u)); fence(); }
      if constexpr (u == 2 || (v >= 0 && v % 6 == 5)) {
#pragma unroll
        for (int i = 0; i < 2; ++i) { fin[i][0] = acc[i][0]; fin[i][1] = acc[i][1]; }
      }
    });
    po[3][0] = pack(fin[0][0], fin[0][1]);
    po[3][1] = pack(fin[1][0], fin[1][1]);
    store(7);
    store(8);
    TS(9);
  }
  if constexpr (PROBE == 11) { asm volatile("s_waitcnt vmcnt(0)" ::: "memory"); probe_now(tprobe); }
  if (wv == PROBE_WAVE) { while (__builtin_amdgcn_s_memrealtime() < tprobe + 4000ull) __builtin_amdgcn_s_sleep(1); }
  asm volatile(".Lpf_end:");
}
}

extern "C" void kernel_launch(void* const* d_in, const int* in_sizes, int n_in, void* d_out, int out_size, void* d_ws,
                              size_t ws_size, hipStream_t stream) {
  (void)in_sizes; (void)n_in; (void)out_size; (void)d_ws; (void)ws_size;
  (void)hipFuncSetAttribute((const void*)ln_linear_kernel, hipFuncAttributeMaxDynamicSharedMemorySize, LDS_BYTES);
  ln_linear_kernel<<<256, 512, LDS_BYTES, stream>>>((const uint16_t*)d_in[0], (const uint16_t*)d_in[1], (const uint16_t*)d_in[2],
                                                    (const uint16_t*)d_in[3], (uint16_t*)d_out
  );
}
```

```cpp
#include <hip/hip_runtime.h>
#include <stdint.h>

typedef short s16x4 __attribute__((ext_vector_type(4)));
typedef __bf16 bf16x2 __attribute__((ext_vector_type(2)));
typedef __bf16 bf16x8 __attribute__((ext_vector_type(8)));
typedef float f32x2 __attribute__((ext_vector_type(2)));
typedef float f32x4 __attribute__((ext_vector_type(4)));
typedef unsigned u32x4 __attribute__((ext_vector_type(4)));
typedef unsigned u32x8 __attribute__((ext_vector_type(8)));
#define LDS_AS __attribute__((address_space(3)))

namespace {
constexpr int D = 576;
constexpr int ROWS = 8192;
constexpr int ROWB = D * 2;
constexpr int HSLOT = 12288;
constexpr int XRES = 6 * HSLOT;
constexpr int GBB = 12 * HSLOT;
constexpr int FLAG = GBB + 8192;
constexpr int SINK = FLAG + 1024;
constexpr int LDS_BYTES = SINK + 1024;
constexpr int GQ_POS = 1;
constexpr int GB_PIECE = 5;
constexpr int ST_FROM = 22;
constexpr unsigned OOB = 0xfffffff0u;

template <int V> struct IC { static constexpr int v = V; };
template <int I, int N, class F> __device__ __forceinline__ void static_for(F&& f) {
  if constexpr (I < N) { f(IC<I>{}); static_for<I + 1, N>(f); }
}
__device__ __forceinline__ u32x4 make_srd(unsigned lo, unsigned hi, unsigned bytes) { return u32x4{lo, hi & 0xffffu, bytes, 0x00020000u}; }
template <int N> __device__ __forceinline__ void wait_vm() { asm volatile("s_waitcnt vmcnt(%0)" ::"i"(N) : "memory"); }
template <int N> __device__ __forceinline__ void wait_vm_for(u32x4& v) { asm volatile("s_waitcnt vmcnt(%1)" : "+v"(v) : "i"(N)); }
__device__ __forceinline__ void wait_lgkm0() { asm volatile("s_waitcnt lgkmcnt(0)" ::: "memory"); }
__device__ __forceinline__ void barrier() { asm volatile("s_barrier" ::: "memory"); }
__device__ __forceinline__ void fence() { __builtin_amdgcn_sched_barrier(0); }
__device__ __forceinline__ void dma16(unsigned lds_addr, unsigned voff, u32x4 srd, unsigned soff) {
  asm volatile("s_mov_b32 m0, %0\n\ts_nop 0\n\tbuffer_load_dwordx4 %1, %2, %3 offen lds" ::"s"(lds_addr), "v"(voff), "s"(srd), "s"(soff) : "memory");
}
__device__ __forceinline__ void dma3(unsigned lds_addr, unsigned voff, u32x4 srd, unsigned soff) {
  unsigned s1, s2;
  asm volatile("s_mov_b32 m0, %2\n\ts_add_u32 %0, %5, 0x9000\n\tbuffer_load_dwordx4 %3, %4, %5 offen lds\n\t"
               "s_add_u32 m0, m0, 0x1000\n\ts_add_u32 %1, %5, 0x12000\n\tbuffer_load_dwordx4 %3, %4, %0 offen lds\n\t"
               "s_add_u32 m0, m0, 0x1000\n\ts_nop 0\n\tbuffer_load_dwordx4 %3, %4, %1 offen lds"
               : "=&s"(s1), "=&s"(s2) : "s"(lds_addr), "v"(voff), "s"(srd), "s"(soff) : "memory", "scc");
}
__device__ __forceinline__ void touch(unsigned lds_addr, unsigned voff, u32x4 srd) {
  asm volatile("s_mov_b32 m0, %0\n\ts_nop 0\n\tbuffer_load_dword %1, %2, 0 offen lds" ::"s"(lds_addr), "v"(voff), "s"(srd) : "memory");
}
__device__ __forceinline__ void st16_through(u32x4 v, unsigned voff, u32x4 srd, unsigned soff) {
  asm volatile("buffer_store_dwordx4 %0, %1, %2, %3 offen sc1\n\ts_nop 1" ::"v"(v), "v"(voff), "s"(srd), "s"(soff) : "memory");
}
template <int OFF> __device__ __forceinline__ void ldx(u32x4& dst, unsigned voff, unsigned long long base) {
  asm volatile("global_load_dwordx4 %0, %1, %2 offset:%3" : "=v"(dst) : "v"(voff), "s"(base), "i"(OFF));
}
__device__ __forceinline__ void ldg(u32x4& dst, unsigned voff, u32x4 srd) {
  asm volatile("buffer_load_dwordx4 %0, %1, %2, 0 offen" : "=v"(dst) : "v"(voff), "s"(srd));
}
template <int CTRL> __device__ __forceinline__ float dpp_add(float v) {
  return v + __builtin_bit_cast(float, __builtin_amdgcn_update_dpp(0, __builtin_bit_cast(int, v), CTRL, 0xf, 0xf, true));
}
__device__ __forceinline__ float oct_sum(float v) {
  v = dpp_add<0xB1>(v); v = dpp_add<0x4E>(v); v = dpp_add<0x141>(v);
  return v;
}
__device__ __forceinline__ unsigned pack2(f32x2 v) { return __builtin_bit_cast(unsigned, __builtin_convertvector(v, bf16x2)); }
__device__ __forceinline__ f32x2 unpack2(unsigned u) { return f32x2{__uint_as_float(u << 16), __uint_as_float(u & 0xffff0000u)}; }
template <int N> __device__ __forceinline__ void piece_stats(const u32x4& x, float& s, float& q, f32x2 (&y)[4]) {
  float a0, a1, a2, a3, a4, a5, a6, a7;
  const unsigned x0 = x[0], x1 = x[1], x2 = x[2], x3 = x[3];
  asm volatile(
      "s_waitcnt vmcnt(%16)\n\t"
      "v_dot2c_f32_bf16 %0, %14, %10\n\tv_dot2c_f32_bf16 %1, %10, %10\n\t"
      "v_dot2c_f32_bf16 %0, %14, %11\n\tv_dot2c_f32_bf16 %1, %11, %11\n\t"
      "v_dot2c_f32_bf16 %0, %14, %12\n\tv_dot2c_f32_bf16 %1, %12, %12\n\t"
      "v_dot2c_f32_bf16 %0, %14, %13\n\tv_dot2c_f32_bf16 %1, %13, %13\n\t"
      "v_lshlrev_b32 %2, 16, %10\n\tv_and_b32 %3, %15, %10\n\t"
      "v_lshlrev_b32 %4, 16, %11\n\tv_and_b32 %5, %15, %11\n\t"
      "v_lshlrev_b32 %6, 16, %12\n\tv_and_b32 %7, %15, %12\n\t"
      "v_lshlrev_b32 %8, 16, %13\n\tv_and_b32 %9, %15, %13"
      : "+v"(s), "+v"(q), "=&v"(a0), "=&v"(a1), "=&v"(a2), "=&v"(a3), "=&v"(a4), "=&v"(a5), "=&v"(a6), "=&v"(a7)
      : "v"(x0), "v"(x1), "v"(x2), "v"(x3), "s"(0x3f803f80u), "s"(0xffff0000u), "i"(N));
  y[0] = f32x2{a0, a1}; y[1] = f32x2{a2, a3}; y[2] = f32x2{a4, a5}; y[3] = f32x2{a6, a7};
}
__device__ __forceinline__ void count_in(unsigned lds_addr) {
  asm volatile("s_mov_b64 exec, 1\n\tds_add_u32 %0, %1\n\ts_mov_b64 exec, -1" ::"v"(lds_addr), "v"(1u) : "memory");
}
__device__ __forceinline__ void retire2(unsigned a, unsigned b) { asm volatile("s_waitcnt lgkmcnt(0)" ::"s"(a), "s"(b) : "memory"); }
__device__ __forceinline__ void pinf(float& v) { asm volatile("" : "+v"(v)); }
__device__ __forceinline__ LDS_AS char* opaque(LDS_AS char* p) { asm volatile("" : "+v"(p)); return p; }
template <int MASK> __device__ __forceinline__ float sel8(float v) {
  const int i = __builtin_bit_cast(int, v);
  return __builtin_bit_cast(float, __builtin_amdgcn_update_dpp(i, i, 0x128, 0xf, MASK, false));
}
__device__ __forceinline__ float fold8(float a, float b) {
  float t;
  asm("s_nop 1\n\tv_add_f32_dpp %0, %1, %1 row_ror:8 row_mask:0xf bank_mask:0x3\n\tv_add_f32_dpp %0, %2, %2 row_ror:8 row_mask:0xf bank_mask:0xc" : "=&v"(t) : "v"(a), "v"(b));
  return t;
}

__global__ __launch_bounds__(512, 2) void ln_linear_kernel(const uint16_t* __restrict__ x, const uint16_t* __restrict__ g,
                                                           const uint16_t* __restrict__ b, const uint16_t* __restrict__ w,
                                                           uint16_t* __restrict__ out) {
  extern __shared__ __attribute__((aligned(1024))) char smem[];
  LDS_AS char* lds = (LDS_AS char*)smem;
  unsigned long long kx;
  u32x8 ka;
  asm volatile("s_load_dwordx2 %0, %2, 0x0\n\ts_load_dwordx8 %1, %2, 0x8" : "=&s"(kx), "=&s"(ka) : "s"(__builtin_amdgcn_kernarg_segment_ptr()));
  const int tid = threadIdx.x, lane = tid & 63;
  const unsigned bid = blockIdx.x;
  const unsigned row0 = (bid & 7) * 1024 + (bid >> 4) * 64;
  unsigned xvt = (unsigned)(tid >> 3) * ROWB + (unsigned)(tid & 7) * 16, xvm = (unsigned)(tid >> 4) * 2 * ROWB + (unsigned)(tid & 15) * 16;
  unsigned xo = row0 * ROWB;
  const unsigned t0 = __builtin_amdgcn_readfirstlane(tid);
  asm volatile("" : "+v"(xvt), "+v"(xvm));
  asm volatile("" : "+s"(xo));
  unsigned pf0 = 0, pf1 = 0;
  asm volatile("s_waitcnt lgkmcnt(0)" : "+s"(kx), "+s"(ka));
  if (t0 >= 256) {
    *(LDS_AS unsigned*)(lds + FLAG) = 0u;
    unsigned long long pc;
    unsigned len;
    asm volatile("s_getpc_b64 %0\n.Lpf_here:\n\ts_mov_b32 %1, .Lpf_end-.Lpf_here-128" : "=s"(pc), "=s"(len));
    const unsigned o = min((bid >> 3) * 128u + (t0 & 192u) * 64u, len);
    asm volatile("s_waitcnt lgkmcnt(0)\n\ts_load_dword %0, %2, %3\n\ts_load_dword %1, %2, %3 offset:64\n\ts_barrier" : "=&s"(pf0), "=&s"(pf1) : "s"(pc), "s"(o) : "memory");
  }
  u32x4 xr[9], gq;
  {
    const unsigned long long xb = kx + xo;
    ldx<1024>(xr[8], xvt, xb);
    static_for<0, GQ_POS>([&](auto jc) { constexpr int j = decltype(jc)::v; ldx<(j >> 2) * ROWB + (j & 3) * 256>(xr[j], xvm, xb); });
    fence();
    ldg(gq, (unsigned)(tid & 127) * 16u, make_srd(t0 < 128 ? ka[0] : ka[2], t0 < 128 ? ka[1] : ka[3], t0 < 256 ? (unsigned)ROWB : 0u));
    static_for<GQ_POS, 8>([&](auto jc) { constexpr int j = decltype(jc)::v; ldx<(j >> 2) * ROWB + (j & 3) * 256>(xr[j], xvm, xb); });
    fence();
  }
  const int wv = t0 >> 6, wi = wv & 3;
  const unsigned jb = bid >> 3, h = jb & 1;
  const int r7 = lane >> 3, c7 = lane & 7, c15 = lane & 15, rq = lane >> 4;
  const unsigned ldsbase = (unsigned)(uintptr_t)lds;
  if (wv < 4) barrier();
  const u32x4 srdw = make_srd(ka[4], ka[5], (unsigned)D * ROWB), srdo = make_srd(ka[6], ka[7], (unsigned)ROWS * ROWB);
  touch(ldsbase + SINK, (wv >= 4 && wv < 7 && lane < 18) ? (jb * 18u + (unsigned)lane) * ROWB + (wv == 4 ? 512u : wv == 5 ? 0u : 640u) : OOB, srdw);

  {
    f32x4 gv[5][2], bv[5][2];
    f32x2 y[9][4];
    LDS_AS char* aw[3] = {opaque(lds + XRES + wv * 1024 + (c15 >> 3) * 8192 + (2 * rq) * 128 + ((c7 ^ (2 * rq)) * 16)),
                          opaque(lds + XRES + wv * 1024 + (c15 >> 3) * 8192 + (2 * rq + 1) * 128 + ((c7 ^ (2 * rq + 1)) * 16)),
                          opaque(lds + XRES + 8 * 8192 + wv * 1024 + r7 * 128 + ((c7 ^ r7) * 16))};
    LDS_AS char* gr = opaque(lds + GBB + c15 * 16);
    LDS_AS char* grt = opaque(lds + GBB + 2048 + c7 * 16);
    float sm[3] = {0.f, 0.f, 0.f}, sq[3] = {0.f, 0.f, 0.f};
#pragma unroll
    for (int i = 0; i < 3; ++i) { pinf(sm[i]); pinf(sq[i]); }
    fence();
    static_for<0, 9>([&](auto jc) {
      constexpr int jj = decltype(jc)::v, j = jj == 0 ? 8 : jj - 1;
      piece_stats<9 - jj + (jj <= GQ_POS ? 1 : 0)>(xr[j], sm[j >> 2], sq[j >> 2], y[j]);
      if constexpr (jj == GQ_POS) {
        wait_vm_for<9 - jj>(gq);
        if (wv < 4) {
          const unsigned g0 = gq[0], g1 = gq[1], g2 = gq[2], g3 = gq[3];
          const f32x2 a0 = unpack2(g0), a1 = unpack2(g1), a2 = unpack2(g2), a3 = unpack2(g3);
          *(LDS_AS f32x4*)(lds + GBB + wi * 2048 + rq * 512 + c15 * 16) = f32x4{a0[0], a0[1], a1[0], a1[1]};
          *(LDS_AS f32x4*)(lds + GBB + wi * 2048 + rq * 512 + c15 * 16 + 256) = f32x4{a2[0], a2[1], a3[0], a3[1]};
          count_in(ldsbase + FLAG);
        }
        fence();
      }
      if constexpr (jj == GB_PIECE) {
        while (__builtin_amdgcn_readfirstlane(*(volatile LDS_AS unsigned*)(lds + FLAG)) < 4u) __builtin_amdgcn_s_sleep(1);
        retire2(pf0, pf1);
      }
      if constexpr (jj >= GB_PIECE) {
#pragma unroll
        for (int kk = jj - GB_PIECE; kk < (jj == 8 ? 5 : jj - GB_PIECE + 1); ++kk) {
          const int k = kk == 0 ? 4 : kk - 1;
          LDS_AS char* p = k < 4 ? gr + k * 512 : grt;
#pragma unroll
          for (int e = 0; e < 2; ++e) { gv[k][e] = *(LDS_AS f32x4*)(p + e * 256); bv[k][e] = *(LDS_AS f32x4*)(p + 4096 + e * 256); }
        }
        fence();
      }
    });
    fence();
    const float mean = oct_sum(fold8(sm[0], sm[1]) + sm[2]) * (1.0f / D);
    const float var = fmaxf(oct_sum(fold8(sq[0], sq[1]) + sq[2]) * (1.0f / D) - mean * mean, 0.f);
    float rs[3], nm[3];
    rs[2] = __builtin_amdgcn_rsqf(var + 1e-5f);
    nm[2] = -mean * rs[2];
    rs[0] = sel8<0xc>(rs[2]); rs[1] = sel8<0x3>(rs[2]);
    nm[0] = sel8<0xc>(nm[2]); nm[1] = sel8<0x3>(nm[2]);
#pragma unroll
    for (int jj = 0; jj < 9; ++jj) {
      const int j = jj == 0 ? 8 : jj - 1, G = j >> 2, k = j == 8 ? 4 : (j & 3);
#pragma unroll
      for (int e = 0; e < 4; ++e) y[j][e] = __builtin_elementwise_fma(y[j][e], f32x2{rs[G], rs[G]}, f32x2{nm[G], nm[G]});
      fence();
#pragma unroll
      for (int e = 0; e < 4; ++e) {
        const f32x4 gg = gv[k][e >> 1], bb = bv[k][e >> 1];
        y[j][e] = __builtin_elementwise_fma(y[j][e], (e & 1) ? f32x2{gg[2], gg[3]} : f32x2{gg[0], gg[1]}, (e & 1) ? f32x2{bb[2], bb[3]} : f32x2{bb[0], bb[1]});
      }
      fence();
      *(LDS_AS u32x4*)(aw[G] + (j == 8 ? 0 : (j & 3) * 16384)) = u32x4{pack2(y[j][0]), pack2(y[j][1]), pack2(y[j][2]), pack2(y[j][3])};
      fence();
    }
  }

  const unsigned wvoff = (unsigned)r7 * ROWB + (unsigned)(((((c7 >> 1) ^ (((r7 >> 1) & 1) | ((tid >> 5) & 2))) << 1) | (c7 & 1)) * 16);
  const unsigned wbase = h * 640u + (unsigned)wi * 8u * ROWB;

  if (wv >= 4) {
    wait_lgkm0();
    barrier();
    __builtin_amdgcn_s_setprio(2);
    auto req = [&](auto cc) {
      constexpr int c = decltype(cc)::v;
      unsigned wb = wbase, lb = ldsbase + (unsigned)wi * 1024u;
      asm volatile("" : "+s"(wb), "+s"(lb));
      dma3(lb + ((c + 3) % 12) * HSLOT, wvoff, srdw, wb + (c / 6) * 128u + (c % 6) * 96u * ROWB);
    };
    barrier(); static_for<3, 5>(req);
    barrier(); static_for<5, 6>(req);
    barrier(); static_for<6, 8>(req);
    static_for<0, 11>([&](auto qc) {
      constexpr int q = decltype(qc)::v;
      wait_vm<(q < 9 ? 12 : q == 9 ? 6 : 0)>();
      barrier();
      if constexpr (q < 8) static_for<2 * q + 8, 2 * q + 10>(req);
    });
  } else {
    {
      const unsigned hv = (unsigned)(lane >> 2) * ROWB + (unsigned)(((lane & 3) ^ ((lane >> 4) & 2)) * 16);
      const unsigned hs = 512u + h * 64u + (unsigned)wi * 16u * ROWB;
#pragma unroll
      for (int i = 0; i < 9; ++i) dma16(ldsbase + (i * 4 + wi) * 1024, hv, srdw, hs + (unsigned)(i * 64) * ROWB);
#pragma unroll
      for (int c = 0; c < 3; ++c) dma3(ldsbase + (c + 3) * HSLOT + (unsigned)wi * 1024u, wvoff, srdw, wbase + c * 96u * ROWB);
      const unsigned li = (unsigned)tid, kk = (li * 43u) >> 8, ln = li - kk * 6u;
      touch(ldsbase + SINK, li < 108u ? (jb * 18u + kk) * ROWB + (ln + 1u + (ln >= 3u) * 2u) * 128u : OOB, srdw);
    }
    const int m = wi >> 1, n = wi & 1;
    const int fr = lane & 15, fg = lane >> 4;
    bf16x8 xa[2][18];
    LDS_AS char* ard[2][2][2];
#pragma unroll
    for (int e = 0; e < 2; ++e) {
      const unsigned o = (m * 2 + (fr >> 3)) * 1024 + (fr & 7) * 128 + (((4 * e + fg) ^ (fr & 7)) * 16);
#pragma unroll
      for (int i = 0; i < 2; ++i) {
        ard[i][e][0] = opaque(lds + XRES + (i ^ n) * 4096 + o);
        ard[i][e][1] = opaque(lds + XRES + 7 * 8192 + (i ^ n) * 4096 + o);
      }
    }
    LDS_AS char* wrd[2][3];
#pragma unroll
    for (int t = 0; t < 2; ++t)
#pragma unroll
      for (int z = 0; z < 3; ++z)
        wrd[t][z] = opaque(lds + z * 4 * HSLOT + (8 * fg + (fr >> 2)) * 128 + (((2 * n + t) ^ (((fr >> 3) & 1) | ((fg & 1) << 1))) * 32) + (fr & 3) * 8);
    LDS_AS char* wh[2];
#pragma unroll
    for (int t = 0; t < 2; ++t) wh[t] = opaque(lds + (8 * fg + (fr >> 2)) * 64 + ((t ^ (fg & 1)) * 32) + (fr & 3) * 8);
    const unsigned ovoff = (unsigned)fr * ROWB + (unsigned)(2 * (fg & 1) + (fg >> 1)) * 16u;
    const unsigned orow = (row0 + m * 16) * ROWB;
    const unsigned osoff[2] = {orow + (unsigned)n * 32u * ROWB, orow + (unsigned)(n ^ 1) * 32u * ROWB};
    wait_lgkm0();
    wait_vm<16>();
    barrier();

    bf16x8 wf[2][6];
    f32x4 acc[2][2], fin[2][2];
    u32x4 po[4][2], ph;
    auto tr_frag = [&](LDS_AS char* p, int step) {
      const s16x4 lo = __builtin_amdgcn_ds_read_tr16_b64_v4i16((LDS_AS s16x4*)p);
      const s16x4 hi = __builtin_amdgcn_ds_read_tr16_b64_v4i16((LDS_AS s16x4*)(p + step));
      return __builtin_bit_cast(bf16x8, __builtin_shufflevector(lo, hi, 0, 1, 2, 3, 4, 5, 6, 7));
    };
    auto rd = [&](auto uc) {
      constexpr int u = decltype(uc)::v, hsl = (u - 3) % 12;
#pragma unroll
      for (int s = 0; s < 3; ++s) {
        if constexpr (u >= 6) {
#pragma unroll
          for (int t = 0; t < 2; ++t) wf[u & 1][s * 2 + t] = tr_frag(wrd[t][hsl >> 2] + (hsl & 3) * HSLOT + s * 4096, 512);
        } else {
          const int ks = u * 3 + s;
#pragma unroll
          for (int t = 0; t < 2; ++t) wf[u & 1][s * 2 + t] = tr_frag(wh[t] + ks * 2048, 256);
#pragma unroll
          for (int i = 0; i < 2; ++i) xa[i][ks] = *(LDS_AS bf16x8*)(ard[i][ks & 1][ks >= 14] + ((ks >> 1) % 7) * 8192);
        }
      }
    };
    auto mm = [&](auto uc) {
      constexpr int u = decltype(uc)::v;
#pragma unroll
      for (int f = 0; f < 6; ++f) {
        const int ks = (u % 6) * 3 + f / 2, t = f & 1;
#pragma unroll
        for (int i = 0; i < (u >= 6 ? 2 : 1); ++i)
          acc[i][t] = __builtin_amdgcn_mfma_f32_16x16x32_bf16(wf[u & 1][f], xa[i][ks], ks == 0 ? f32x4{0.f, 0.f, 0.f, 0.f} : acc[i][t], 0, 0, 0);
      }
    };
    auto pack = [&](const f32x4& a, const f32x4& c) {
      const auto lo = __builtin_amdgcn_permlane16_swap(pack2(f32x2{a[0], a[1]}), pack2(f32x2{c[0], c[1]}), false, false);
      const auto hi = __builtin_amdgcn_permlane16_swap(pack2(f32x2{a[2], a[3]}), pack2(f32x2{c[2], c[3]}), false, false);
      return u32x4{(unsigned)lo[0], (unsigned)hi[0], (unsigned)lo[1], (unsigned)hi[1]};
    };
    auto store = [&](int k) {
      if (k == 0) st16_through(ph, ovoff, srdo, osoff[0] + 512u + h * 64u);
      else st16_through(po[(k - 1) / 2][(k - 1) & 1], ovoff, srdo, osoff[(k - 1) & 1] + h * 640u + (unsigned)n * 64u + ((k - 1) / 2) * 128u);
    };
    rd(IC<0>{});
    fence();
    static_for<0, 30>([&](auto uc) {
      constexpr int u = decltype(uc)::v;
      constexpr bool pk = u == 6 || (u >= 12 && u % 6 < 2);
      if constexpr ((u & 1) && u < 29) {
        if constexpr (u < 8) wait_vm<(u == 1 ? 13 : u == 3 ? 10 : u == 5 ? 4 : 1)>();
        if constexpr (u < 6) wait_lgkm0();
        barrier();
        fence();
      }
      if constexpr (u < 29) rd(IC<u + 1>{});
      if constexpr (u == 6) ph = pack(fin[0][0], fin[0][1]);
      else if constexpr (pk) po[u / 6 - 2][u % 6] = pack(fin[u % 6][0], fin[u % 6][1]);
      mm(uc);
      if constexpr (u < 29) {
        static_for<0, (u < 6 ? 6 : 12)>([&](auto ic) {
          constexpr int i = decltype(ic)::v;
          __builtin_amdgcn_sched_group_barrier(0x008, 1, 0);
          __builtin_amdgcn_sched_group_barrier(0x100, u < 5 ? 3 : u == 5 ? 2 : 1, 0);
          if constexpr (pk && i >= 2 && i < 8) __builtin_amdgcn_sched_group_barrier(0x002, 1, 0);
        });
      }
      fence();
      if constexpr (u >= ST_FROM && u < ST_FROM + 7) { store(u - ST_FROM); fence(); }
      if constexpr (u % 6 == 5) {
#pragma unroll
        for (int i = 0; i < (u >= 6 ? 2 : 1); ++i) { fin[i][0] = acc[i][0]; fin[i][1] = acc[i][1]; }
      }
    });
    po[3][0] = pack(fin[0][0], fin[0][1]);
    po[3][1] = pack(fin[1][0], fin[1][1]);
    store(7);
    store(8);
  }
  asm volatile(".Lpf_end:");
}
}

extern "C" void kernel_launch(void* const* d_in, const int* in_sizes, int n_in, void* d_out, int out_size, void* d_ws,
                              size_t ws_size, hipStream_t stream) {
  (void)in_sizes; (void)n_in; (void)out_size; (void)d_ws; (void)ws_size;
  (void)hipFuncSetAttribute((const void*)ln_linear_kernel, hipFuncAttributeMaxDynamicSharedMemorySize, LDS_BYTES);
  ln_linear_kernel<<<256, 512, LDS_BYTES, stream>>>((const uint16_t*)d_in[0], (const uint16_t*)d_in[1], (const uint16_t*)d_in[2],
                                                    (const uint16_t*)d_in[3], (uint16_t*)d_out);
}
```

```cpp
#include <hip/hip_runtime.h>
#include <stdint.h>

typedef short s16x4 __attribute__((ext_vector_type(4)));
typedef __bf16 bf16x2 __attribute__((ext_vector_type(2)));
typedef __bf16 bf16x8 __attribute__((ext_vector_type(8)));
typedef float f32x2 __attribute__((ext_vector_type(2)));
typedef float f32x4 __attribute__((ext_vector_type(4)));
typedef unsigned u32x4 __attribute__((ext_vector_type(4)));
typedef unsigned u32x8 __attribute__((ext_vector_type(8)));
#define LDS_AS __attribute__((address_space(3)))

namespace {
constexpr int D = 576;
constexpr int ROWS = 8192;
constexpr int ROWB = D * 2;
constexpr int HSLOT = 12288;
constexpr int XRES = 6 * HSLOT;
constexpr int GBB = 12 * HSLOT;
constexpr int FLAG = GBB + 8192;
constexpr int SINK = FLAG + 1024;
constexpr int LDS_BYTES = SINK + 1024;
constexpr int GQ_POS = 1;
constexpr int GB_PIECE = 5;
constexpr int ST_FROM = 22;
constexpr unsigned OOB = 0xfffffff0u;

template <int V> struct IC { static constexpr int v = V; };
template <int I, int N, class F> __device__ __forceinline__ void static_for(F&& f) {
  if constexpr (I < N) { f(IC<I>{}); static_for<I + 1, N>(f); }
}
__device__ __forceinline__ u32x4 make_srd(unsigned lo, unsigned hi, unsigned bytes) { return u32x4{lo, hi & 0xffffu, bytes, 0x00020000u}; }
template <int N> __device__ __forceinline__ void wait_vm() { asm volatile("s_waitcnt vmcnt(%0)" ::"i"(N) : "memory"); }
template <int N> __device__ __forceinline__ void wait_vm_for(u32x4& v) { asm volatile("s_waitcnt vmcnt(%1)" : "+v"(v) : "i"(N)); }
__device__ __forceinline__ void wait_lgkm0() { asm volatile("s_waitcnt lgkmcnt(0)" ::: "memory"); }
__device__ __forceinline__ void barrier() { asm volatile("s_barrier" ::: "memory"); }
__device__ __forceinline__ void fence() { __builtin_amdgcn_sched_barrier(0); }
__device__ __forceinline__ void dma16(unsigned lds_addr, unsigned voff, u32x4 srd, unsigned soff) {
  asm volatile("s_mov_b32 m0, %0\n\ts_nop 0\n\tbuffer_load_dwordx4 %1, %2, %3 offen lds" ::"s"(lds_addr), "v"(voff), "s"(srd), "s"(soff) : "memory");
}
__device__ __forceinline__ void dma3(unsigned lds_addr, unsigned voff, u32x4 srd, unsigned soff) {
  unsigned s1, s2;
  asm volatile("s_mov_b32 m0, %2\n\ts_add_u32 %0, %5, 0x9000\n\tbuffer_load_dwordx4 %3, %4, %5 offen lds\n\t"
               "s_add_u32 m0, m0, 0x1000\n\ts_add_u32 %1, %5, 0x12000\n\tbuffer_load_dwordx4 %3, %4, %0 offen lds\n\t"
               "s_add_u32 m0, m0, 0x1000\n\ts_nop 0\n\tbuffer_load_dwordx4 %3, %4, %1 offen lds"
               : "=&s"(s1), "=&s"(s2) : "s"(lds_addr), "v"(voff), "s"(srd), "s"(soff) : "memory", "scc");
}
__device__ __forceinline__ void touch(unsigned lds_addr, unsigned voff, u32x4 srd) {
  asm volatile("s_mov_b32 m0, %0\n\ts_nop 0\n\tbuffer_load_dword %1, %2, 0 offen lds" ::"s"(lds_addr), "v"(voff), "s"(srd) : "memory");
}
__device__ __forceinline__ void st16_through(u32x4 v, unsigned voff, u32x4 srd, unsigned soff) {
  asm volatile("buffer_store_dwordx4 %0, %1, %2, %3 offen sc1\n\ts_nop 1" ::"v"(v), "v"(voff), "s"(srd), "s"(soff) : "memory");
}
template <int OFF> __device__ __forceinline__ void ldx(u32x4& dst, unsigned voff, unsigned long long base) {
  asm volatile("global_load_dwordx4 %0, %1, %2 offset:%3" : "=v"(dst) : "v"(voff), "s"(base), "i"(OFF));
}
__device__ __forceinline__ void ldg(u32x4& dst, unsigned voff, u32x4 srd) {
  asm volatile("buffer_load_dwordx4 %0, %1, %2, 0 offen" : "=v"(dst) : "v"(voff), "s"(srd));
}
__device__ __forceinline__ void ts_raw(unsigned long long& t) { asm volatile("s_memtime %0" : "=s"(t)); }
template <int I> __device__ __forceinline__ void ts_lane(unsigned& tsv, unsigned long long t) { asm volatile("" : "+s"(t)); asm volatile("v_writelane_b32 %0, %1, %2" : "+v"(tsv) : "s"((unsigned)t), "i"(I)); }
template <int I> __device__ __forceinline__ void ts_put(unsigned& tsv) {
  unsigned long long t_; asm volatile("s_memtime %0\n\ts_waitcnt lgkmcnt(0)" : "=s"(t_) :: "memory");
  asm volatile("v_writelane_b32 %0, %1, %2" : "+v"(tsv) : "s"((unsigned)t_), "i"(I));
}
template <int CTRL> __device__ __forceinline__ float dpp_add(float v) {
  return v + __builtin_bit_cast(float, __builtin_amdgcn_update_dpp(0, __builtin_bit_cast(int, v), CTRL, 0xf, 0xf, true));
}
__device__ __forceinline__ float oct_sum(float v) {
  v = dpp_add<0xB1>(v); v = dpp_add<0x4E>(v); v = dpp_add<0x141>(v);
  return v;
}
__device__ __forceinline__ unsigned pack2(f32x2 v) { return __builtin_bit_cast(unsigned, __builtin_convertvector(v, bf16x2)); }
__device__ __forceinline__ f32x2 unpack2(unsigned u) { return f32x2{__uint_as_float(u << 16), __uint_as_float(u & 0xffff0000u)}; }
template <int N> __device__ __forceinline__ void piece_stats(const u32x4& x, float& s, float& q, f32x2 (&y)[4]) {
  float a0, a1, a2, a3, a4, a5, a6, a7;
  const unsigned x0 = x[0], x1 = x[1], x2 = x[2], x3 = x[3];
  asm volatile(
      "s_waitcnt vmcnt(%16)\n\t"
      "v_dot2c_f32_bf16 %0, %14, %10\n\tv_dot2c_f32_bf16 %1, %10, %10\n\t"
      "v_dot2c_f32_bf16 %0, %14, %11\n\tv_dot2c_f32_bf16 %1, %11, %11\n\t"
      "v_dot2c_f32_bf16 %0, %14, %12\n\tv_dot2c_f32_bf16 %1, %12, %12\n\t"
      "v_dot2c_f32_bf16 %0, %14, %13\n\tv_dot2c_f32_bf16 %1, %13, %13\n\t"
      "v_lshlrev_b32 %2, 16, %10\n\tv_and_b32 %3, %15, %10\n\t"
      "v_lshlrev_b32 %4, 16, %11\n\tv_and_b32 %5, %15, %11\n\t"
      "v_lshlrev_b32 %6, 16, %12\n\tv_and_b32 %7, %15, %12\n\t"
      "v_lshlrev_b32 %8, 16, %13\n\tv_and_b32 %9, %15, %13"
      : "+v"(s), "+v"(q), "=&v"(a0), "=&v"(a1), "=&v"(a2), "=&v"(a3), "=&v"(a4), "=&v"(a5), "=&v"(a6), "=&v"(a7)
      : "v"(x0), "v"(x1), "v"(x2), "v"(x3), "s"(0x3f803f80u), "s"(0xffff0000u), "i"(N));
  y[0] = f32x2{a0, a1}; y[1] = f32x2{a2, a3}; y[2] = f32x2{a4, a5}; y[3] = f32x2{a6, a7};
}
__device__ __forceinline__ void count_in(unsigned lds_addr) {
  asm volatile("s_mov_b64 exec, 1\n\tds_add_u32 %0, %1\n\ts_mov_b64 exec, -1" ::"v"(lds_addr), "v"(1u) : "memory");
}
__device__ __forceinline__ void retire2(unsigned a, unsigned b) { asm volatile("s_waitcnt lgkmcnt(0)" ::"s"(a), "s"(b) : "memory"); }
__device__ __forceinline__ void pinu(unsigned& v) { asm volatile("" : "+v"(v)); }
__device__ __forceinline__ void pinf(float& v) { asm volatile("" : "+v"(v)); }
__device__ __forceinline__ LDS_AS char* opaque(LDS_AS char* p) { asm volatile("" : "+v"(p)); return p; }
template <int MASK> __device__ __forceinline__ float sel8(float v) {
  const int i = __builtin_bit_cast(int, v);
  return __builtin_bit_cast(float, __builtin_amdgcn_update_dpp(i, i, 0x128, 0xf, MASK, false));
}
__device__ __forceinline__ float fold8(float a, float b) {
  float t;
  asm("s_nop 1\n\tv_add_f32_dpp %0, %1, %1 row_ror:8 row_mask:0xf bank_mask:0x3\n\tv_add_f32_dpp %0, %2, %2 row_ror:8 row_mask:0xf bank_mask:0xc" : "=&v"(t) : "v"(a), "v"(b));
  return t;
}

#define STAMPS 0
#define TS(i) do {} while (0)
#define TS2(i) do {} while (0)

__global__ __launch_bounds__(512, 2) void ln_linear_kernel(const uint16_t* __restrict__ x, const uint16_t* __restrict__ g,
                                                           const uint16_t* __restrict__ b, const uint16_t* __restrict__ w,
                                                           uint16_t* __restrict__ out
) {
  extern __shared__ __attribute__((aligned(1024))) char smem[];
  LDS_AS char* lds = (LDS_AS char*)smem;
  unsigned long long kx;
  u32x8 ka;
  asm volatile("s_load_dwordx2 %0, %2, 0x0\n\ts_load_dwordx8 %1, %2, 0x8" : "=&s"(kx), "=&s"(ka) : "s"(__builtin_amdgcn_kernarg_segment_ptr()));
  const int tid = threadIdx.x, lane = tid & 63;
  const unsigned bid = blockIdx.x;
  const unsigned row0 = (bid & 7) * 1024 + (bid >> 4) * 64;
  unsigned xvt = (unsigned)(tid >> 3) * ROWB + (unsigned)(tid & 7) * 16, xvm = (unsigned)(tid >> 4) * 2 * ROWB + (unsigned)(tid & 15) * 16;
  unsigned xo = row0 * ROWB;
  const unsigned t0 = __builtin_amdgcn_readfirstlane(tid);
  asm volatile("" : "+v"(xvt), "+v"(xvm));
  asm volatile("" : "+s"(xo));
  asm volatile("s_waitcnt lgkmcnt(0)" : "+s"(kx), "+s"(ka));
  u32x4 xr[9], gq;
  const unsigned long long xb = kx + xo;
  auto load_x = [&](unsigned glo, unsigned ghi, unsigned gbytes) {
    ldx<1024>(xr[8], xvt, xb);
    static_for<0, GQ_POS>([&](auto jc) { constexpr int j = decltype(jc)::v; ldx<(j >> 2) * ROWB + (j & 3) * 256>(xr[j], xvm, xb); });
    fence();
    ldg(gq, (unsigned)(tid & 127) * 16u, make_srd(glo, ghi, gbytes));
    static_for<GQ_POS, 8>([&](auto jc) { constexpr int j = decltype(jc)::v; ldx<(j >> 2) * ROWB + (j & 3) * 256>(xr[j], xvm, xb); });
    fence();
  };
  if (t0 < 256) load_x(t0 < 128 ? ka[0] : ka[2], t0 < 128 ? ka[1] : ka[3], ROWB);
  fence();
  unsigned pf0 = 0, pf1 = 0;
  if (t0 >= 256) {
    *(LDS_AS unsigned*)(lds + FLAG) = 0u;
    unsigned long long pc;
    unsigned len;
    asm volatile("s_getpc_b64 %0\n.Lpf_here:\n\ts_mov_b32 %1, .Lpf_end-.Lpf_here-128" : "=s"(pc), "=s"(len));
    const unsigned o = min((bid >> 3) * 128u + (t0 & 192u) * 64u, len);
    asm volatile("s_waitcnt lgkmcnt(0)\n\ts_load_dword %0, %2, %3\n\ts_load_dword %1, %2, %3 offset:64" : "=&s"(pf0), "=&s"(pf1) : "s"(pc), "s"(o) : "memory");
  }
  const int wv = t0 >> 6, wi = wv & 3;
  const unsigned jb = bid >> 3, h = jb & 1;
  const int r7 = lane >> 3, c7 = lane & 7, c15 = lane & 15, rq = lane >> 4;
  const unsigned ldsbase = (unsigned)(uintptr_t)lds;
  const u32x4 srdw = make_srd(ka[4], ka[5], (unsigned)D * ROWB), srdo = make_srd(ka[6], ka[7], (unsigned)ROWS * ROWB);
  LDS_AS char* aw[3] = {opaque(lds + XRES + wv * 1024 + (c15 >> 3) * 8192 + (2 * rq) * 128 + ((c7 ^ (2 * rq)) * 16)),
                        opaque(lds + XRES + wv * 1024 + (c15 >> 3) * 8192 + (2 * rq + 1) * 128 + ((c7 ^ (2 * rq + 1)) * 16)),
                        opaque(lds + XRES + 8 * 8192 + wv * 1024 + r7 * 128 + ((c7 ^ r7) * 16))};
  LDS_AS char* gr = opaque(lds + GBB + c15 * 16);
  LDS_AS char* grt = opaque(lds + GBB + 2048 + c7 * 16);
  float sm[3] = {0.f, 0.f, 0.f}, sq[3] = {0.f, 0.f, 0.f};
#pragma unroll
  for (int i = 0; i < 3; ++i) { pinf(sm[i]); pinf(sq[i]); }
  unsigned tvoff = (wv >= 4 && wv < 7 && lane < 18) ? (jb * 18u + (unsigned)lane) * ROWB + (wv == 4 ? 512u : wv == 5 ? 0u : 640u) : OOB;
  pinu(tvoff);
  if (wv < 4) touch(ldsbase + SINK, tvoff, srdw);
  barrier();
  if (wv >= 4) {
    load_x(ka[0], ka[1], 0u);
    touch(ldsbase + SINK, tvoff, srdw);
  }

  {
    f32x4 gv[5][2], bv[5][2];
    f32x2 y[9][4];
    fence();
    static_for<0, 9>([&](auto jc) {
      constexpr int jj = decltype(jc)::v, j = jj == 0 ? 8 : jj - 1;
      piece_stats<9 - jj + (jj <= GQ_POS ? 1 : 0)>(xr[j], sm[j >> 2], sq[j >> 2], y[j]);
      if constexpr (jj == GQ_POS) {
        wait_vm_for<9 - jj>(gq);
        if (wv < 4) {
          const unsigned g0 = gq[0], g1 = gq[1], g2 = gq[2], g3 = gq[3];
          const f32x2 a0 = unpack2(g0), a1 = unpack2(g1), a2 = unpack2(g2), a3 = unpack2(g3);
          *(LDS_AS f32x4*)(lds + GBB + wi * 2048 + rq * 512 + c15 * 16) = f32x4{a0[0], a0[1], a1[0], a1[1]};
          *(LDS_AS f32x4*)(lds + GBB + wi * 2048 + rq * 512 + c15 * 16 + 256) = f32x4{a2[0], a2[1], a3[0], a3[1]};
          count_in(ldsbase + FLAG);
        }
        fence();
      }
      if constexpr (jj == GB_PIECE) {
        while (__builtin_amdgcn_readfirstlane(*(volatile LDS_AS unsigned*)(lds + FLAG)) < 4u) __builtin_amdgcn_s_sleep(1);
        retire2(pf0, pf1);
      }
      if constexpr (jj >= GB_PIECE) {
#pragma unroll
        for (int kk = jj - GB_PIECE; kk < (jj == 8 ? 5 : jj - GB_PIECE + 1); ++kk) {
          const int k = kk == 0 ? 4 : kk - 1;
          LDS_AS char* p = k < 4 ? gr + k * 512 : grt;
#pragma unroll
          for (int e = 0; e < 2; ++e) { gv[k][e] = *(LDS_AS f32x4*)(p + e * 256); bv[k][e] = *(LDS_AS f32x4*)(p + 4096 + e * 256); }
        }
        fence();
      }
    });
    fence();
    TS(3);
    const float mean = oct_sum(fold8(sm[0], sm[1]) + sm[2]) * (1.0f / D);
    const float var = fmaxf(oct_sum(fold8(sq[0], sq[1]) + sq[2]) * (1.0f / D) - mean * mean, 0.f);
    float rs[3], nm[3];
    rs[2] = __builtin_amdgcn_rsqf(var + 1e-5f);
    nm[2] = -mean * rs[2];
    rs[0] = sel8<0xc>(rs[2]); rs[1] = sel8<0x3>(rs[2]);
    nm[0] = sel8<0xc>(nm[2]); nm[1] = sel8<0x3>(nm[2]);
#pragma unroll
    for (int jj = 0; jj < 9; ++jj) {
      const int j = jj == 0 ? 8 : jj - 1, G = j >> 2, k = j == 8 ? 4 : (j & 3);
#pragma unroll
      for (int e = 0; e < 4; ++e) y[j][e] = __builtin_elementwise_fma(y[j][e], f32x2{rs[G], rs[G]}, f32x2{nm[G], nm[G]});
      fence();
#pragma unroll
      for (int e = 0; e < 4; ++e) {
        const f32x4 gg = gv[k][e >> 1], bb = bv[k][e >> 1];
        y[j][e] = __builtin_elementwise_fma(y[j][e], (e & 1) ? f32x2{gg[2], gg[3]} : f32x2{gg[0], gg[1]}, (e & 1) ? f32x2{bb[2], bb[3]} : f32x2{bb[0], bb[1]});
      }
      fence();
      *(LDS_AS u32x4*)(aw[G] + (j == 8 ? 0 : (j & 3) * 16384)) = u32x4{pack2(y[j][0]), pack2(y[j][1]), pack2(y[j][2]), pack2(y[j][3])};
      fence();
    }
  }

  TS(4);
  asm volatile("s_cmp_lt_u32 %0, 4\n\ts_cbranch_scc1 .Lskip_b\n\ts_waitcnt lgkmcnt(0)\n\ts_barrier\n.Lskip_b:" ::"s"(wv) : "memory", "scc");
  const unsigned wvoff = (unsigned)r7 * ROWB + (unsigned)(((((c7 >> 1) ^ (((r7 >> 1) & 1) | ((tid >> 5) & 2))) << 1) | (c7 & 1)) * 16);
  const unsigned wbase = h * 640u + (unsigned)wi * 8u * ROWB;

  if (wv >= 4) {
    __builtin_amdgcn_s_setprio(2);
    auto req = [&](auto cc) {
      constexpr int c = decltype(cc)::v;
      unsigned wb = wbase, lb = ldsbase + (unsigned)wi * 1024u;
      asm volatile("" : "+s"(wb), "+s"(lb));
      dma3(lb + ((c + 3) % 12) * HSLOT, wvoff, srdw, wb + (c / 6) * 128u + (c % 6) * 96u * ROWB);
    };
    barrier(); static_for<3, 5>(req);
    barrier(); static_for<5, 6>(req);
    barrier(); static_for<6, 8>(req);
    static_for<0, 11>([&](auto qc) {
      constexpr int q = decltype(qc)::v;
      wait_vm<(q < 9 ? 12 : q == 9 ? 6 : 0)>();
      barrier();
      if constexpr (q < 8) static_for<2 * q + 8, 2 * q + 10>(req);
    });
  } else {
    {
      const unsigned hv = (unsigned)(lane >> 2) * ROWB + (unsigned)(((lane & 3) ^ ((lane >> 4) & 2)) * 16);
      const unsigned hs = 512u + h * 64u + (unsigned)wi * 16u * ROWB;
#pragma unroll
      for (int i = 0; i < 9; ++i) dma16(ldsbase + (i * 4 + wi) * 1024, hv, srdw, hs + (unsigned)(i * 64) * ROWB);
#pragma unroll
      for (int c = 0; c < 3; ++c) dma3(ldsbase + (c + 3) * HSLOT + (unsigned)wi * 1024u, wvoff, srdw, wbase + c * 96u * ROWB);
      const unsigned li = (unsigned)tid, kk = (li * 43u) >> 8, ln = li - kk * 6u;
      touch(ldsbase + SINK, li < 108u ? (jb * 18u + kk) * ROWB + (ln + 1u + (ln >= 3u) * 2u) * 128u : OOB, srdw);
    }
    const int m = wi >> 1, n = wi & 1;
    const int fr = lane & 15, fg = lane >> 4;
    bf16x8 xa[2][18];
    LDS_AS char* ard[2][2][2];
#pragma unroll
    for (int e = 0; e < 2; ++e) {
      const unsigned o = (m * 2 + (fr >> 3)) * 1024 + (fr & 7) * 128 + (((4 * e + fg) ^ (fr & 7)) * 16);
#pragma unroll
      for (int i = 0; i < 2; ++i) {
        ard[i][e][0] = opaque(lds + XRES + (i ^ n) * 4096 + o);
        ard[i][e][1] = opaque(lds + XRES + 7 * 8192 + (i ^ n) * 4096 + o);
      }
    }
    LDS_AS char* wrd[2][3];
#pragma unroll
    for (int t = 0; t < 2; ++t)
#pragma unroll
      for (int z = 0; z < 3; ++z)
        wrd[t][z] = opaque(lds + z * 4 * HSLOT + (8 * fg + (fr >> 2)) * 128 + (((2 * n + t) ^ (((fr >> 3) & 1) | ((fg & 1) << 1))) * 32) + (fr & 3) * 8);
    LDS_AS char* wh[2];
#pragma unroll
    for (int t = 0; t < 2; ++t) wh[t] = opaque(lds + (8 * fg + (fr >> 2)) * 64 + ((t ^ (fg & 1)) * 32) + (fr & 3) * 8);
    const unsigned ovoff = (unsigned)fr * ROWB + (unsigned)(2 * (fg & 1) + (fg >> 1)) * 16u;
    const unsigned orow = (row0 + m * 16) * ROWB;
    const unsigned osoff[2] = {orow + (unsigned)n * 32u * ROWB, orow + (unsigned)(n ^ 1) * 32u * ROWB};
    wait_lgkm0();
    wait_vm<16>();
    barrier();
    TS(6);

    bf16x8 wf[2][6];
    f32x4 acc[2][2], fin[2][2];
    u32x4 po[4][2], ph;
    auto tr_frag = [&](LDS_AS char* p, int step) {
      const s16x4 lo = __builtin_amdgcn_ds_read_tr16_b64_v4i16((LDS_AS s16x4*)p);
      const s16x4 hi = __builtin_amdgcn_ds_read_tr16_b64_v4i16((LDS_AS s16x4*)(p + step));
      return __builtin_bit_cast(bf16x8, __builtin_shufflevector(lo, hi, 0, 1, 2, 3, 4, 5, 6, 7));
    };
    auto rd = [&](auto uc) {
      constexpr int u = decltype(uc)::v, hsl = (u - 3) % 12;
#pragma unroll
      for (int s = 0; s < 3; ++s) {
        if constexpr (u >= 6) {
#pragma unroll
          for (int t = 0; t < 2; ++t) wf[u & 1][s * 2 + t] = tr_frag(wrd[t][hsl >> 2] + (hsl & 3) * HSLOT + s * 4096, 512);
        } else {
          const int ks = u * 3 + s;
#pragma unroll
          for (int t = 0; t < 2; ++t) wf[u & 1][s * 2 + t] = tr_frag(wh[t] + ks * 2048, 256);
#pragma unroll
          for (int i = 0; i < 2; ++i) xa[i][ks] = *(LDS_AS bf16x8*)(ard[i][ks & 1][ks >= 14] + ((ks >> 1) % 7) * 8192);
        }
      }
    };
    auto mm = [&](auto uc) {
      constexpr int u = decltype(uc)::v;
#pragma unroll
      for (int f = 0; f < 6; ++f) {
        const int ks = (u % 6) * 3 + f / 2, t = f & 1;
#pragma unroll
        for (int i = 0; i < (u >= 6 ? 2 : 1); ++i)
          acc[i][t] = __builtin_amdgcn_mfma_f32_16x16x32_bf16(wf[u & 1][f], xa[i][ks], ks == 0 ? f32x4{0.f, 0.f, 0.f, 0.f} : acc[i][t], 0, 0, 0);
      }
    };
    auto pack = [&](const f32x4& a, const f32x4& c) {
      const auto lo = __builtin_amdgcn_permlane16_swap(pack2(f32x2{a[0], a[1]}), pack2(f32x2{c[0], c[1]}), false, false);
      const auto hi = __builtin_amdgcn_permlane16_swap(pack2(f32x2{a[2], a[3]}), pack2(f32x2{c[2], c[3]}), false, false);
      return u32x4{(unsigned)lo[0], (unsigned)hi[0], (unsigned)lo[1], (unsigned)hi[1]};
    };
    auto store = [&](int k) {
      if (k == 0) st16_through(ph, ovoff, srdo, osoff[0] + 512u + h * 64u);
      else st16_through(po[(k - 1) / 2][(k - 1) & 1], ovoff, srdo, osoff[(k - 1) & 1] + h * 640u + (unsigned)n * 64u + ((k - 1) / 2) * 128u);
    };
    rd(IC<0>{});
    fence();
    static_for<0, 30>([&](auto uc) {
      constexpr int u = decltype(uc)::v;
      constexpr bool pk = u == 6 || (u >= 12 && u % 6 < 2);
      if constexpr ((u & 1) && u < 29) {
        if constexpr (u < 8) wait_vm<(u == 1 ? 13 : u == 3 ? 10 : u == 5 ? 4 : 1)>();
        if constexpr (u < 6) wait_lgkm0();
        barrier();
        fence();
      }
      if constexpr (u < 29) rd(IC<u + 1>{});
      if constexpr (u == 6) ph = pack(fin[0][0], fin[0][1]);
      else if constexpr (pk) po[u / 6 - 2][u % 6] = pack(fin[u % 6][0], fin[u % 6][1]);
      mm(uc);
      if constexpr (u < 29) {
        static_for<0, (u < 6 ? 6 : 12)>([&](auto ic) {
          constexpr int i = decltype(ic)::v;
          __builtin_amdgcn_sched_group_barrier(0x008, 1, 0);
          __builtin_amdgcn_sched_group_barrier(0x100, u < 5 ? 3 : u == 5 ? 2 : 1, 0);
          if constexpr (pk && i >= 2 && i < 8) __builtin_amdgcn_sched_group_barrier(0x002, 1, 0);
        });
      }
      fence();
      if constexpr (u >= ST_FROM && u < ST_FROM + 7) { store(u - ST_FROM); fence(); }
      if constexpr (u % 6 == 5) {
#pragma unroll
        for (int i = 0; i < (u >= 6 ? 2 : 1); ++i) { fin[i][0] = acc[i][0]; fin[i][1] = acc[i][1]; }
      }
    });
    po[3][0] = pack(fin[0][0], fin[0][1]);
    po[3][1] = pack(fin[1][0], fin[1][1]);
    store(7);
    store(8);
    TS(9);
  }
  asm volatile(".Lpf_end:");
}
}

extern "C" void kernel_launch(void* const* d_in, const int* in_sizes, int n_in, void* d_out, int out_size, void* d_ws,
                              size_t ws_size, hipStream_t stream) {
  (void)in_sizes; (void)n_in; (void)out_size; (void)d_ws; (void)ws_size;
  (void)hipFuncSetAttribute((const void*)ln_linear_kernel, hipFuncAttributeMaxDynamicSharedMemorySize, LDS_BYTES);
  ln_linear_kernel<<<256, 512, LDS_BYTES, stream>>>((const uint16_t*)d_in[0], (const uint16_t*)d_in[1], (const uint16_t*)d_in[2],
                                                    (const uint16_t*)d_in[3], (uint16_t*)d_out
);
}
```

```cpp
#include <hip/hip_runtime.h>
#include <stdint.h>

typedef short s16x4 __attribute__((ext_vector_type(4)));
typedef __bf16 bf16x2 __attribute__((ext_vector_type(2)));
typedef __bf16 bf16x8 __attribute__((ext_vector_type(8)));
typedef float f32x2 __attribute__((ext_vector_type(2)));
typedef float f32x4 __attribute__((ext_vector_type(4)));
typedef unsigned u32x4 __attribute__((ext_vector_type(4)));
typedef unsigned u32x8 __attribute__((ext_vector_type(8)));
#define LDS_AS __attribute__((address_space(3)))

namespace {
constexpr int D = 576;
constexpr int ROWS = 8192;
constexpr int ROWB = D * 2;
constexpr int HSLOT = 12288;
constexpr int XRES = 6 * HSLOT;
constexpr int GBB = 12 * HSLOT;
constexpr int FLAG = GBB + 8192;
constexpr int SINK = FLAG + 1024;
constexpr int LDS_BYTES = SINK + 1024;
constexpr int GQ_POS = 1;
constexpr int GB_PIECE = 5;
constexpr int ST_FROM = 22;
constexpr unsigned OOB = 0xfffffff0u;

template <int V> struct IC { static constexpr int v = V; };
template <int I, int N, class F> __device__ __forceinline__ void static_for(F&& f) {
  if constexpr (I < N) { f(IC<I>{}); static_for<I + 1, N>(f); }
}
__device__ __forceinline__ u32x4 make_srd(unsigned lo, unsigned hi, unsigned bytes) { return u32x4{lo, hi & 0xffffu, bytes, 0x00020000u}; }
template <int N> __device__ __forceinline__ void wait_vm() { asm volatile("s_waitcnt vmcnt(%0)" ::"i"(N) : "memory"); }
template <int N> __device__ __forceinline__ void wait_vm_for(u32x4& v) { asm volatile("s_waitcnt vmcnt(%1)" : "+v"(v) : "i"(N)); }
__device__ __forceinline__ void wait_lgkm0() { asm volatile("s_waitcnt lgkmcnt(0)" ::: "memory"); }
__device__ __forceinline__ void barrier() { asm volatile("s_barrier" ::: "memory"); }
__device__ __forceinline__ void fence() { __builtin_amdgcn_sched_barrier(0); }
__device__ __forceinline__ void dma16(unsigned lds_addr, unsigned voff, u32x4 srd, unsigned soff) {
  asm volatile("s_mov_b32 m0, %0\n\ts_nop 0\n\tbuffer_load_dwordx4 %1, %2, %3 offen lds" ::"s"(lds_addr), "v"(voff), "s"(srd), "s"(soff) : "memory");
}
__device__ __forceinline__ void dma3(unsigned lds_addr, unsigned voff, u32x4 srd, unsigned soff) {
  unsigned s1, s2;
  asm volatile("s_mov_b32 m0, %2\n\ts_add_u32 %0, %5, 0x9000\n\tbuffer_load_dwordx4 %3, %4, %5 offen lds\n\t"
               "s_add_u32 m0, m0, 0x1000\n\ts_add_u32 %1, %5, 0x12000\n\tbuffer_load_dwordx4 %3, %4, %0 offen lds\n\t"
               "s_add_u32 m0, m0, 0x1000\n\ts_nop 0\n\tbuffer_load_dwordx4 %3, %4, %1 offen lds"
               : "=&s"(s1), "=&s"(s2) : "s"(lds_addr), "v"(voff), "s"(srd), "s"(soff) : "memory", "scc");
}
__device__ __forceinline__ void touch(unsigned lds_addr, unsigned voff, u32x4 srd) {
  asm volatile("s_mov_b32 m0, %0\n\ts_nop 0\n\tbuffer_load_dword %1, %2, 0 offen lds" ::"s"(lds_addr), "v"(voff), "s"(srd) : "memory");
}
__device__ __forceinline__ void st16_through(u32x4 v, unsigned voff, u32x4 srd, unsigned soff) {
  asm volatile("buffer_store_dwordx4 %0, %1, %2, %3 offen sc1\n\ts_nop 1" ::"v"(v), "v"(voff), "s"(srd), "s"(soff) : "memory");
}
template <int OFF> __device__ __forceinline__ void ldx(u32x4& dst, unsigned voff, unsigned long long base) {
  asm volatile("global_load_dwordx4 %0, %1, %2 offset:%3" : "=v"(dst) : "v"(voff), "s"(base), "i"(OFF));
}
__device__ __forceinline__ void ldg(u32x4& dst, unsigned voff, u32x4 srd) {
  asm volatile("buffer_load_dwordx4 %0, %1, %2, 0 offen" : "=v"(dst) : "v"(voff), "s"(srd));
}
template <int CTRL> __device__ __forceinline__ float dpp_add(float v) {
  return v + __builtin_bit_cast(float, __builtin_amdgcn_update_dpp(0, __builtin_bit_cast(int, v), CTRL, 0xf, 0xf, true));
}
__device__ __forceinline__ float oct_sum(float v) {
  v = dpp_add<0xB1>(v); v = dpp_add<0x4E>(v); v = dpp_add<0x141>(v);
  return v;
}
__device__ __forceinline__ unsigned pack2(f32x2 v) { return __builtin_bit_cast(unsigned, __builtin_convertvector(v, bf16x2)); }
__device__ __forceinline__ f32x2 unpack2(unsigned u) { return f32x2{__uint_as_float(u << 16), __uint_as_float(u & 0xffff0000u)}; }
template <int N> __device__ __forceinline__ void piece_stats(const u32x4& x, float& s, float& q, f32x2 (&y)[4]) {
  float a0, a1, a2, a3, a4, a5, a6, a7;
  const unsigned x0 = x[0], x1 = x[1], x2 = x[2], x3 = x[3];
  asm volatile(
      "s_waitcnt vmcnt(%16)\n\t"
      "v_dot2c_f32_bf16 %0, %14, %10\n\tv_dot2c_f32_bf16 %1, %10, %10\n\t"
      "v_dot2c_f32_bf16 %0, %14, %11\n\tv_dot2c_f32_bf16 %1, %11, %11\n\t"
      "v_dot2c_f32_bf16 %0, %14, %12\n\tv_dot2c_f32_bf16 %1, %12, %12\n\t"
      "v_dot2c_f32_bf16 %0, %14, %13\n\tv_dot2c_f32_bf16 %1, %13, %13\n\t"
      "v_lshlrev_b32 %2, 16, %10\n\tv_and_b32 %3, %15, %10\n\t"
      "v_lshlrev_b32 %4, 16, %11\n\tv_and_b32 %5, %15, %11\n\t"
      "v_lshlrev_b32 %6, 16, %12\n\tv_and_b32 %7, %15, %12\n\t"
      "v_lshlrev_b32 %8, 16, %13\n\tv_and_b32 %9, %15, %13"
      : "+v"(s), "+v"(q), "=&v"(a0), "=&v"(a1), "=&v"(a2), "=&v"(a3), "=&v"(a4), "=&v"(a5), "=&v"(a6), "=&v"(a7)
      : "v"(x0), "v"(x1), "v"(x2), "v"(x3), "s"(0x3f803f80u), "s"(0xffff0000u), "i"(N));
  y[0] = f32x2{a0, a1}; y[1] = f32x2{a2, a3}; y[2] = f32x2{a4, a5}; y[3] = f32x2{a6, a7};
}
__device__ __forceinline__ void count_in(unsigned lds_addr) {
  asm volatile("s_mov_b64 exec, 1\n\tds_add_u32 %0, %1\n\ts_mov_b64 exec, -1" ::"v"(lds_addr), "v"(1u) : "memory");
}
__device__ __forceinline__ void retire2(unsigned a, unsigned b) { asm volatile("s_waitcnt lgkmcnt(0)" ::"s"(a), "s"(b) : "memory"); }
__device__ __forceinline__ void pinf(float& v) { asm volatile("" : "+v"(v)); }
__device__ __forceinline__ LDS_AS char* opaque(LDS_AS char* p) { asm volatile("" : "+v"(p)); return p; }
template <int MASK> __device__ __forceinline__ float sel8(float v) {
  const int i = __builtin_bit_cast(int, v);
  return __builtin_bit_cast(float, __builtin_amdgcn_update_dpp(i, i, 0x128, 0xf, MASK, false));
}
__device__ __forceinline__ float fold8(float a, float b) {
  float t;
  asm("s_nop 1\n\tv_add_f32_dpp %0, %1, %1 row_ror:8 row_mask:0xf bank_mask:0x3\n\tv_add_f32_dpp %0, %2, %2 row_ror:8 row_mask:0xf bank_mask:0xc" : "=&v"(t) : "v"(a), "v"(b));
  return t;
}

__global__ __launch_bounds__(512, 2) void ln_linear_kernel(const uint16_t* __restrict__ x, const uint16_t* __restrict__ g,
                                                           const uint16_t* __restrict__ b, const uint16_t* __restrict__ w,
                                                           uint16_t* __restrict__ out) {
  extern __shared__ __attribute__((aligned(1024))) char smem[];
  LDS_AS char* lds = (LDS_AS char*)smem;
  unsigned long long kx;
  u32x8 ka;
  asm volatile("s_load_dwordx2 %0, %2, 0x0\n\ts_load_dwordx8 %1, %2, 0x8" : "=&s"(kx), "=&s"(ka) : "s"(__builtin_amdgcn_kernarg_segment_ptr()));
  const int tid = threadIdx.x, lane = tid & 63;
  const unsigned bid = blockIdx.x;
  const unsigned row0 = (bid & 7) * 1024 + (bid >> 4) * 64;
  unsigned xvt = (unsigned)(tid >> 3) * ROWB + (unsigned)(tid & 7) * 16, xvm = (unsigned)(tid >> 4) * 2 * ROWB + (unsigned)(tid & 15) * 16;
  unsigned xo = row0 * ROWB;
  const unsigned t0 = __builtin_amdgcn_readfirstlane(tid);
  asm volatile("" : "+v"(xvt), "+v"(xvm));
  asm volatile("" : "+s"(xo));
  unsigned pf0 = 0, pf1 = 0;
  asm volatile("s_waitcnt lgkmcnt(0)" : "+s"(kx), "+s"(ka));
  if (t0 >= 256) {
    *(LDS_AS unsigned*)(lds + FLAG) = 0u;
    unsigned long long pc;
    unsigned len;
    asm volatile("s_getpc_b64 %0\n.Lpf_here:\n\ts_mov_b32 %1, .Lpf_end-.Lpf_here-128" : "=s"(pc), "=s"(len));
    const unsigned o = min((bid >> 3) * 128u + (t0 & 192u) * 64u, len);
    asm volatile("s_waitcnt lgkmcnt(0)\n\ts_load_dword %0, %2, %3\n\ts_load_dword %1, %2, %3 offset:64\n\ts_barrier" : "=&s"(pf0), "=&s"(pf1) : "s"(pc), "s"(o) : "memory");
  }
  u32x4 xr[9], gq;
  {
    const unsigned long long xb = kx + xo;
    ldx<1024>(xr[8], xvt, xb);
    static_for<0, GQ_POS>([&](auto jc) { constexpr int j = decltype(jc)::v; ldx<(j >> 2) * ROWB + (j & 3) * 256>(xr[j], xvm, xb); });
    fence();
    ldg(gq, (unsigned)(tid & 127) * 16u, make_srd(t0 < 128 ? ka[0] : ka[2], t0 < 128 ? ka[1] : ka[3], t0 < 256 ? (unsigned)ROWB : 0u));
    static_for<GQ_POS, 8>([&](auto jc) { constexpr int j = decltype(jc)::v; ldx<(j >> 2) * ROWB + (j & 3) * 256>(xr[j], xvm, xb); });
    fence();
  }
  const int wv = t0 >> 6, wi = wv & 3;
  const unsigned jb = bid >> 3, h = jb & 1;
  const int r7 = lane >> 3, c7 = lane & 7, c15 = lane & 15, rq = lane >> 4;
  const unsigned ldsbase = (unsigned)(uintptr_t)lds;
  if (wv < 4) barrier();
  const u32x4 srdw = make_srd(ka[4], ka[5], (unsigned)D * ROWB), srdo = make_srd(ka[6], ka[7], (unsigned)ROWS * ROWB);
  touch(ldsbase + SINK, (wv >= 4 && wv < 7 && lane < 18) ? (jb * 18u + (unsigned)lane) * ROWB + (wv == 4 ? 512u : wv == 5 ? 0u : 640u) : OOB, srdw);

  {
    f32x4 gv[5][2], bv[5][2];
    f32x2 y[9][4];
    LDS_AS char* aw[3] = {opaque(lds + XRES + wv * 1024 + (c15 >> 3) * 8192 + (2 * rq) * 128 + ((c7 ^ (2 * rq)) * 16)),
                          opaque(lds + XRES + wv * 1024 + (c15 >> 3) * 8192 + (2 * rq + 1) * 128 + ((c7 ^ (2 * rq + 1)) * 16)),
                          opaque(lds + XRES + 8 * 8192 + wv * 1024 + r7 * 128 + ((c7 ^ r7) * 16))};
    LDS_AS char* gr = opaque(lds + GBB + c15 * 16);
    LDS_AS char* grt = opaque(lds + GBB + 2048 + c7 * 16);
    float sm[3] = {0.f, 0.f, 0.f}, sq[3] = {0.f, 0.f, 0.f};
#pragma unroll
    for (int i = 0; i < 3; ++i) { pinf(sm[i]); pinf(sq[i]); }
    fence();
    static_for<0, 9>([&](auto jc) {
      constexpr int jj = decltype(jc)::v, j = jj == 0 ? 8 : jj - 1;
      piece_stats<9 - jj + (jj <= GQ_POS ? 1 : 0)>(xr[j], sm[j >> 2], sq[j >> 2], y[j]);
      if constexpr (jj == GQ_POS) {
        wait_vm_for<9 - jj>(gq);
        if (wv < 4) {
          const unsigned g0 = gq[0], g1 = gq[1], g2 = gq[2], g3 = gq[3];
          const f32x2 a0 = unpack2(g0), a1 = unpack2(g1), a2 = unpack2(g2), a3 = unpack2(g3);
          *(LDS_AS f32x4*)(lds + GBB + wi * 2048 + rq * 512 + c15 * 16) = f32x4{a0[0], a0[1], a1[0], a1[1]};
          *(LDS_AS f32x4*)(lds + GBB + wi * 2048 + rq * 512 + c15 * 16 + 256) = f32x4{a2[0], a2[1], a3[0], a3[1]};
          count_in(ldsbase + FLAG);
        }
        fence();
      }
      if constexpr (jj == GB_PIECE) {
        while (__builtin_amdgcn_readfirstlane(*(volatile LDS_AS unsigned*)(lds + FLAG)) < 4u) __builtin_amdgcn_s_sleep(1);
        retire2(pf0, pf1);
      }
      if constexpr (jj >= GB_PIECE) {
#pragma unroll
        for (int kk = jj - GB_PIECE; kk < (jj == 8 ? 5 : jj - GB_PIECE + 1); ++kk) {
          const int k = kk == 0 ? 4 : kk - 1;
          LDS_AS char* p = k < 4 ? gr + k * 512 : grt;
#pragma unroll
          for (int e = 0; e < 2; ++e) { gv[k][e] = *(LDS_AS f32x4*)(p + e * 256); bv[k][e] = *(LDS_AS f32x4*)(p + 4096 + e * 256); }
        }
        fence();
      }
    });
    fence();
    const float mean = oct_sum(fold8(sm[0], sm[1]) + sm[2]) * (1.0f / D);
    const float var = fmaxf(oct_sum(fold8(sq[0], sq[1]) + sq[2]) * (1.0f / D) - mean * mean, 0.f);
    float rs[3], nm[3];
    rs[2] = __builtin_amdgcn_rsqf(var + 1e-5f);
    nm[2] = -mean * rs[2];
    rs[0] = sel8<0xc>(rs[2]); rs[1] = sel8<0x3>(rs[2]);
    nm[0] = sel8<0xc>(nm[2]); nm[1] = sel8<0x3>(nm[2]);
#pragma unroll
    for (int jj = 0; jj < 9; ++jj) {
      const int j = jj == 0 ? 8 : jj - 1, G = j >> 2, k = j == 8 ? 4 : (j & 3);
#pragma unroll
      for (int e = 0; e < 4; ++e) y[j][e] = __builtin_elementwise_fma(y[j][e], f32x2{rs[G], rs[G]}, f32x2{nm[G], nm[G]});
      fence();
#pragma unroll
      for (int e = 0; e < 4; ++e) {
        const f32x4 gg = gv[k][e >> 1], bb = bv[k][e >> 1];
        y[j][e] = __builtin_elementwise_fma(y[j][e], (e & 1) ? f32x2{gg[2], gg[3]} : f32x2{gg[0], gg[1]}, (e & 1) ? f32x2{bb[2], bb[3]} : f32x2{bb[0], bb[1]});
      }
      fence();
      *(LDS_AS u32x4*)(aw[G] + (j == 8 ? 0 : (j & 3) * 16384)) = u32x4{pack2(y[j][0]), pack2(y[j][1]), pack2(y[j][2]), pack2(y[j][3])};
      fence();
    }
  }

  asm volatile("s_cmp_lt_u32 %0, 4\n\ts_cbranch_scc1 .Lskip_b\n\ts_waitcnt lgkmcnt(0)\n\ts_barrier\n.Lskip_b:" ::"s"(wv) : "memory", "scc");

  const unsigned wvoff = (unsigned)r7 * ROWB + (unsigned)(((((c7 >> 1) ^ (((r7 >> 1) & 1) | ((tid >> 5) & 2))) << 1) | (c7 & 1)) * 16);
  const unsigned wbase = h * 640u + (unsigned)wi * 8u * ROWB;

  if (wv >= 4) {
    __builtin_amdgcn_s_setprio(2);
    auto req = [&](auto cc) {
      constexpr int c = decltype(cc)::v;
      unsigned wb = wbase, lb = ldsbase + (unsigned)wi * 1024u;
      asm volatile("" : "+s"(wb), "+s"(lb));
      dma3(lb + ((c + 3) % 12) * HSLOT, wvoff, srdw, wb + (c / 6) * 128u + (c % 6) * 96u * ROWB);
    };
    barrier(); static_for<3, 5>(req);
    barrier(); static_for<5, 6>(req);
    barrier(); static_for<6, 8>(req);
    static_for<0, 11>([&](auto qc) {
      constexpr int q = decltype(qc)::v;
      wait_vm<(q < 9 ? 12 : q == 9 ? 6 : 0)>();
      barrier();
      if constexpr (q < 8) static_for<2 * q + 8, 2 * q + 10>(req);
    });
  } else {
    {
      const unsigned hv = (unsigned)(lane >> 2) * ROWB + (unsigned)(((lane & 3) ^ ((lane >> 4) & 2)) * 16);
      const unsigned hs = 512u + h * 64u + (unsigned)wi * 16u * ROWB;
#pragma unroll
      for (int i = 0; i < 9; ++i) dma16(ldsbase + (i * 4 + wi) * 1024, hv, srdw, hs + (unsigned)(i * 64) * ROWB);
#pragma unroll
      for (int c = 0; c < 3; ++c) dma3(ldsbase + (c + 3) * HSLOT + (unsigned)wi * 1024u, wvoff, srdw, wbase + c * 96u * ROWB);
      const unsigned li = (unsigned)tid, kk = (li * 43u) >> 8, ln = li - kk * 6u;
      touch(ldsbase + SINK, li < 108u ? (jb * 18u + kk) * ROWB + (ln + 1u + (ln >= 3u) * 2u) * 128u : OOB, srdw);
    }
    const int m = wi >> 1, n = wi & 1;
    const int fr = lane & 15, fg = lane >> 4;
    bf16x8 xa[2][18];
    LDS_AS char* ard[2][2][2];
#pragma unroll
    for (int e = 0; e < 2; ++e) {
      const unsigned o = (m * 2 + (fr >> 3)) * 1024 + (fr & 7) * 128 + (((4 * e + fg) ^ (fr & 7)) * 16);
#pragma unroll
      for (int i = 0; i < 2; ++i) {
        ard[i][e][0] = opaque(lds + XRES + (i ^ n) * 4096 + o);
        ard[i][e][1] = opaque(lds + XRES + 7 * 8192 + (i ^ n) * 4096 + o);
      }
    }
    LDS_AS char* wrd[2][3];
#pragma unroll
    for (int t = 0; t < 2; ++t)
#pragma unroll
      for (int z = 0; z < 3; ++z)
        wrd[t][z] = opaque(lds + z * 4 * HSLOT + (8 * fg + (fr >> 2)) * 128 + (((2 * n + t) ^ (((fr >> 3) & 1) | ((fg & 1) << 1))) * 32) + (fr & 3) * 8);
    LDS_AS char* wh[2];
#pragma unroll
    for (int t = 0; t < 2; ++t) wh[t] = opaque(lds + (8 * fg + (fr >> 2)) * 64 + ((t ^ (fg & 1)) * 32) + (fr & 3) * 8);
    const unsigned ovoff = (unsigned)fr * ROWB + (unsigned)(2 * (fg & 1) + (fg >> 1)) * 16u;
    const unsigned orow = (row0 + m * 16) * ROWB;
    const unsigned osoff[2] = {orow + (unsigned)n * 32u * ROWB, orow + (unsigned)(n ^ 1) * 32u * ROWB};
    wait_lgkm0();
    wait_vm<16>();
    barrier();

    bf16x8 wf[2][6];
    f32x4 acc[2][2], fin[2][2];
    u32x4 po[4][2], ph;
    auto tr_frag = [&](LDS_AS char* p, int step) {
      const s16x4 lo = __builtin_amdgcn_ds_read_tr16_b64_v4i16((LDS_AS s16x4*)p);
      const s16x4 hi = __builtin_amdgcn_ds_read_tr16_b64_v4i16((LDS_AS s16x4*)(p + step));
      return __builtin_bit_cast(bf16x8, __builtin_shufflevector(lo, hi, 0, 1, 2, 3, 4, 5, 6, 7));
    };
    auto rd = [&](auto uc) {
      constexpr int u = decltype(uc)::v, hsl = (u - 3) % 12;
#pragma unroll
      for (int s = 0; s < 3; ++s) {
        if constexpr (u >= 6) {
#pragma unroll
          for (int t = 0; t < 2; ++t) wf[u & 1][s * 2 + t] = tr_frag(wrd[t][hsl >> 2] + (hsl & 3) * HSLOT + s * 4096, 512);
        } else {
          const int ks = u * 3 + s;
#pragma unroll
          for (int t = 0; t < 2; ++t) wf[u & 1][s * 2 + t] = tr_frag(wh[t] + ks * 2048, 256);
#pragma unroll
          for (int i = 0; i < 2; ++i) xa[i][ks] = *(LDS_AS bf16x8*)(ard[i][ks & 1][ks >= 14] + ((ks >> 1) % 7) * 8192);
        }
      }
    };
    auto mm = [&](auto uc) {
      constexpr int u = decltype(uc)::v;
#pragma unroll
      for (int f = 0; f < 6; ++f) {
        const int ks = (u % 6) * 3 + f / 2, t = f & 1;
#pragma unroll
        for (int i = 0; i < (u >= 6 ? 2 : 1); ++i)
          acc[i][t] = __builtin_amdgcn_mfma_f32_16x16x32_bf16(wf[u & 1][f], xa[i][ks], ks == 0 ? f32x4{0.f, 0.f, 0.f, 0.f} : acc[i][t], 0, 0, 0);
      }
    };
    auto pack = [&](const f32x4& a, const f32x4& c) {
      const auto lo = __builtin_amdgcn_permlane16_swap(pack2(f32x2{a[0], a[1]}), pack2(f32x2{c[0], c[1]}), false, false);
      const auto hi = __builtin_amdgcn_permlane16_swap(pack2(f32x2{a[2], a[3]}), pack2(f32x2{c[2], c[3]}), false, false);
      return u32x4{(unsigned)lo[0], (unsigned)hi[0], (unsigned)lo[1], (unsigned)hi[1]};
    };
    auto store = [&](int k) {
      if (k == 0) st16_through(ph, ovoff, srdo, osoff[0] + 512u + h * 64u);
      else st16_through(po[(k - 1) / 2][(k - 1) & 1], ovoff, srdo, osoff[(k - 1) & 1] + h * 640u + (unsigned)n * 64u + ((k - 1) / 2) * 128u);
    };
    rd(IC<0>{});
    fence();
    static_for<0, 30>([&](auto uc) {
      constexpr int u = decltype(uc)::v;
      constexpr bool pk = u == 6 || (u >= 12 && u % 6 < 2);
      if constexpr ((u & 1) && u < 29) {
        if constexpr (u < 8) wait_vm<(u == 1 ? 13 : u == 3 ? 10 : u == 5 ? 4 : 1)>();
        if constexpr (u < 6) wait_lgkm0();
        barrier();
        fence();
      }
      if constexpr (u < 29) rd(IC<u + 1>{});
      if constexpr (u == 6) ph = pack(fin[0][0], fin[0][1]);
      else if constexpr (pk) po[u / 6 - 2][u % 6] = pack(fin[u % 6][0], fin[u % 6][1]);
      mm(uc);
      if constexpr (u < 29) {
        static_for<0, (u < 6 ? 6 : 12)>([&](auto ic) {
          constexpr int i = decltype(ic)::v;
          __builtin_amdgcn_sched_group_barrier(0x008, 1, 0);
          __builtin_amdgcn_sched_group_barrier(0x100, u < 5 ? 3 : u == 5 ? 2 : 1, 0);
          if constexpr (pk && i >= 2 && i < 8) __builtin_amdgcn_sched_group_barrier(0x002, 1, 0);
        });
      }
      fence();
      if constexpr (u >= ST_FROM && u < ST_FROM + 7) { store(u - ST_FROM); fence(); }
      if constexpr (u % 6 == 5) {
#pragma unroll
        for (int i = 0; i < (u >= 6 ? 2 : 1); ++i) { fin[i][0] = acc[i][0]; fin[i][1] = acc[i][1]; }
      }
    });
    po[3][0] = pack(fin[0][0], fin[0][1]);
    po[3][1] = pack(fin[1][0], fin[1][1]);
    store(7);
    store(8);
  }
  asm volatile(".Lpf_end:");
}
}

extern "C" void kernel_launch(void* const* d_in, const int* in_sizes, int n_in, void* d_out, int out_size, void* d_ws,
                              size_t ws_size, hipStream_t stream) {
  (void)in_sizes; (void)n_in; (void)out_size; (void)d_ws; (void)ws_size;
  (void)hipFuncSetAttribute((const void*)ln_linear_kernel, hipFuncAttributeMaxDynamicSharedMemorySize, LDS_BYTES);
  ln_linear_kernel<<<256, 512, LDS_BYTES, stream>>>((const uint16_t*)d_in[0], (const uint16_t*)d_in[1], (const uint16_t*)d_in[2],
                                                    (const uint16_t*)d_in[3], (uint16_t*)d_out);
}
```
